```python
import torch

def _softmax(x, axis=-1):
    return torch.softmax(x, dim=axis)
def _gelu(x):
    return torch.nn.functional.gelu(x, approximate="tanh")
def _silu(x):
    return torch.nn.functional.silu(x)
def _sigmoid(x):
    return torch.sigmoid(x)
def _layernorm(x, axis=-1, eps=1e-5):
    m = x.mean(dim=axis, keepdim=True)
    v = x.var(dim=axis, keepdim=True, unbiased=False)
    return (x - m) / torch.sqrt(v + eps)
def _scatter_add(tgt, idx, src, axis=0):
    return tgt.clone().index_add_(axis, idx, src)
def _segment_sum(src, seg, n):
    out = torch.zeros((n,) + src.shape[1:], dtype=src.dtype)
    return out.index_add_(0, seg, src)
def _pad(x, pw):
    flat = tuple(v for pair in reversed(pw) for v in pair)
    return torch.nn.functional.pad(x, flat)

kernel_name = 'parallel_sum__sliding_local_dilated__dense_activation_squared_relu__prompt'

def setup_inputs():

    _w = (lambda a: (lambda t: t.to(torch.bfloat16) if t.is_floating_point() else t)(torch.as_tensor(a)))
    def _A_setup():
        B, S, D = 1, 2048, 576
        return {
            "x": _w((lambda *s: torch.randn(*s, dtype=torch.bfloat16) * 0.05)(B, S, D)),
            "wqkv": _w((lambda *s: torch.randn(*s, dtype=torch.bfloat16) * 0.1)(D, 3 * D)),
            "wo": _w((lambda *s: torch.randn(*s, dtype=torch.bfloat16) * 0.05)(D, D)),
        }
    _A_in = _A_setup()

    def _B_setup():
        B, S, D, F = 1, 2048, 576, 4608
        return {
            "x": _w((lambda *s: torch.randn(*s, dtype=torch.bfloat16) * 0.05)(B, S, D)),
            "w1": _w((lambda *s: torch.randn(*s, dtype=torch.bfloat16) * 0.05)(D, F)),
            "b1": _w((lambda *s: torch.randn(*s, dtype=torch.bfloat16) * 0.2)(F)),
            "w2": _w((lambda *s: torch.randn(*s, dtype=torch.bfloat16) * 0.05)(F, D)),
            "b2": _w((lambda *s: torch.randn(*s, dtype=torch.bfloat16) * 1.6)(D)),
        }
    _B_in = _B_setup()

    return {
        "A_x": _A_in["x"],
        "A_wqkv": _A_in["wqkv"],
        "A_wo": _A_in["wo"],
        "B_x": _B_in["x"],
        "B_w1": _B_in["w1"],
        "B_b1": _B_in["b1"],
        "B_w2": _B_in["w2"],
        "B_b2": _B_in["b2"],
    }

def reference(A_x, A_wqkv, A_wo, B_x, B_w1, B_b1, B_w2, B_b2):
    def _A_ref(x, wqkv, wo):
        B, S, D = x.shape
        H = 32; Dh = D // H
        W, dil = 8, 2
        qkv = (lambda x, *d: x.permute(*d))((x @ wqkv).reshape(B, S, 3, H, Dh), 2, 0, 3, 1, 4)
        q, k, v = qkv[0], qkv[1], qkv[2]
        scale = Dh ** -0.5
        pos = torch.arange(S)
        off = torch.arange(W) * dil
        idx = pos[:, None] - off[None, :]
        mask = idx >= 0
        idx_c = torch.where(mask, idx, 0).reshape(-1)
        kg = k[:, :, idx_c, :].reshape(B, H, S, W, Dh)
        vg = v[:, :, idx_c, :].reshape(B, H, S, W, Dh)
        logits = torch.einsum("bhsd,bhswd->bhsw", q, kg) * scale
        logits = torch.where(mask[None, None, :, :], logits, -1e9)
        attn = _softmax(logits, axis=-1)
        o = torch.einsum("bhsw,bhswd->bhsd", attn, vg)
        return (lambda x, *d: x.permute(*d))(o, 0, 2, 1, 3).reshape(B, S, D) @ wo

    def _B_ref(x, w1, b1, w2, b2):
        h = torch.einsum("bsd,df->bsf", x, w1) + b1
        zero = torch.zeros_like(h)
        r = torch.where(h > zero, h, zero)
        h = r * r
        out = torch.einsum("bsf,fd->bsd", h, w2) + b2
        return out

    x = A_x
    xn = _layernorm(x, axis=-1)
    return x + _A_ref(xn, A_wqkv, A_wo) + _B_ref(xn, B_w1, B_b1, B_w2, B_b2)
```

```cpp
#include <hip/hip_runtime.h>
#include <hip/hip_bf16.h>
typedef __hip_bfloat16 bf16;
constexpr int S=2048, D=576, F=4608, H=32, DH=18;

__device__ inline float b2f(bf16 x){ return __bfloat162float(x); }

__global__ void ln_k(const bf16* x, float* xn){
  int r = blockIdx.x; int t = threadIdx.x;
  float s=0; for(int i=t;i<D;i+=64) s+=b2f(x[r*D+i]);
  for(int o=32;o;o>>=1) s+=__shfl_xor(s,o);
  float m=s/D; float v=0; for(int i=t;i<D;i+=64){float d=b2f(x[r*D+i])-m; v+=d*d;}
  for(int o=32;o;o>>=1) v+=__shfl_xor(v,o);
  float rs=rsqrtf(v/D+1e-5f);
  for(int i=t;i<D;i+=64) xn[r*D+i]=b2f(__float2bfloat16((b2f(x[r*D+i])-m)*rs));
}
template<int MODE>
__global__ void gemm_k(const float* A, const bf16* B, float* C, int M,int N,int K, const bf16* bias){
  __shared__ float As[16][17]; __shared__ float Bs[16][17];
  int tx=threadIdx.x, ty=threadIdx.y; int r=blockIdx.y*16+ty, c=blockIdx.x*16+tx;
  float acc=0;
  for(int k0=0;k0<K;k0+=16){
    As[ty][tx]=A[(long)r*K+k0+tx]; Bs[ty][tx]=b2f(B[(long)(k0+ty)*N+c]);
    __syncthreads();
    for(int k=0;k<16;k++) acc+=As[ty][k]*Bs[k][tx];
    __syncthreads();
  }
  if(MODE==1){ acc+=b2f(bias[c]); acc=fmaxf(acc,0.f); acc=acc*acc; }
  if(MODE==2){ acc+=b2f(bias[c]); }
  if(MODE!=2 && MODE!=3) acc=b2f(__float2bfloat16(acc));
  C[(long)r*N+c]=acc;
}
__global__ void attn_k(const float* qkv, float* o){
  int idx=blockIdx.x*blockDim.x+threadIdx.x; if(idx>=S*H) return;
  int s=idx/H, h=idx%H;
  const float* q=qkv+(long)s*3*D+h*DH;
  float lg[8]; float mx=-1e30f;
  for(int w=0;w<8;w++){ int p=s-2*w; float a=-1e9f; if(p>=0){ const float* k=qkv+(long)p*3*D+D+h*DH; a=0; for(int d=0;d<DH;d++) a+=q[d]*k[d]; a*=0.23570226039551584f;} lg[w]=a; mx=fmaxf(mx,a);}
  float sum=0; for(int w=0;w<8;w++){ lg[w]=__expf(lg[w]-mx); sum+=lg[w]; }
  for(int d=0;d<DH;d++){ float a=0; for(int w=0;w<8;w++){ int p=s-2*w; if(p<0)p=0; a+=lg[w]*qkv[(long)p*3*D+2*D+h*DH+d]; } o[(long)s*D+h*DH+d]=b2f(__float2bfloat16(a/sum)); }
}
__global__ void fin_k(const bf16* x, const float* a, const float* b, bf16* out){
  int i=blockIdx.x*blockDim.x+threadIdx.x; if(i<S*D) out[i]=__float2bfloat16(b2f(x[i])+a[i]+b[i]);
}
extern "C" void kernel_launch(void* const* d_in, const int* in_sizes, int n_in, void* d_out, int out_size, void* d_ws, size_t ws_size, hipStream_t stream){
  const bf16* x=(const bf16*)d_in[0]; const bf16* wqkv=(const bf16*)d_in[1]; const bf16* wo=(const bf16*)d_in[2];
  const bf16* w1=(const bf16*)d_in[4]; const bf16* b1=(const bf16*)d_in[5]; const bf16* w2=(const bf16*)d_in[6]; const bf16* b2=(const bf16*)d_in[7];
  float* ws=(float*)d_ws;
  float* xn=ws; float* qkv=xn+S*D; float* o=qkv+S*3*D; float* a=o+S*D; float* h=a+S*D; float* bo=h+(long)S*F;
  ln_k<<<S,64,0,stream>>>(x,xn);
  gemm_k<0><<<dim3(3*D/16,S/16),dim3(16,16),0,stream>>>(xn,wqkv,qkv,S,3*D,D,nullptr);
  attn_k<<<S*H/256,256,0,stream>>>(qkv,o);
  gemm_k<3><<<dim3(D/16,S/16),dim3(16,16),0,stream>>>(o,wo,a,S,D,D,nullptr);
  gemm_k<1><<<dim3(F/16,S/16),dim3(16,16),0,stream>>>(xn,w1,h,S,F,D,b1);
  gemm_k<2><<<dim3(D/16,S/16),dim3(16,16),0,stream>>>(h,w2,bo,S,D,F,b2);
  fin_k<<<S*D/256,256,0,stream>>>(x,a,bo,(bf16*)d_out);
}
```

```cpp
#include <hip/hip_runtime.h>
#include <stdint.h>
#include <utility>

#define ROLL1 1

typedef __bf16 bf16x8 __attribute__((ext_vector_type(8)));
typedef __bf16 bf16x4 __attribute__((ext_vector_type(4)));
typedef __bf16 bf16x2 __attribute__((ext_vector_type(2)));
typedef float f32x16 __attribute__((ext_vector_type(16)));
typedef float f32x4 __attribute__((ext_vector_type(4)));
typedef float f32x2 __attribute__((ext_vector_type(2)));
typedef unsigned u32x4 __attribute__((ext_vector_type(4)));
typedef unsigned u32x2 __attribute__((ext_vector_type(2)));
#define LAS __attribute__((address_space(3)))
#define DI __device__ __forceinline__
#define SB() __builtin_amdgcn_sched_barrier(0)

template <class F, int... I> DI void sfor_(F&& f, std::integer_sequence<int, I...>) { (f(std::integral_constant<int, I>{}), ...); }
template <int N, class F> DI void sfor(F&& f) { sfor_(f, std::make_integer_sequence<int, N>{}); }

constexpr int S_ = 2048, D_ = 576, F_ = 4608;
constexpr int SLOT = 24576;
constexpr int XA_OFF = 3 * SLOT;
constexpr int TAB_OFF = 155648;
constexpr int LDS_BYTES = 163840;
constexpr int IMG_RS = 872;
constexpr int OSLOT = 20480, OBASE = 16384;
constexpr unsigned PART_BYTES = 16u * S_ * D_ * 2u;
constexpr unsigned OOB = 0x40000000u;
constexpr int A_H = 56;
constexpr int A_HH = 168;
constexpr int A_HX = 184;

DI u32x4 make_srd(const void* p, unsigned bytes) {
  uint64_t a = (uint64_t)p;
  u32x4 r; r[0] = (unsigned)a; r[1] = (unsigned)(a >> 32) & 0xffffu; r[2] = bytes; r[3] = 0x00020000u;
  return r;
}
DI void glds16(unsigned m0v, unsigned voff, u32x4 srd, unsigned soff) {
  asm volatile("s_mov_b32 m0, %0\n\ts_nop 0\n\tbuffer_load_dwordx4 %1, %2, %3 offen lds"
               :: "s"(m0v), "v"(voff), "s"(srd), "s"(soff) : "memory");
}
DI u32x4 bload16(unsigned voff, u32x4 srd, unsigned soff) {
  u32x4 r;
  asm volatile("buffer_load_dwordx4 %0, %1, %2, %3 offen" : "=v"(r) : "v"(voff), "s"(srd), "s"(soff) : "memory");
  return r;
}
DI unsigned bload4(unsigned voff, u32x4 srd, unsigned soff) {
  unsigned r;
  asm volatile("buffer_load_dword %0, %1, %2, %3 offen" : "=v"(r) : "v"(voff), "s"(srd), "s"(soff) : "memory");
  return r;
}
#define WAITV(n) asm volatile("s_waitcnt vmcnt(" #n ")" ::: "memory")
#define BAR() asm volatile("s_barrier" ::: "memory")
#define WAITL0() asm volatile("s_waitcnt lgkmcnt(0)" ::: "memory")
#define PIN(x) asm volatile("" : "+v"(x))

DI unsigned cvtpk(float lo, float hi) { f32x2 v = {lo, hi}; bf16x2 b = __builtin_convertvector(v, bf16x2); return __builtin_bit_cast(unsigned, b); }
DI float bflo(unsigned u) { return __uint_as_float(u << 16); }
DI float bfhi(unsigned u) { return __uint_as_float(u & 0xffff0000u); }
DI float dot2(unsigned a, unsigned b, float c) { return __builtin_amdgcn_fdot2_f32_bf16(__builtin_bit_cast(bf16x2, a), __builtin_bit_cast(bf16x2, b), c, false); }
DI float xor32(float v) { auto r = __builtin_amdgcn_permlane32_swap(__float_as_uint(v), __float_as_uint(v), false, false); return __uint_as_float(r[0]) + __uint_as_float(r[1]); }
DI float xor16(float v) { auto r = __builtin_amdgcn_permlane16_swap(__float_as_uint(v), __float_as_uint(v), false, false); return __uint_as_float(r[0]) + __uint_as_float(r[1]); }
DI float fmul_s(float a, float b) { float r; asm("v_mul_f32_e32 %0, %1, %2" : "=v"(r) : "v"(a), "v"(b)); return r; }
DI float ffma_s(float a, float b, float c) { float r; asm("v_fma_f32 %0, %1, %2, %3" : "=v"(r) : "v"(a), "v"(b), "v"(c)); return r; }
DI float relu_s(float a) { float r; asm("v_max_f32_e32 %0, 0, %1" : "=v"(r) : "v"(a)); return r; }

template <int UOFF> DI bf16x8 rdfrag(LAS char* p) {
  bf16x4 a = __builtin_amdgcn_ds_read_tr16_b64_v4bf16((LAS bf16x4*)p);
  bf16x4 b = __builtin_amdgcn_ds_read_tr16_b64_v4bf16((LAS bf16x4*)(p + UOFF));
  return __builtin_shufflevector(a, b, 0, 1, 2, 3, 4, 5, 6, 7);
}
template <bool FIRST> DI void mfma_vv(f32x16& acc, bf16x8 a, u32x4 b) {
  if constexpr (FIRST) asm volatile("v_mfma_f32_32x32x16_bf16 %0, %1, %2, 0" : "=&v"(acc) : "v"(a), "v"(b));
  else                 asm volatile("v_mfma_f32_32x32x16_bf16 %0, %1, %2, %0" : "+v"(acc) : "v"(a), "v"(b));
}
template <bool FIRST, int AR> DI void mfma_va(f32x16& acc, bf16x8 a) {
  if constexpr (FIRST) asm volatile("v_mfma_f32_32x32x16_bf16 %0, %1, a[%c2:%c3], 0" : "=&v"(acc) : "v"(a), "i"(AR), "i"(AR + 3));
  else                 asm volatile("v_mfma_f32_32x32x16_bf16 %0, %1, a[%c2:%c3], %0" : "+v"(acc) : "v"(a), "i"(AR), "i"(AR + 3));
}
template <bool FIRST, int AR> DI void mfma16_va(f32x4& acc, bf16x8 a) {
  if constexpr (FIRST) asm volatile("v_mfma_f32_16x16x32_bf16 %0, %1, a[%c2:%c3], 0" : "=&v"(acc) : "v"(a), "i"(AR), "i"(AR + 3));
  else                 asm volatile("v_mfma_f32_16x16x32_bf16 %0, %1, a[%c2:%c3], %0" : "+v"(acc) : "v"(a), "i"(AR), "i"(AR + 3));
}
template <int AR> DI void awrite(unsigned v) { asm volatile("v_accvgpr_write_b32 a[%c1], %0" :: "v"(v), "i"(AR)); }
#define AW8(b) "v_accvgpr_write_b32 a[%c9+" #b "], %0\n\tv_accvgpr_write_b32 a[%c9+" #b "+1], %1\n\tv_accvgpr_write_b32 a[%c9+" #b "+2], %2\n\tv_accvgpr_write_b32 a[%c9+" #b "+3], %3\n\t" \
               "v_accvgpr_write_b32 a[%c9+" #b "+4], %4\n\tv_accvgpr_write_b32 a[%c9+" #b "+5], %5\n\tv_accvgpr_write_b32 a[%c9+" #b "+6], %6\n\tv_accvgpr_write_b32 a[%c9+" #b "+7], %7\n\ts_branch 1f\n\t"
template <int BASE> DI void awrite8_dyn(const unsigned (&e)[8], unsigned joff) {
  asm volatile("s_getpc_b64 vcc\n\ts_add_u32 vcc_lo, vcc_lo, %8\n\ts_addc_u32 vcc_hi, vcc_hi, 0\n\ts_setpc_b64 vcc\n\t"
               AW8(0) AW8(16) AW8(32) AW8(48) AW8(64) AW8(80) AW8(96) "1:"
               :: "v"(e[0]), "v"(e[1]), "v"(e[2]), "v"(e[3]), "v"(e[4]), "v"(e[5]), "v"(e[6]), "v"(e[7]), "s"(joff + 12), "i"(BASE) : "vcc", "scc");
}
#define AW2(b) "v_accvgpr_write_b32 a[%c3+" #b "], %0\n\tv_accvgpr_write_b32 a[%c3+" #b "+1], %1\n\ts_branch 1f\n\t"
template <int BASE> DI void awrite2_dyn(unsigned e0, unsigned e1, unsigned joff) {
  asm volatile("s_getpc_b64 vcc\n\ts_add_u32 vcc_lo, vcc_lo, %2\n\ts_addc_u32 vcc_hi, vcc_hi, 0\n\ts_setpc_b64 vcc\n\t"
               AW2(0) AW2(2) AW2(4) AW2(6) AW2(8) AW2(10) AW2(12) "1:"
               :: "v"(e0), "v"(e1), "s"(joff + 12), "i"(BASE) : "vcc", "scc");
}
template <int AR, int OFF> DI void ds_write_a64(unsigned addr) { asm volatile("ds_write_b64 %0, a[%c1:%c2] offset:%c3" :: "v"(addr), "i"(AR), "i"(AR + 1), "i"(OFF) : "memory"); }

template <int N, bool QUAD> DI float center_rows(u32x4 (&xf)[N]) {
  float s0 = 0.f, s1 = 0.f, s2 = 0.f, s3 = 0.f;
#pragma unroll
  for (int s = 0; s < N; ++s) { s0 = dot2(xf[s][0], 0x3f803f80u, s0); s1 = dot2(xf[s][1], 0x3f803f80u, s1); s2 = dot2(xf[s][2], 0x3f803f80u, s2); s3 = dot2(xf[s][3], 0x3f803f80u, s3); }
  float sum = (s0 + s1) + (s2 + s3);
  if (QUAD) sum = xor16(sum);
  sum = xor32(sum);
  const float mu = sum * (1.0f / D_);
  float q0 = 0.f, q1 = 0.f, q2 = 0.f, q3 = 0.f;
  SB();
#pragma unroll
  for (int s = 0; s < N; ++s) {
    PIN(xf[s]);
#pragma unroll
    for (int j = 0; j < 4; ++j) xf[s][j] = cvtpk(bflo(xf[s][j]) - mu, bfhi(xf[s][j]) - mu);
    q0 = dot2(xf[s][0], xf[s][0], q0); q1 = dot2(xf[s][1], xf[s][1], q1); q2 = dot2(xf[s][2], xf[s][2], q2); q3 = dot2(xf[s][3], xf[s][3], q3);
    if ((s & 1) == 1) SB();
  }
  float ss = (q0 + q1) + (q2 + q3);
  if (QUAD) ss = xor16(ss);
  ss = xor32(ss);
  return __builtin_amdgcn_rsqf(ss * (1.0f / D_) + 1e-5f);
}

#define STAMPT(i)
#define STAMPOUT()

template <bool attn>
DI void body(const void* __restrict__ x, const void* __restrict__ wqkv, const void* __restrict__ wo,
             const void* __restrict__ w1, const void* __restrict__ b1, const void* __restrict__ w2, void* __restrict__ part, unsigned long long* stamps) {
  extern __shared__ __attribute__((aligned(16))) char smem[];
  LAS char* lds = (LAS char*)smem;
  const int tid = threadIdx.x, lane = tid & 63;
  const int w = __builtin_amdgcn_readfirstlane(tid >> 6);
  const int bid = blockIdx.x, xcd = bid & 7, idx = bid >> 3;
  const int sp = xcd + 8 * (idx >> 4), rt = idx & 15;
  const int tokbase = 256 * (rt >> 1) + (rt & 1);
  const int m = lane & 31, h = lane >> 5, r8 = lane >> 3, pos = lane & 7;
  const int G = lane >> 4, q4 = (lane >> 2) & 3, p4 = lane & 3;
  STAMPT(0);

  const u32x4 srdx = make_srd(x, S_ * D_ * 2);
  const u32x4 srdA = attn ? make_srd(wqkv, D_ * 3 * D_ * 2) : make_srd(w1, D_ * F_ * 2);
  const u32x4 srdB = attn ? make_srd(wo, D_ * D_ * 2) : make_srd(w2, F_ * D_ * 2);
  const __amdgpu_buffer_rsrc_t rpart = __builtin_amdgcn_make_buffer_rsrc(part, 0, PART_BYTES, 0x00020000);
  const unsigned swz = pos ^ (((r8 >> 1) & 1) << 2);
  const unsigned voffB = r8 * 1152 + swz * 16;
  const int g = sp - 12, f0 = sp * 384;

  const unsigned vx0 = r8 * 2304 + ((pos ^ (lane >> 4)) << 4), vx1 = r8 * 2304 + ((pos ^ (4 + (lane >> 4))) << 4);
  const unsigned sx = tokbase * 1152 + w * 32 * 2304;
  unsigned braw = 0;
  u32x4 hx[18];
  if constexpr (!attn) braw = bload4(tid * 4, make_srd((const char*)b1 + f0 * 2, 768), 0);
  else {
    int ht = tokbase + 2 * ((lane & 7) - 8); ht = ht < 0 ? 0 : ht;
    const unsigned vh = ht * 1152 + G * 16;
#pragma unroll
    for (int s = 0; s < 18; ++s) hx[s] = bload16(vh, srdx, s * 64);
  }
#pragma unroll
  for (int t = 0; t < 5; ++t)
#pragma unroll
    for (int rg = 0; rg < 4; ++rg) glds16(XA_OFF + w * 20480 + t * 4096 + rg * 1024, (rg & 1) ? vx1 : vx0, srdx, sx + rg * 8 * 2304 + t * 128);
  u32x4 xb[16];
#pragma unroll
  for (int t = 0; t < 4; ++t)
#pragma unroll
    for (int rg = 0; rg < 4; ++rg) xb[t * 4 + rg] = bload16((rg & 1) ? vx1 : vx0, srdx, sx + rg * 8 * 2304 + (t + 5) * 128);

  constexpr unsigned strideA = attn ? 3456u : 9216u;
  auto voffA_of = [&](int strip) -> unsigned {
    const unsigned v = 8 * strip + swz;
    return attn ? r8 * 3456 + v * 16 + ((v >= 18) + (v >= 36) + (v >= 54)) * 864 : r8 * 9216 + swz * 16;
  };
  auto soffA_of = [&](int strip, int j) -> unsigned {
    return (192 * j + 48 * w) * strideA + (attn ? 288 * g : (f0 + 64 * strip) * 2);
  };
  {
    const unsigned va = voffA_of(0);
#pragma unroll
    for (int j = 0; j < 3; ++j)
#pragma unroll
      for (int p = 0; p < 6; ++p) glds16(j * SLOT + (6 * w + p) * 1024, va, srdA, soffA_of(0, j) + p * 8 * strideA);
  }
  u32x4 xf[36];
  LAS char* xa = lds + XA_OFF + w * 20480;
  const unsigned xrd = m * 128 + ((h ^ ((m >> 1) & 7)) << 4);
  float rstdh = 0.f;
  if constexpr (attn) {
    asm volatile("s_waitcnt vmcnt(54)" : "+v"(hx[0]), "+v"(hx[1]), "+v"(hx[2]), "+v"(hx[3]), "+v"(hx[4]), "+v"(hx[5]), "+v"(hx[6]), "+v"(hx[7]), "+v"(hx[8]),
                 "+v"(hx[9]), "+v"(hx[10]), "+v"(hx[11]), "+v"(hx[12]), "+v"(hx[13]), "+v"(hx[14]), "+v"(hx[15]), "+v"(hx[16]), "+v"(hx[17]) :: "memory");
    rstdh = center_rows<18, true>(hx);
    sfor<72>([&](auto I) { constexpr int i = I; awrite<A_HX + i>(hx[i >> 2][i & 3]); });
  }
  asm volatile("s_waitcnt vmcnt(34)" : "+v"(braw) :: "memory");
  if constexpr (!attn) { if (tid < 192) { f32x2 bv = {bflo(braw), bfhi(braw)}; *(LAS f32x2*)(lds + TAB_OFF + tid * 8) = bv; } }
#pragma unroll
  for (int s = 0; s < 20; ++s) xf[s] = *(LAS u32x4*)(xa + (s >> 2) * 4096 + (xrd ^ ((s & 3) << 5)));
  WAITL0();
  asm volatile("s_waitcnt vmcnt(18)" : "+v"(xb[0]), "+v"(xb[1]), "+v"(xb[2]), "+v"(xb[3]), "+v"(xb[4]), "+v"(xb[5]), "+v"(xb[6]), "+v"(xb[7]),
               "+v"(xb[8]), "+v"(xb[9]), "+v"(xb[10]), "+v"(xb[11]), "+v"(xb[12]), "+v"(xb[13]), "+v"(xb[14]), "+v"(xb[15]) :: "memory");
#pragma unroll
  for (int i = 0; i < 16; ++i) *(LAS u32x4*)(xa + i * 1024 + lane * 16) = xb[i];
#pragma unroll
  for (int s = 20; s < 36; ++s) xf[s] = *(LAS u32x4*)(xa + ((s >> 2) - 5) * 4096 + (xrd ^ ((s & 3) << 5)));
  WAITL0();
  WAITV(12);
  BAR();
  STAMPT(1);
  {
    const unsigned va = voffA_of(1);
#pragma unroll
    for (int j = 0; j < 2; ++j)
#pragma unroll
      for (int p = 0; p < 6; ++p) glds16((3 + j) * SLOT + (6 * w + p) * 1024, va, srdA, soffA_of(1, j) + p * 8 * strideA);
  }
  const float rstd = center_rows<36, false>(xf);

  const unsigned colo = (G & 1) * 32 + p4 * 8, q1 = (q4 >> 1) & 1;
  const unsigned p1o0 = (8 * h + q4) * 128 + q1 * 64 + colo, p1o1 = (8 * h + q4) * 128 + (q1 ^ 1) * 64 + colo;
  const unsigned pbase = (((sp * 16 + rt) * 4 + w) * 18) * 2048;
  bf16x8 wf[6];
  f32x16 acc0, acc1;
#pragma unroll
  for (int i = 0; i < 16; ++i) { acc0[i] = 0.f; acc1[i] = 0.f; }
  unsigned e[8];
  auto epi2 = [&](auto U, const f32x16& acc, unsigned so) {
    constexpr int u = U;
    if constexpr (u >= 3 && u < 7) {
      constexpr int d = 2 * (u - 3);
      e[d] = cvtpk(acc[2 * d], acc[2 * d + 1]); e[d + 1] = cvtpk(acc[2 * d + 2], acc[2 * d + 3]);
      PIN(e[d]); PIN(e[d + 1]);
    }
    if constexpr (u == 5) __builtin_amdgcn_raw_buffer_store_b128(u32x4{e[0], e[1], e[2], e[3]}, rpart, lane * 16, so, 0);
    if constexpr (u == 7) __builtin_amdgcn_raw_buffer_store_b128(u32x4{e[4], e[5], e[6], e[7]}, rpart, lane * 16 + 1024, so, 0);
  };

  if constexpr (!attn) {
    STAMPT(2);
    const unsigned p2o0 = (4 * h + q4) * 128 + q1 * 64 + colo, p2o1 = (4 * h + q4) * 128 + (q1 ^ 1) * 64 + colo;
#pragma unroll
    for (int t = 0; t < 5; ++t) wf[t] = rdfrag<512>(lds + p1o0 + t * 2048);
    const unsigned voffA = voffA_of(0);
    LAS char* tab = lds + TAB_OFF + h * 16;
    f32x4 bs[4];
    auto epi1 = [&](auto U, auto ARc, const f32x16& acc, int T) {
      constexpr int u = U, AR = ARc;
      if constexpr (u == 0) { bs[0] = *(LAS f32x4*)(tab + T * 128); bs[1] = *(LAS f32x4*)(tab + T * 128 + 32); }
      if constexpr (u == 1) { bs[2] = *(LAS f32x4*)(tab + T * 128 + 64); bs[3] = *(LAS f32x4*)(tab + T * 128 + 96); }
      if constexpr (u >= 3 && u < 11) {
        constexpr int d = u - 3;
        const float a = relu_s(ffma_s(acc[2 * d], rstd, bs[d >> 1][(2 * d) & 3])), b = relu_s(ffma_s(acc[2 * d + 1], rstd, bs[d >> 1][(2 * d + 1) & 3]));
        e[d] = cvtpk(fmul_s(a, a), fmul_s(b, b));
        if constexpr (AR >= 0) awrite<AR + d>(e[d]); else PIN(e[d]);
      }
    };
    auto strip1 = [&](auto I, const int i) {
      constexpr int Is = I;
      LAS char* rb = lds + (i & 1) * 3 * SLOT;
      LAS char* nb = lds + ((i & 1) ^ 1) * 3 * SLOT;
      const int Tp = (i > 0) ? 2 * i - 1 : 0;
      sfor<3>([&](auto J) {
        constexpr int j = J;
        const int np = 3 * i + j + 5;
        const bool pa = np < 18;
        const int mm = np - 18;
        const u32x4 psrd = pa ? srdA : srdB;
        const unsigned pv = pa ? voffA : voffB;
        const unsigned pst = pa ? 8u * 9216u : 8u * 1152u;
        constexpr int jp = (j + 2) % 3;
        const int ip = i + (j + 5) / 3;
        const unsigned pso = pa ? (unsigned)((192 * jp + 48 * w) * 9216 + (f0 + 64 * ip) * 2) : (unsigned)((f0 + 192 * (mm & 1) + 48 * w) * 1152 + 128 * (mm >> 1));
        const unsigned pm0 = ((j == 0) ? ((i & 1) ^ 1) * 3 + 2 : (i & 1) * 3 + j - 1) * SLOT + 6 * w * 1024;
        constexpr int bpos = (j == 0) ? 12 : 1;
        sfor<24>([&](auto Tt) {
          constexpr int t = Tt, tt = (t + 5) % 24, jj = j + (t + 5) / 24, ks = 12 * j + t % 12;
          LAS char* base = (jj < 3) ? rb + jj * SLOT : nb;
          if constexpr (t < 12) mfma_vv<ks == 0>(acc0, wf[t % 6], xf[ks]); else mfma_vv<ks == 0>(acc1, wf[t % 6], xf[ks]);
          wf[(t + 5) % 6] = rdfrag<512>(base + ((tt < 12) ? p1o0 : p1o1) + (tt % 12) * 2048);
          if constexpr (j == 0 && t < 12) {
            if constexpr (Is < 0) { epi1(Tt, std::integral_constant<int, -1>{}, acc1, Tp); if constexpr (t == 11) awrite8_dyn<A_H + 8>(e, (i > 0 ? i - 1 : 6) * 68); }
            else if constexpr (Is > 0) epi1(Tt, std::integral_constant<int, A_H + 16 * Is - 8>{}, acc1, Tp);
          }
          if constexpr (j == 2 && t >= 12) {
            if constexpr (Is < 0) { epi1(std::integral_constant<int, t - 12>{}, std::integral_constant<int, -1>{}, acc0, 2 * i); if constexpr (t == 23) awrite8_dyn<A_H>(e, i * 68); }
            else epi1(std::integral_constant<int, t - 12>{}, std::integral_constant<int, A_H + 16 * Is>{}, acc0, 2 * i);
          }
          if constexpr (t == bpos) { WAITV(18); BAR(); }
          if constexpr (t > bpos && t <= bpos + 6) glds16(pm0 + (t - bpos - 1) * 1024, pv, psrd, pso + (t - bpos - 1) * pst);
          SB();
        });
      });
    };
#pragma unroll 1
    for (int i = 0; i < 6; ++i) strip1(std::integral_constant<int, -1>{}, i);
    STAMPT(3);
#pragma unroll
    for (int t = 0; t < 5; ++t) wf[t] = rdfrag<1024>(lds + p2o0 + t * 2048);
    asm volatile("s_nop 7" ::: "memory");
    SB();
    sfor<12>([&](auto U) { epi1(U, std::integral_constant<int, A_H + 88>{}, acc1, 11); });
    SB();
    int sl = 0;
#pragma unroll 1
    for (int i = 0; i < 9; ++i) {
      const int sn = (sl == 4) ? 0 : sl + 2;
      LAS char* rb = lds + sl * SLOT;
      LAS char* nb = lds + sn * SLOT;
      const unsigned sprev = (i > 0) ? pbase + (2 * i - 1) * 2048 : OOB;
      sfor<2>([&](auto J) {
        constexpr int j = J;
        const int mm = 2 * i + j + 5;
        const unsigned pso = (mm < 18) ? (unsigned)((f0 + 192 * (mm & 1) + 48 * w) * 1152 + 128 * (mm >> 1)) : OOB;
        const unsigned pm0 = ((j == 0) ? ((sl == 0) ? 5 : sl - 1) : sl) * SLOT + 6 * w * 1024;
        constexpr int bpos = (j == 0) ? 8 : 1;
        sfor<24>([&](auto Tt) {
          constexpr int t = Tt, tt = (t + 5) % 24, jj = j + (t + 5) / 24, ks = 12 * j + t % 12;
          LAS char* base = (jj < 2) ? rb + jj * SLOT : nb;
          if constexpr (t < 12) mfma_va<ks == 0, A_H + 4 * ks>(acc0, wf[t % 6]); else mfma_va<ks == 0, A_H + 4 * ks>(acc1, wf[t % 6]);
          wf[(t + 5) % 6] = rdfrag<1024>(base + ((tt < 12) ? p2o0 : p2o1) + (tt % 12) * 2048);
          if constexpr (j == 0 && t < 12) epi2(Tt, acc1, sprev);
          if constexpr (j == 1 && t >= 12) epi2(std::integral_constant<int, t - 12>{}, acc0, pbase + 2 * i * 2048);
          if constexpr (t == bpos) { WAITV(18); BAR(); }
          if constexpr (t > bpos && t <= bpos + 6) glds16(pm0 + (t - bpos - 1) * 1024, voffB, srdB, pso + (t - bpos - 1) * 8 * 1152);
          SB();
        });
      });
      sl = sn;
    }
    asm volatile("s_nop 11" : "+v"(acc1));
    sfor<12>([&](auto U) { epi2(U, acc1, pbase + 17 * 2048); });
    STAMPT(4);
  } else {
    STAMPT(2);
    bf16x8 wh[2];
    f32x4 acch = {0.f, 0.f, 0.f, 0.f};
    const unsigned pho = (8 * G + q4) * 128 + (((w >> 1) ^ q1) * 64) + (w & 1) * 32 + p4 * 8;
#pragma unroll
    for (int t = 0; t < 5; ++t) wf[t] = rdfrag<512>(lds + p1o0 + t * 2048);
    wh[0] = rdfrag<512>(lds + pho);
    auto epia = [&](auto U, auto ARc, const f32x16& acc) {
      constexpr int u = U, AR = ARc;
      if constexpr (u >= 3 && u < 11) {
        constexpr int d = u - 3;
        e[d] = cvtpk(fmul_s(acc[2 * d], rstd), fmul_s(acc[2 * d + 1], rstd));
        if constexpr (AR >= 0) awrite<AR + d>(e[d]); else PIN(e[d]);
      }
    };
    unsigned va1 = voffA_of(1);
    auto strip1 = [&](auto I, const int i) {
      constexpr int Is = I;
      LAS char* rb = lds + (i & 1) * 3 * SLOT;
      LAS char* nb = lds + ((i & 1) ^ 1) * 3 * SLOT;
      const unsigned va2 = voffA_of(i + 2);
      sfor<3>([&](auto J) {
        constexpr int j = J;
        const int np = 3 * i + j + 5;
        constexpr int jp = (j + 2) % 3;
        const unsigned pso = (np < 21) ? (unsigned)((192 * jp + 48 * w) * 3456 + 288 * g) : OOB;
        const unsigned pv = (j == 0) ? va1 : va2;
        const unsigned pm0 = ((j == 0) ? ((i & 1) ^ 1) * 3 + 2 : (i & 1) * 3 + j - 1) * SLOT + 6 * w * 1024;
        constexpr int bpos = (j == 0) ? 12 : 1;
        sfor<24>([&](auto Tt) {
          constexpr int t = Tt, tt = (t + 5) % 24, jj = j + (t + 5) / 24, ks = 12 * j + t % 12;
          LAS char* base = (jj < 3) ? rb + jj * SLOT : nb;
          if constexpr (t < 12) mfma_vv<ks == 0>(acc0, wf[t % 6], xf[ks]); else mfma_vv<ks == 0>(acc1, wf[t % 6], xf[ks]);
          wf[(t + 5) % 6] = rdfrag<512>(base + ((tt < 12) ? p1o0 : p1o1) + (tt % 12) * 2048);
          if constexpr ((t & 3) == 0) {
            constexpr int sg = t >> 2;
            LAS char* hb = (sg < 5) ? rb + j * SLOT : ((j < 2) ? rb + (j + 1) * SLOT : nb);
            if constexpr (j == 0 && t == 0) {
              const unsigned h0 = cvtpk(fmul_s(acch[0], rstdh), fmul_s(acch[1], rstdh)), h1 = cvtpk(fmul_s(acch[2], rstdh), fmul_s(acch[3], rstdh));
              if constexpr (Is < 0) awrite2_dyn<A_HH>(h0, h1, (i > 0 ? i - 1 : 6) * 20);
              else if constexpr (Is > 0) { awrite<A_HH + 2 * Is - 2>(h0); awrite<A_HH + 2 * Is - 1>(h1); }
            }
            mfma16_va<j == 0 && sg == 0, A_HX + 4 * (6 * j + sg)>(acch, wh[sg & 1]);
            wh[(sg + 1) & 1] = rdfrag<512>(hb + pho + ((sg + 1) % 6) * 4096);
          }
          if constexpr (j == 0 && t < 12) {
            if constexpr (Is < 0) { epia(Tt, std::integral_constant<int, -1>{}, acc1); if constexpr (t == 11) awrite8_dyn<A_H + 8>(e, (i > 0 ? i - 1 : 6) * 68); }
            else if constexpr (Is > 0) epia(Tt, std::integral_constant<int, A_H + 16 * Is - 8>{}, acc1);
          }
          if constexpr (j == 2 && t >= 12) {
            if constexpr (Is < 0) { epia(std::integral_constant<int, t - 12>{}, std::integral_constant<int, -1>{}, acc0); if constexpr (t == 23) awrite8_dyn<A_H>(e, i * 68); }
            else epia(std::integral_constant<int, t - 12>{}, std::integral_constant<int, A_H + 16 * Is>{}, acc0);
          }
          if constexpr (t == bpos) { WAITV(18); BAR(); }
          if constexpr (t > bpos && t <= bpos + 6) glds16(pm0 + (t - bpos - 1) * 1024, pv, srdA, pso + (t - bpos - 1) * 8 * 3456);
          SB();
        });
      });
      va1 = va2;
    };
#pragma unroll 1
    for (int i = 0; i < 7; ++i) strip1(std::integral_constant<int, -1>{}, i);
    asm volatile("s_nop 11" : "+v"(acc1), "+v"(acch));
    sfor<12>([&](auto U) { epia(U, std::integral_constant<int, A_H + 104>{}, acc1); });
    awrite<A_HH + 12>(cvtpk(acch[0] * rstdh, acch[1] * rstdh)); awrite<A_HH + 13>(cvtpk(acch[2] * rstdh, acch[3] * rstdh));
    WAITV(0); WAITL0(); BAR();
    STAMPT(3);
    auto issue_o = [&](int n) {
      const int slot = (n + 5) % 7;
#pragma unroll
      for (int p = 0; p < 5; ++p) {
        const int pc = 5 * w + p;
        glds16(OBASE + slot * OSLOT + pc * 1024, voffB, srdB, (pc < 18) ? (unsigned)((144 * g + 8 * pc) * 1152 + 128 * n) : OOB);
      }
    };
    issue_o(0); issue_o(1);
    {
      const unsigned rowp = (unsigned)(uintptr_t)lds + (32 * w + m + 8) * IMG_RS + h * 8;
      sfor<54>([&](auto C) { constexpr int c = C; ds_write_a64<A_H + 2 * c, 16 * c>(rowp); });
      if ((lane & 15) < 8) {
        const unsigned hp = (unsigned)(uintptr_t)lds + (lane & 7) * IMG_RS + w * 32 + G * 8;
        sfor<4>([&](auto I) { constexpr int i = I + 2; ds_write_a64<A_HH + 2 * i, 128 * i>(hp); });
        if (w < 3) ds_write_a64<A_HH + 12, 768>(hp);
      }
    }
    WAITL0(); BAR();
    STAMPT(4);
    {
      const int hd = lane & 7, i0 = 32 * w + 4 * r8;
      LAS char* qp = lds + (i0 + 8) * IMG_RS + hd * 36;
      LAS char* kp = lds + (i0 + 1) * IMG_RS + 288 + hd * 36;
      unsigned qv[4][9];
#pragma unroll
      for (int a = 0; a < 4; ++a)
#pragma unroll
        for (int c = 0; c < 9; ++c) qv[a][c] = *(LAS unsigned*)(qp + a * IMG_RS + c * 4);
      float lg[4][8];
      const int first = (rt < 2) ? 7 - i0 : 0;
#pragma unroll
      for (int jj = 0; jj < 11; ++jj) {
        unsigned kv[9];
#pragma unroll
        for (int c = 0; c < 9; ++c) kv[c] = *(LAS unsigned*)(kp + jj * IMG_RS + c * 4);
#pragma unroll
        for (int a = 0; a < 4; ++a) if (jj >= a && jj <= a + 7) {
          float s = 0.f;
#pragma unroll
          for (int c = 0; c < 9; ++c) s = dot2(qv[a][c], kv[c], s);
          s *= 0.23570226039551584f * 1.4426950408889634f;
          if (jj < 7) s = (jj < first) ? -1e9f : s;
          lg[a][jj - a] = s;
        }
      }
      float inv[4];
#pragma unroll
      for (int a = 0; a < 4; ++a) {
        float mx = lg[a][0];
#pragma unroll
        for (int u = 1; u < 8; ++u) mx = fmaxf(mx, lg[a][u]);
        float sum = 0.f;
#pragma unroll
        for (int u = 0; u < 8; ++u) { lg[a][u] = __builtin_amdgcn_exp2f(lg[a][u] - mx); sum += lg[a][u]; }
        inv[a] = __builtin_amdgcn_rcpf(sum);
      }
      float o[4][18];
#pragma unroll
      for (int a = 0; a < 4; ++a)
#pragma unroll
        for (int d = 0; d < 18; ++d) o[a][d] = 0.f;
#pragma unroll
      for (int jj = 0; jj < 11; ++jj) {
        float vv[18];
#pragma unroll
        for (int c = 0; c < 9; ++c) { const unsigned u = *(LAS unsigned*)(kp + 288 + jj * IMG_RS + c * 4); vv[2 * c] = bflo(u); vv[2 * c + 1] = bfhi(u); }
#pragma unroll
        for (int a = 0; a < 4; ++a) if (jj >= a && jj <= a + 7) {
#pragma unroll
          for (int d = 0; d < 18; ++d) o[a][d] = fmaf(lg[a][jj - a], vv[d], o[a][d]);
        }
      }
#pragma unroll
      for (int a = 0; a < 4; ++a)
#pragma unroll
        for (int c = 0; c < 9; ++c) *(LAS unsigned*)(qp + a * IMG_RS + c * 4) = cvtpk(o[a][2 * c] * inv[a], o[a][2 * c + 1] * inv[a]);
    }
    u32x4 of[9];
    {
      LAS char* rowp = lds + (32 * w + m + 8) * IMG_RS + h * 16;
#pragma unroll
      for (int s = 0; s < 9; ++s) { const u32x2 a = *(LAS u32x2*)(rowp + s * 32), b = *(LAS u32x2*)(rowp + s * 32 + 8); of[s] = u32x4{a[0], a[1], b[0], b[1]}; }
    }
    WAITL0(); WAITV(0); BAR();
    STAMPT(5);
    issue_o(2); issue_o(3); issue_o(4); issue_o(5); issue_o(6);
#pragma unroll
    for (int t = 0; t < 5; ++t) wf[t] = rdfrag<512>(lds + OBASE + 5 * OSLOT + p1o0 + t * 2048);
    sfor<9>([&](auto N) {
      constexpr int n = N;
      LAS char* rb = lds + OBASE + ((n + 5) % 7) * OSLOT;
      LAS char* nb = lds + OBASE + ((n + 6) % 7) * OSLOT;
      sfor<18>([&](auto Tt) {
        constexpr int t = Tt, tt = (t + 5) % 18, ks = t % 9;
        if constexpr (t < 9) mfma_vv<ks == 0>(acc0, wf[t % 6], of[ks]); else mfma_vv<ks == 0>(acc1, wf[t % 6], of[ks]);
        if constexpr (n < 8 || t + 5 < 18) wf[(t + 5) % 6] = rdfrag<512>(((t + 5 < 18) ? rb : nb) + ((tt < 9) ? p1o0 : p1o1) + (tt % 9) * 2048);
        if constexpr (n > 0 && t < 9) epi2(Tt, acc1, pbase + (2 * n - 1) * 2048);
        if constexpr (t >= 9) epi2(std::integral_constant<int, t - 9>{}, acc0, pbase + 2 * n * 2048);
        if constexpr (t == 8 && n >= 1 && n < 8) {
          if constexpr (n <= 3) WAITV(20); else if constexpr (n == 4) WAITV(15); else if constexpr (n == 5) WAITV(10); else if constexpr (n == 6) WAITV(5); else WAITV(0);
          BAR();
          if constexpr (n == 1) issue_o(7);
          if constexpr (n == 2) issue_o(8);
        }
        SB();
      });
    });
    asm volatile("s_nop 11" : "+v"(acc1));
    sfor<12>([&](auto U) { epi2(U, acc1, pbase + 17 * 2048); });
    STAMPT(6);
  }
  STAMPOUT();
}

__global__ __launch_bounds__(256, 1) void main_k(const void* __restrict__ x, const void* __restrict__ wqkv, const void* __restrict__ wo,
                                                 const void* __restrict__ w1, const void* __restrict__ b1, const void* __restrict__ w2,
                                                 void* __restrict__ part) {
  asm volatile("" ::: "a255");
  unsigned long long* stamps = (unsigned long long*)((char*)part + (64u << 20));
  const int bid = blockIdx.x;
  if ((bid & 7) + 8 * (bid >> 7) >= 12) body<true>(x, wqkv, wo, w1, b1, w2, part, stamps);
  else body<false>(x, wqkv, wo, w1, b1, w2, part, stamps);
}

__global__ __launch_bounds__(256) void reduce_k(const u32x4* __restrict__ part, const char* __restrict__ x, const char* __restrict__ b2, char* __restrict__ out) {
  const unsigned u = blockIdx.x * 256 + threadIdx.x;
  u32x4 v[16];
#pragma unroll
  for (int s = 0; s < 16; ++s) v[s] = __builtin_nontemporal_load(part + (size_t)s * (S_ * D_ / 8) + u);
  const unsigned lane = u & 63, q = (u >> 6) & 1, tile = u >> 7, dt = tile % 18, rw = tile / 18, w = rw & 3, rt = rw >> 2;
  const unsigned tok = 256 * (rt >> 1) + (rt & 1) + 2 * (32 * w + (lane & 31));
  const unsigned dB = (32 * dt + 16 * q + 4 * (lane >> 5)) * 2;
  const u32x2 x0 = *(const u32x2*)(x + tok * 1152 + dB), x1 = *(const u32x2*)(x + tok * 1152 + dB + 16);
  const u32x2 c0 = *(const u32x2*)(b2 + dB), c1 = *(const u32x2*)(b2 + dB + 16);
  float a[8];
  a[0] = bflo(x0[0]) + bflo(c0[0]); a[1] = bfhi(x0[0]) + bfhi(c0[0]); a[2] = bflo(x0[1]) + bflo(c0[1]); a[3] = bfhi(x0[1]) + bfhi(c0[1]);
  a[4] = bflo(x1[0]) + bflo(c1[0]); a[5] = bfhi(x1[0]) + bfhi(c1[0]); a[6] = bflo(x1[1]) + bflo(c1[1]); a[7] = bfhi(x1[1]) + bfhi(c1[1]);
#pragma unroll
  for (int s = 0; s < 16; ++s)
#pragma unroll
    for (int j = 0; j < 4; ++j) { a[2 * j] += bflo(v[s][j]); a[2 * j + 1] += bfhi(v[s][j]); }
  *(u32x2*)(out + tok * 1152 + dB) = u32x2{cvtpk(a[0], a[1]), cvtpk(a[2], a[3])};
  *(u32x2*)(out + tok * 1152 + dB + 16) = u32x2{cvtpk(a[4], a[5]), cvtpk(a[6], a[7])};
}

extern "C" void kernel_launch(void* const* d_in, const int* in_sizes, int n_in, void* d_out, int out_size, void* d_ws, size_t ws_size, hipStream_t stream) {
  (void)hipFuncSetAttribute((const void*)main_k, hipFuncAttributeMaxDynamicSharedMemorySize, LDS_BYTES);
  main_k<<<256, 256, LDS_BYTES, stream>>>(d_in[0], d_in[1], d_in[2], d_in[4], d_in[5], d_in[6], d_ws);
  reduce_k<<<S_ * D_ / 8 / 256, 256, 0, stream>>>((const u32x4*)d_ws, (const char*)d_in[0], (const char*)d_in[7], (char*)d_out);
}
```

```cpp
#include <hip/hip_runtime.h>
#include <stdint.h>
#include <utility>

typedef __bf16 bf16x8 __attribute__((ext_vector_type(8)));
typedef __bf16 bf16x4 __attribute__((ext_vector_type(4)));
typedef __bf16 bf16x2 __attribute__((ext_vector_type(2)));
typedef float f32x16 __attribute__((ext_vector_type(16)));
typedef float f32x4 __attribute__((ext_vector_type(4)));
typedef float f32x2 __attribute__((ext_vector_type(2)));
typedef unsigned u32x4 __attribute__((ext_vector_type(4)));
typedef unsigned u32x2 __attribute__((ext_vector_type(2)));
typedef unsigned long long u64;
#define LAS __attribute__((address_space(3)))
typedef __attribute__((address_space(1))) u64 gu64;
#define DI __device__ __forceinline__
#define SB() __builtin_amdgcn_sched_barrier(0)
#define IC(n) std::integral_constant<int, (n)>{}
#define RLX_AGENT __ATOMIC_RELAXED, __HIP_MEMORY_SCOPE_AGENT

template <class F, int... I> DI void sfor_(F&& f, std::integer_sequence<int, I...>) { (f(std::integral_constant<int, I>{}), ...); }
template <int N, class F> DI void sfor(F&& f) { sfor_(f, std::make_integer_sequence<int, N>{}); }

constexpr int S_ = 2048, D_ = 576, F_ = 4608;
constexpr int SLOT = 24576;
constexpr int TAB_OFF = 147456;
constexpr int LDS_BYTES = 163840;
constexpr int IMG_RS = 872;
constexpr unsigned H_OFF = 0, O_OFF = 20u << 20, P_OFF = 24u << 20, FLAG_OFF = 40u << 20, WS_BYTES = 41u << 20;
constexpr unsigned OOB = 0x40000000u;
constexpr int FL_HA = 0, FL_HB = 12, FL_O = 24, FL_FIN = 28, FL_EPOCH = 40, FL_N = 41;
constexpr unsigned CODE_BYTES = 81920;
constexpr int A_H = 56;
constexpr int A_HH = 168;
constexpr int A_HX = 184;

__host__ DI int map_role(int bid) { return (bid >> 3) & 3; }
__host__ DI int map_kg(int bid) { return bid & 3; }
__host__ DI int map_rt(int bid) { return ((bid >> 2) & 1) * 8 + (bid >> 5); }
__host__ DI int map_sp(int bid) { return map_role(bid) == 3 ? 12 + map_kg(bid) : 3 * map_kg(bid) + map_role(bid); }

DI u32x4 make_srd(const void* p, unsigned bytes) {
  uint64_t a = (uint64_t)p;
  u32x4 r; r[0] = (unsigned)a; r[1] = (unsigned)(a >> 32) & 0xffffu; r[2] = bytes; r[3] = 0x00020000u;
  return r;
}
DI void glds16(unsigned m0v, unsigned voff, u32x4 srd, unsigned soff) {
  asm volatile("s_mov_b32 m0, %0\n\ts_nop 0\n\tbuffer_load_dwordx4 %1, %2, %3 offen lds" :: "s"(m0v), "v"(voff), "s"(srd), "s"(soff) : "memory");
}
template <unsigned MC, unsigned SC> DI void glds16c(unsigned mb, unsigned voff, u32x4 srd, unsigned sb) {
  unsigned tmp;
  asm volatile("s_add_u32 m0, %1, %5\n\ts_add_u32 %0, %4, %6\n\tbuffer_load_dwordx4 %2, %3, %0 offen lds"
               : "=&s"(tmp) : "s"(mb), "v"(voff), "s"(srd), "s"(sb), "i"(MC), "i"(SC) : "memory", "scc");
}
DI u32x4 bload16(unsigned voff, u32x4 srd, unsigned soff) {
  u32x4 r;
  asm volatile("buffer_load_dwordx4 %0, %1, %2, %3 offen" : "=v"(r) : "v"(voff), "s"(srd), "s"(soff) : "memory");
  return r;
}
DI unsigned bload4(unsigned voff, u32x4 srd, unsigned soff) {
  unsigned r;
  asm volatile("buffer_load_dword %0, %1, %2, %3 offen" : "=v"(r) : "v"(voff), "s"(srd), "s"(soff) : "memory");
  return r;
}
DI u32x2 bload8(unsigned voff, u32x4 srd, unsigned soff) {
  u32x2 r;
  asm volatile("buffer_load_dwordx2 %0, %1, %2, %3 offen" : "=v"(r) : "v"(voff), "s"(srd), "s"(soff) : "memory");
  return r;
}
DI void warm(unsigned voff, u32x4 srd, unsigned soff) {
  asm volatile("buffer_load_dword a183, %0, %1, %2 offen" :: "v"(voff), "s"(srd), "s"(soff) : "memory");
}
DI u64 bload8_sc1(unsigned voff, u32x4 srd, unsigned soff) {
  u64 r;
  asm volatile("buffer_load_dwordx2 %0, %1, %2, %3 offen sc1" : "=v"(r) : "v"(voff), "s"(srd), "s"(soff) : "memory");
  return r;
}
template <int AR, int OFF> DI void aload16_sc1(unsigned voff, u32x4 srd, unsigned soff) {
  asm volatile("buffer_load_dwordx4 a[%c3:%c4], %0, %1, %2 offen offset:%c5 sc1" :: "v"(voff), "s"(srd), "s"(soff), "i"(AR), "i"(AR + 3), "i"(OFF) : "memory");
}
template <int AR, int OFF> DI void aload16(unsigned voff, u32x4 srd) {
  asm volatile("buffer_load_dwordx4 a[%c2:%c3], %0, %1, 0 offen offset:%c4" :: "v"(voff), "s"(srd), "i"(AR), "i"(AR + 3), "i"(OFF) : "memory");
}
template <int N> DI void waitv() { asm volatile("s_waitcnt vmcnt(%c0)" :: "i"(N > 63 ? 63 : N) : "memory"); }
#define WAITV(n) asm volatile("s_waitcnt vmcnt(" #n ")" ::: "memory")
#define BAR() asm volatile("s_barrier" ::: "memory")
#define WAITL0() asm volatile("s_waitcnt lgkmcnt(0)" ::: "memory")
#define PIN(x) asm volatile("" : "+v"(x))
#define PINS(x) asm volatile("" : "+s"(x))

DI unsigned cvtpk(float lo, float hi) { f32x2 v = {lo, hi}; bf16x2 b = __builtin_convertvector(v, bf16x2); return __builtin_bit_cast(unsigned, b); }
DI float bflo(unsigned u) { return __uint_as_float(u << 16); }
DI float bfhi(unsigned u) { return __uint_as_float(u & 0xffff0000u); }
DI float dot2(unsigned a, unsigned b, float c) { return __builtin_amdgcn_fdot2_f32_bf16(__builtin_bit_cast(bf16x2, a), __builtin_bit_cast(bf16x2, b), c, false); }
DI float xor32(float v) { auto r = __builtin_amdgcn_permlane32_swap(__float_as_uint(v), __float_as_uint(v), false, false); return __uint_as_float(r[0]) + __uint_as_float(r[1]); }
DI float xor16(float v) { auto r = __builtin_amdgcn_permlane16_swap(__float_as_uint(v), __float_as_uint(v), false, false); return __uint_as_float(r[0]) + __uint_as_float(r[1]); }
DI float fmul_s(float a, float b) { float r; asm("v_mul_f32_e32 %0, %1, %2" : "=v"(r) : "v"(a), "v"(b)); return r; }

template <int UOFF> DI bf16x8 rdfrag(LAS char* p) {
  bf16x4 a = __builtin_amdgcn_ds_read_tr16_b64_v4bf16((LAS bf16x4*)p);
  bf16x4 b = __builtin_amdgcn_ds_read_tr16_b64_v4bf16((LAS bf16x4*)(p + UOFF));
  return __builtin_shufflevector(a, b, 0, 1, 2, 3, 4, 5, 6, 7);
}
template <bool FIRST, class TA> DI void mfma_vv(f32x16& acc, TA a, u32x4 b) {
  if constexpr (FIRST) asm volatile("v_mfma_f32_32x32x16_bf16 %0, %1, %2, 0" : "=&v"(acc) : "v"(a), "v"(b));
  else                 asm volatile("v_mfma_f32_32x32x16_bf16 %0, %1, %2, %0" : "+v"(acc) : "v"(a), "v"(b));
}
DI void mfma_vvc(f32x16& acc, u32x4 a, u32x4 b, const f32x16& c) { asm volatile("v_mfma_f32_32x32x16_bf16 %0, %1, %2, %3" : "=&v"(acc) : "v"(a), "v"(b), "v"(c)); }
template <bool FIRST, int AR> DI void mfma_va(f32x16& acc, bf16x8 a) {
  if constexpr (FIRST) asm volatile("v_mfma_f32_32x32x16_bf16 %0, %1, a[%c2:%c3], 0" : "=&v"(acc) : "v"(a), "i"(AR), "i"(AR + 3));
  else                 asm volatile("v_mfma_f32_32x32x16_bf16 %0, %1, a[%c2:%c3], %0" : "+v"(acc) : "v"(a), "i"(AR), "i"(AR + 3));
}
template <bool FIRST, int AR> DI void mfma16_va(f32x4& acc, bf16x8 a) {
  if constexpr (FIRST) asm volatile("v_mfma_f32_16x16x32_bf16 %0, %1, a[%c2:%c3], 0" : "=&v"(acc) : "v"(a), "i"(AR), "i"(AR + 3));
  else                 asm volatile("v_mfma_f32_16x16x32_bf16 %0, %1, a[%c2:%c3], %0" : "+v"(acc) : "v"(a), "i"(AR), "i"(AR + 3));
}
template <bool FIRST, int AR> DI void mfma16_aa(f32x4& acc) {
  if constexpr (FIRST) asm volatile("v_mfma_f32_16x16x32_bf16 %0, a[%c1:%c2], a[%c1:%c2], 0" : "=&v"(acc) : "i"(AR), "i"(AR + 3));
  else                 asm volatile("v_mfma_f32_16x16x32_bf16 %0, a[%c1:%c2], a[%c1:%c2], %0" : "+v"(acc) : "i"(AR), "i"(AR + 3));
}
DI float ror8(float v) { return __int_as_float(__builtin_amdgcn_update_dpp(0, __float_as_int(v), 0x128, 0xf, 0xf, false)); }
template <int AR> DI void awrite(unsigned v) { asm volatile("v_accvgpr_write_b32 a[%c1], %0" :: "v"(v), "i"(AR)); }
template <int AR> DI unsigned hpair(float a0, float a1, float r, float b0, float b1) {
  unsigned e; float t;
  asm volatile("v_fma_f32 %0, %2, %4, %5\n\tv_fma_f32 %1, %3, %4, %6\n\tv_max_f32_e32 %0, 0, %0\n\tv_max_f32_e32 %1, 0, %1\n\t"
               "v_mul_f32_e32 %0, %0, %0\n\tv_mul_f32_e32 %1, %1, %1\n\tv_cvt_pk_bf16_f32 %0, %0, %1\n\tv_accvgpr_write_b32 a[%c7], %0"
               : "=&v"(e), "=&v"(t) : "v"(a0), "v"(a1), "v"(r), "v"(b0), "v"(b1), "i"(AR));
  return e;
}
template <int AR> DI void spair(float a0, float a1, float r) {
  float t0, t1;
  asm volatile("v_mul_f32_e32 %0, %2, %4\n\tv_mul_f32_e32 %1, %3, %4\n\tv_cvt_pk_bf16_f32 %0, %0, %1\n\tv_accvgpr_write_b32 a[%c5], %0"
               : "=&v"(t0), "=&v"(t1) : "v"(a0), "v"(a1), "v"(r), "i"(AR));
}
template <int AR, int OFF> DI void ds_write_a64(unsigned addr) { asm volatile("ds_write_b64 %0, a[%c1:%c2] offset:%c3" :: "v"(addr), "i"(AR), "i"(AR + 1), "i"(OFF) : "memory"); }

#define STAMPT(i)
#define STAMPOUT()

constexpr int NST = 39;
constexpr int ST_PUB_HA = 12, ST_PUB_HB = 21, ST_POLL_HA = 15, ST_POLL_HB = 24, ST_POLL_O = 33, ST_LD_X = 31;
constexpr int ST_LD_P1A = 18, ST_LD_P2A = 20, ST_LD_P1B = 26, ST_LD_P2B = 28, ST_LD_O = 34;
constexpr int m_steps(int n) { return n < 36 ? 24 : 18; }
constexpr int m_glds(int n) { return n < 2 ? 12 : n + 5 < 36 ? 6 : (n + 5 < NST ? 5 : 0); }
constexpr int m_warm(int n) { return n == 2 ? 2 : n == 5 ? 3 : n == 8 ? 4 : -1; }
constexpr bool in2(int n, int s) { return n == s || n == s + 1; }
constexpr int m_ops(int n) {
  if (n < 0) return 0;
  int c = m_glds(n);
  if (n <= 18 && n % 3 == 0 && n > 0) c += 2;
  if (n < 18 && n % 3 == 2) c += 2;
  if (m_warm(n) >= 0) c += 1;
  if (n == ST_LD_X) c += 12;
  if (n == ST_POLL_HA || n == ST_POLL_HB || n == ST_POLL_O) c += 1;
  if (in2(n, ST_LD_P1A) || in2(n, ST_LD_P2A) || in2(n, ST_LD_P1B) || in2(n, ST_LD_P2B)) c += 6;
  if (n == ST_LD_O) c += 9;
  if (n >= 36) c += (n > 36 ? 4 : 2);
  return c;
}
constexpr int kb_areg(int kb) { return kb == 0 ? A_H : kb == 1 ? A_H + 48 : kb == 2 ? A_H + 96 : kb == 3 ? A_H + 144 : kb == 4 ? A_H : kb == 5 ? A_H + 48 : A_H + 96; }

DI void spin_until(gu64* p, u64 tag) {
  for (unsigned spins = 0; spins < (1u << 18); ++spins) {
    const u64 f = __hip_atomic_load(p, RLX_AGENT);
    if (__all(f == tag)) break;
    __builtin_amdgcn_s_sleep(1);
  }
}

template <bool attn>
DI void body(const void* __restrict__ x, const void* __restrict__ wqkv, const void* __restrict__ wo, const void* __restrict__ w1, const void* __restrict__ b1,
             const void* __restrict__ w2, const char* __restrict__ b2, char* __restrict__ out, void* __restrict__ ws, const void* code) {
  extern __shared__ __attribute__((aligned(16))) char smem[];
  LAS char* lds = (LAS char*)smem;
  const int tid = threadIdx.x, lane = tid & 63;
  const int w = __builtin_amdgcn_readfirstlane(tid >> 6);
  const int bid = blockIdx.x;
  const int kg = map_kg(bid), cg = map_role(bid), rt = map_rt(bid);
  const int tokbase = 256 * (rt >> 1) + (rt & 1);
  const int m = lane & 31, h = lane >> 5, r8 = lane >> 3, pos = lane & 7;
  const int G = lane >> 4, q4 = (lane >> 2) & 3, p4 = lane & 3;
  STAMPT(0);

  const u32x4 srdx = make_srd(x, S_ * D_ * 2);
  const u32x4 srdA = attn ? make_srd(wqkv, D_ * 3 * D_ * 2) : make_srd(w1, D_ * F_ * 2);
  const u32x4 srdW2 = make_srd(w2, F_ * D_ * 2), srdWo = make_srd(wo, D_ * D_ * 2), srdws = make_srd(ws, WS_BYTES);
  const __amdgpu_buffer_rsrc_t rws = __builtin_amdgcn_make_buffer_rsrc(ws, 0, WS_BYTES, 0x00020000);
  const unsigned swz = pos ^ (((r8 >> 1) & 1) << 2);
  const int fs = 3 * kg + cg, f0 = fs * 384;
  const unsigned flo = FLAG_OFF + rt * FL_N * 128;
  gu64* flags = (gu64*)((char*)ws + flo);
  const unsigned l16 = lane * 16;

  u64 epoch = bload8_sc1(0, srdws, flo + FL_EPOCH * 128);
  unsigned braw = 0;
  if constexpr (!attn) braw = bload4(tid * 4, make_srd((const char*)b1 + f0 * 2, 768), 0);
  {
    const unsigned vx0 = r8 * 2304 + ((pos ^ G) << 4), vx1 = r8 * 2304 + ((pos ^ (4 + G)) << 4);
    unsigned sx = tokbase * 1152 + w * 32 * 2304, mx = w * 36864;
#pragma unroll 1
    for (int t = 0; t < 9; ++t) {
      glds16c<0, 0>(mx, vx0, srdx, sx); glds16c<1024, 8 * 2304>(mx, vx1, srdx, sx); glds16c<2048, 16 * 2304>(mx, vx0, srdx, sx); glds16c<3072, 24 * 2304>(mx, vx1, srdx, sx);
      sx += 128; mx += 4096;
    }
  }
  const unsigned wi = w * 64 + lane;
  auto warm_w = [&](auto K) {
    constexpr int k = K;
    const unsigned r = rt & 7;
    if constexpr (!attn) {
      if constexpr (k < 3) { const unsigned a = r * 144 + wi; warm(wi < 144 ? (a >> 1) * 9216 + (a & 1) * 128 : OOB, srdA, f0 * 2 + k * 256); }
      else warm(wi < 216 ? (r * 216 + wi) * 128 : OOB, srdW2, (f0 + 192 * (k - 3)) * 1152);
    } else {
      if constexpr (k < 3) { const unsigned a = r * 216 + wi; warm(wi < 216 ? (a / 3) * 3456 + (a % 3) * 128 : OOB, srdA, ((288 * kg) & ~127) + k * 1152); }
      else warm(wi < 162 ? (r * 162 + wi) * 128 : OOB, srdWo, 144 * kg * 1152);
    }
  };
  warm_w(IC(0));
  { const unsigned ci = (bid >> 3) * 4 + w; warm(lane * 128, make_srd(code, CODE_BYTES), ci < 10 ? ci * 8192 : OOB); }
  constexpr int NPF = 2 + (attn ? 18 : 0);
  if constexpr (attn) {
    sfor<72>([&](auto I) { awrite<A_HX + I>(0x3f803f80u); });
    int ht = tokbase + 2 * ((lane & 7) - 8); ht = ht < 0 ? 0 : ht;
    const unsigned vh = ht * 1152 + G * 16;
    if ((lane & 15) < 8) sfor<18>([&](auto Sg) { aload16<A_HX + 4 * Sg, 64 * Sg>(vh, srdx); });
  }
  u32x4 xf[36];
  float rstd, negmu;
  {
    LAS char* xa = lds + w * 36864;
    const unsigned xrd = m * 128 + ((h ^ ((m >> 1) & 7)) << 4);
    float s0 = 0.f, s1 = 0.f, q0 = 0.f, q1 = 0.f;
    unsigned ones = 0x3f803f80u; PIN(ones);
    sfor<10>([&](auto T) {
      constexpr int t = T;
      if constexpr (t < 9) {
        waitv<4 * (8 - t) + NPF>();
#pragma unroll
        for (int u = 0; u < 4; ++u) xf[4 * t + u] = *(LAS u32x4*)(xa + t * 4096 + (xrd ^ (u << 5)));
      }
      if constexpr (t > 0) {
#pragma unroll
        for (int u = 0; u < 4; ++u) {
          const u32x4 v = xf[4 * t - 4 + u];
          s0 = dot2(v[0], ones, s0); s1 = dot2(v[1], ones, s1); q0 = dot2(v[0], v[0], q0); q1 = dot2(v[1], v[1], q1);
          s0 = dot2(v[2], ones, s0); s1 = dot2(v[3], ones, s1); q0 = dot2(v[2], v[2], q0); q1 = dot2(v[3], v[3], q1);
        }
      }
      if constexpr (t == 8) { WAITL0(); BAR(); }
      SB();
    });
    const float mu = xor32(s0 + s1) * (1.0f / D_);
    const float var = fmaxf(xor32(q0 + q1) * (1.0f / D_) - mu * mu, 0.f);
    rstd = __builtin_amdgcn_rsqf(var + 1e-5f);
    negmu = -mu;
  }
  STAMPT(1);
  PIN(epoch); PIN(braw);
  const u64 tag = epoch * 0x9E3779B97F4A7C15ull + 0x632BE59BD9B4E019ull;
  if constexpr (!attn) { if (tid < 192) { f32x2 bv = {bflo(braw), bfhi(braw)}; *(LAS f32x2*)(lds + TAB_OFF + tid * 8) = bv; } }

  constexpr unsigned strideA = attn ? 3456u : 9216u;
  auto voffA_of = [&](int strip) -> unsigned {
    const unsigned v = 8 * strip + swz;
    return attn ? r8 * 3456 + v * 16 + ((v >= 18) + (v >= 36) + (v >= 54)) * 864 : r8 * 9216 + swz * 16;
  };
  unsigned soA = 48 * w * strideA + (attn ? 288 * kg : f0 * 2);
  unsigned m6 = 6 * w * 1024;
  PINS(soA); PINS(m6);
  f32x16 acc0, acc1;
  {
    u32x4 id0, id1;
#pragma unroll
    for (int j = 0; j < 4; ++j) {
      const int k0 = 8 * h + 2 * j;
      id0[j] = (m == k0 ? 0x3f80u : 0u) | (m == k0 + 1 ? 0x3f800000u : 0u);
      id1[j] = (m == k0 + 16 ? 0x3f80u : 0u) | (m == k0 + 17 ? 0x3f800000u : 0u);
    }
    f32x16 nm;
#pragma unroll
    for (int i = 0; i < 16; ++i) nm[i] = negmu;
    const unsigned va0 = voffA_of(0);
    asm volatile("s_nop 1" : "+v"(id0), "+v"(id1), "+v"(nm));
    sfor<19>([&](auto B) {
      constexpr int b = B;
      if constexpr (b < 18) {
        f32x16& a = (b & 1) ? acc1 : acc0;
        mfma_vvc(a, id0, xf[2 * b], nm);
                mfma_vv<false>(a, id1, xf[2 * b + 1]);
        { constexpr int n = b / 6, p = b % 6; glds16c<n * SLOT + p * 1024, (192 * n + 8 * p) * strideA>(m6, va0, srdA, soA); }
      } else { warm_w(IC(1)); asm volatile("s_nop 11" : "+v"(acc1)); }
      SB();
      if constexpr (b > 0) {
        const f32x16& a = (b & 1) ? acc0 : acc1;
#pragma unroll
        for (int d = 0; d < 8; ++d) xf[2 * b - 2 + (d >> 2)][d & 3] = cvtpk(a[2 * d], a[2 * d + 1]);
        PIN(xf[2 * b - 2]); PIN(xf[2 * b - 1]);
      }
      SB();
    });
  }

  const unsigned colo = (G & 1) * 32 + p4 * 8, q1 = (q4 >> 1) & 1;
  const unsigned po0 = (4 * h + q4) * 128 + q1 * 64 + colo, po1 = (4 * h + q4) * 128 + (q1 ^ 1) * 64 + colo;
  LAS char* rb[2][3];
#pragma unroll
  for (int i = 0; i < 3; ++i) { rb[0][i] = lds + po0 + 2 * i * SLOT; rb[1][i] = lds + po1 + 2 * i * SLOT; PIN(rb[0][i]); PIN(rb[1][i]); }
  bf16x8 wf[6];
  unsigned e[8];

  if constexpr (!attn) {
    WAITV(13); WAITL0(); BAR();
    STAMPT(2);
#pragma unroll
    for (int t = 0; t < 5; ++t) wf[t] = rdfrag<1024>(rb[0][0] + t * 2048);
    const unsigned voffA = voffA_of(0), voffB = r8 * 1152 + swz * 16;
    const int c1 = cg == 2 ? 0 : cg + 1, c2 = cg == 0 ? 2 : cg - 1;
    unsigned soB[3];
    soB[0] = (f0 + 48 * w) * 1152 + 384 * cg; soB[1] = ((3 * kg + c1) * 384 + 48 * w) * 1152 + 384 * cg; soB[2] = ((3 * kg + c2) * 384 + 48 * w) * 1152 + 384 * cg;
    unsigned soO = (144 * kg + 40 * w) * 1152 + 384 * cg, m5 = 5 * w * 1024;
    unsigned hso[3];
    hso[0] = H_OFF + ((rt * 12 + fs) * 4 + w) * 24576; hso[1] = H_OFF + ((rt * 12 + 3 * kg + c1) * 4 + w) * 24576; hso[2] = H_OFF + ((rt * 12 + 3 * kg + c2) * 4 + w) * 24576;
    const unsigned oso = O_OFF + ((rt * 4 + kg) * 4 + w) * 9216;
    const unsigned pso = P_OFF + (((rt * 3 + cg) * 4 + kg) * 4 + w) * 12288;
    const unsigned vpoll = (3 * kg + ((lane & 1) ? c1 : c2)) * 128;
    LAS char* tab = lds + TAB_OFF + h * 16;
    f32x4 bs[4];
    f32x16 ac[6];
    u64 pollv = 0;
    const unsigned tok = tokbase + 2 * (32 * w + m), dB0 = (192 * cg + 48 * kg + 4 * h) * 2;
    const u32x4 srdb2 = make_srd(b2, D_ * 2);
    u32x2 xr[3][2], cr[3][2];
    auto epi1 = [&](auto U, auto Tc, const f32x16& acc) {
      constexpr int u = U, T = Tc;
      if constexpr (u == 0) { bs[0] = *(LAS f32x4*)(tab + T * 128); bs[1] = *(LAS f32x4*)(tab + T * 128 + 32); }
      if constexpr (u == 2) { bs[2] = *(LAS f32x4*)(tab + T * 128 + 64); bs[3] = *(LAS f32x4*)(tab + T * 128 + 96); }
      if constexpr (u >= 3 && u < 11) {
        constexpr int d = u - 3;
        e[d] = hpair<A_H + 8 * T + d>(acc[2 * d], acc[2 * d + 1], rstd, bs[d >> 1][(2 * d) & 3], bs[d >> 1][(2 * d + 1) & 3]);
      }
      if constexpr (u == 7) __builtin_amdgcn_raw_buffer_store_b128(u32x4{e[0], e[1], e[2], e[3]}, rws, l16 + (T & 1) * 2048, hso[0] + (T >> 1) * 4096, 16);
      if constexpr (u == 11) __builtin_amdgcn_raw_buffer_store_b128(u32x4{e[4], e[5], e[6], e[7]}, rws, l16 + (T & 1) * 2048 + 1024, hso[0] + (T >> 1) * 4096, 16);
    };
    auto epi2 = [&](auto U, auto Tc) {
      constexpr int u = U, T = Tc;
      if constexpr (u >= 3 && u < 7) {
        constexpr int d = 2 * (u - 3);
        e[d] = cvtpk(ac[T][2 * d], ac[T][2 * d + 1]); e[d + 1] = cvtpk(ac[T][2 * d + 2], ac[T][2 * d + 3]);
        PIN(e[d]); PIN(e[d + 1]);
      }
      if constexpr (u == 5) __builtin_amdgcn_raw_buffer_store_b128(u32x4{e[0], e[1], e[2], e[3]}, rws, l16 + (T & 1) * 2048, pso + (T >> 1) * 4096, 16);
      if constexpr (u == 7) __builtin_amdgcn_raw_buffer_store_b128(u32x4{e[4], e[5], e[6], e[7]}, rws, l16 + (T & 1) * 2048 + 1024, pso + (T >> 1) * 4096, 16);
    };
    sfor<NST>([&](auto Nn) {
      constexpr int n = Nn, ns = m_steps(n), hs = ns / 2;
      constexpr int base6 = (n < 36 ? 24 * n : 24 * 36 + 18 * (n - 36)) % 6;
      static_assert(base6 == 0, "fragment ring phase");
      if constexpr (n == 18) { STAMPT(3); }
      sfor<ns>([&](auto Tt) {
        constexpr int t = Tt, c = t / hs, ks = t % hs;
        if constexpr (n < 18) {
          constexpr int kx = 12 * (n % 3) + ks;
          if constexpr (c == 0) mfma_vv<kx == 0>(acc0, wf[t % 6], xf[kx]); else mfma_vv<kx == 0>(acc1, wf[t % 6], xf[kx]);
        } else {
          constexpr int kb = (n - 18) / 3, T = 2 * ((n - 18) % 3) + c;
          mfma_va<kb == 0 && ks == 0, kb_areg(kb) + 4 * ks>(ac[T], wf[t % 6]);
        }
        {
          constexpr int n2 = (t + 5 < ns) ? n : n + 1, t2 = (t + 5 < ns) ? t + 5 : t + 5 - ns;
          if constexpr (n2 < NST) {
            constexpr int hs2 = m_steps(n2) / 2, sl = n2 % 6;
            wf[(t + 5) % 6] = rdfrag<1024>(rb[t2 / hs2][sl >> 1] + (sl & 1) * SLOT + (t2 % hs2) * 2048);
          }
        }
        if constexpr (t == 0 && n <= ST_LD_O) {
          constexpr int allow = (n == 0) ? 7 : (n == 1) ? 1 + m_ops(0) : (n == 2) ? 6 + m_ops(1) : (n >= ST_LD_O) ? 0 : (n == ST_LD_P1B) ? m_ops(n - 1) : m_ops(n - 1) + m_ops(n - 2);
          waitv<allow>(); BAR();
          if constexpr (n == ST_PUB_HA || n == ST_PUB_HB) { if (tid == 0) __hip_atomic_store(flags + ((n == ST_PUB_HA ? FL_HA : FL_HB) + fs) * 16, tag, RLX_AGENT); }
          if constexpr (n == ST_LD_P1A || n == ST_LD_P1B || n == ST_LD_O) {
            PIN(pollv);
            if (__builtin_expect(!__all(pollv == tag), 0))
              spin_until((gu64*)((char*)flags + (n == ST_LD_O ? (FL_O + kg) * 128 : (n == ST_LD_P1A ? FL_HA : FL_HB) * 128 + vpoll)), tag);
          }
        }
        if constexpr (n < 2 ? (t % 2 == 1) : (t % 4 == 1 && t / 4 < m_glds(n))) {
          constexpr int np = n < 2 ? 3 + 2 * n + t / 12 : n + 5, p = n < 2 ? (t / 2) % 6 : t / 4, sl = np % 6;
          if constexpr (np < 18) glds16c<sl * SLOT + p * 1024, (192 * (np % 3) + 8 * p) * 9216 + 128 * (np / 3)>(m6, voffA, srdA, soA);
          else if constexpr (np < 36) {
            constexpr int kb = (np - 18) / 3, src = kb < 2 ? 0 : 1 + (kb & 1), half = kb < 2 ? kb : (kb >= 4);
            glds16c<sl * SLOT + p * 1024, (192 * half + 8 * p) * 1152 + 128 * ((np - 18) % 3)>(m6, voffB, srdW2, soB[src]);
          } else glds16c<sl * SLOT + p * 1024, 8 * p * 1152 + 128 * (np - 36)>(m5, voffB, srdWo, soO);
        }
        if constexpr (n > 0 && n <= 18 && n % 3 == 0 && t < 12) epi1(Tt, IC(2 * (n / 3) - 1), acc1);
        if constexpr (n < 18 && n % 3 == 2 && t >= 12) epi1(IC(t - 12), IC(2 * (n / 3)), acc0);
        if constexpr (n > 36 && t < 9) epi2(Tt, IC(2 * (n - 36) - 1));
        if constexpr (n >= 36 && t >= 9) epi2(IC(t - 9), IC(2 * (n - 36)));
        if constexpr (t == 22 && m_warm(n) >= 0) warm_w(IC(m_warm(n)));
        if constexpr (n == ST_LD_X && t % 2 == 0) {
          constexpr int k = t / 8, j = (t / 4) & 1;
          if constexpr ((t / 2) & 1) cr[k][j] = bload8(dB0, srdb2, 32 * k + 16 * j); else xr[k][j] = bload8(tok * 1152 + dB0, srdx, 32 * k + 16 * j);
        }
        if constexpr (t == 3 && (n == ST_POLL_HA || n == ST_POLL_HB)) pollv = bload8_sc1(vpoll, srdws, flo + (n == ST_POLL_HA ? FL_HA : FL_HB) * 128);
        if constexpr (t == 3 && n == ST_POLL_O) pollv = bload8_sc1(0, srdws, flo + (FL_O + kg) * 128);
        if constexpr (n == ST_LD_O && t % 2 == 1 && t < 18) { constexpr int f = t / 2; aload16_sc1<kb_areg(6) + 4 * f, (f & 3) * 1024>(l16, srdws, oso + (f >> 2) * 4096); }
        if constexpr (n == 35 && t == 23) waitv<0>();
        if constexpr (t % 4 == 3) {
          constexpr int k = t / 4;
          if constexpr (in2(n, ST_LD_P1A)) { constexpr int f = 6 * (n - ST_LD_P1A) + k; aload16_sc1<kb_areg(2) + 4 * f, (f & 3) * 1024>(l16, srdws, hso[1] + (f >> 2) * 4096); }
          if constexpr (in2(n, ST_LD_P2A)) { constexpr int f = 6 * (n - ST_LD_P2A) + k; aload16_sc1<kb_areg(3) + 4 * f, (f & 3) * 1024>(l16, srdws, hso[2] + (f >> 2) * 4096); }
          if constexpr (in2(n, ST_LD_P1B)) { constexpr int f = 6 * (n - ST_LD_P1B) + k; aload16_sc1<kb_areg(4) + 4 * f, (f & 3) * 1024>(l16, srdws, hso[1] + 12288 + (f >> 2) * 4096); }
          if constexpr (in2(n, ST_LD_P2B)) { constexpr int f = 6 * (n - ST_LD_P2B) + k; aload16_sc1<kb_areg(5) + 4 * f, (f & 3) * 1024>(l16, srdws, hso[2] + 12288 + (f >> 2) * 4096); }
        }
        SB();
      });
    });
    asm volatile("s_nop 11" : "+v"(ac[5]));
    sfor<8>([&](auto U) { epi2(U, IC(5)); });
    STAMPT(4);
    asm volatile("s_waitcnt vmcnt(0)" : "+v"(xr[0][0]), "+v"(xr[0][1]), "+v"(xr[1][0]), "+v"(xr[1][1]), "+v"(xr[2][0]), "+v"(xr[2][1]),
                 "+v"(cr[0][0]), "+v"(cr[0][1]), "+v"(cr[1][0]), "+v"(cr[1][1]), "+v"(cr[2][0]), "+v"(cr[2][1]) :: "memory");
    BAR();
    if (tid == 0) __hip_atomic_store(flags + (FL_FIN + fs) * 16, tag, RLX_AGENT);
    STAMPT(5);
    spin_until(flags + (FL_FIN + 3 * (lane & 3) + cg) * 16, tag);
    if (fs == 0 && tid == 0) __hip_atomic_store(flags + FL_EPOCH * 16, tag, RLX_AGENT);
    u32x4 v[3][4];
#pragma unroll
    for (int k = 0; k < 3; ++k)
#pragma unroll
      for (int s = 0; s < 4; ++s) v[k][s] = __builtin_amdgcn_raw_buffer_load_b128(rws, l16, P_OFF + (((rt * 3 + cg) * 4 + s) * 4 + w) * 12288 + (3 * kg + k) * 1024, 16);
#pragma unroll
    for (int k = 0; k < 3; ++k) {
      const unsigned dB = dB0 + 32 * k;
      float a[8];
#pragma unroll
      for (int j = 0; j < 4; ++j) { a[2 * j] = bflo(xr[k][j >> 1][j & 1]) + bflo(cr[k][j >> 1][j & 1]); a[2 * j + 1] = bfhi(xr[k][j >> 1][j & 1]) + bfhi(cr[k][j >> 1][j & 1]); }
#pragma unroll
      for (int s = 0; s < 4; ++s)
#pragma unroll
        for (int j = 0; j < 4; ++j) { a[2 * j] += bflo(v[k][s][j]); a[2 * j + 1] += bfhi(v[k][s][j]); }
      *(u32x2*)(out + tok * 1152 + dB) = u32x2{cvtpk(a[0], a[1]), cvtpk(a[2], a[3])};
      *(u32x2*)(out + tok * 1152 + dB + 16) = u32x2{cvtpk(a[4], a[5]), cvtpk(a[6], a[7])};
    }
    STAMPT(6);
  } else {
    f32x4 acch;
    float rstdh, negmuh;
    {
      WAITV(19);
      sfor<18>([&](auto Sg) { mfma16_aa<Sg == 0, A_HX + 4 * Sg>(acch); });
      asm volatile("s_nop 7" : "+v"(acch));
      const float dg = (p4 == 0) ? acch[0] : (p4 == 1) ? acch[1] : (p4 == 2) ? acch[2] : acch[3];
      const float sq = xor32(xor16(G == q4 ? dg : 0.f)), sm = xor32(xor16(G == 2 ? acch[0] : 0.f));
      const float mu = sm * (1.0f / D_);
      rstdh = __builtin_amdgcn_rsqf(fmaxf(sq * (1.0f / D_) - mu * mu, 0.f) + 1e-5f);
      negmuh = -mu;
    }
    auto epih = [&](auto I) {
      float r[4];
#pragma unroll
      for (int k = 0; k < 4; ++k) r[k] = fmaf(ror8(acch[k]), negmuh, acch[k]);
      spair<A_HH + 2 * I>(r[0], r[1], rstdh); spair<A_HH + 2 * I + 1>(r[2], r[3], rstdh);
    };
    WAITV(13); BAR();
    STAMPT(2);
    bf16x8 wh[2];
    LAS char* hb[3];
#pragma unroll
    for (int i = 0; i < 3; ++i) { hb[i] = lds + (8 * G + q4) * 128 + (((w >> 1) ^ q1) * 64) + (w & 1) * 32 + p4 * 8 + 2 * i * SLOT; PIN(hb[i]); }
#pragma unroll
    for (int t = 0; t < 5; ++t) wf[t] = rdfrag<1024>(rb[0][0] + t * 2048);
    auto epia = [&](auto U, auto Tc, const f32x16& acc) {
      constexpr int u = U, T = Tc;
      if constexpr (u >= 3 && u < 11) spair<A_H + 8 * T + u - 3>(acc[2 * (u - 3)], acc[2 * (u - 3) + 1], rstd);
    };
    unsigned va = voffA_of(1);
    sfor<21>([&](auto Nn) {
      constexpr int n = Nn, j = n % 3, i = n / 3;
      sfor<24>([&](auto Tt) {
        constexpr int t = Tt, c = t / 12, ks = t % 12, kx = 12 * j + ks;
        if constexpr (c == 0) mfma_vv<kx == 0>(acc0, wf[t % 6], xf[kx]); else mfma_vv<kx == 0>(acc1, wf[t % 6], xf[kx]);
        {
          constexpr int n2 = (t + 5 < 24) ? n : n + 1, t2 = (t + 5) % 24, sl = n2 % 6;
          if constexpr (n2 < 21) wf[(t + 5) % 6] = rdfrag<1024>(rb[t2 / 12][sl >> 1] + (sl & 1) * SLOT + (t2 % 12) * 2048);
        }
        if constexpr (t == 0 && n + 1 < 21) { waitv<(n == 0 ? 7 : n == 1 ? 13 : n + 4 < 21 ? 18 : 6 * (19 - n))>(); BAR(); }
        if constexpr ((t & 3) == 2 && n >= 5) {
          constexpr int sg = t >> 2;
          if constexpr (n >= 6) {
            if constexpr (j == 0 && sg == 0 && n > 6) epih(IC(i - 1));
            mfma16_va<j == 0 && sg == 0, A_HX + 4 * (6 * j + sg)>(acch, wh[sg & 1]);
          }
          constexpr int n2 = sg < 5 ? n : n + 1, sl = n2 % 6;
          if constexpr (n2 >= 6 && n2 < 21) wh[(sg + 1) & 1] = rdfrag<512>(hb[sl >> 1] + (sl & 1) * SLOT + ((sg + 1) % 6) * 4096);
        }
        if constexpr (t == (n == 1 ? 12 : 0) && j == 1 && n + 5 < 21) va = voffA_of(i + 2);
        if constexpr (t == 22 && (n == 2 || n == 8)) warm_w(IC(n == 2 ? 2 : 3));
        if constexpr (n < 2 ? (t % 2 == 1) : (t % 4 == 1 && n + 5 < 21)) { constexpr int np = n < 2 ? 3 + 2 * n + t / 12 : n + 5, p = n < 2 ? (t / 2) % 6 : t / 4; glds16c<(np % 6) * SLOT + p * 1024, (192 * (np % 3) + 8 * p) * 3456>(m6, va, srdA, soA); }
        if constexpr (n > 0 && j == 0 && t < 12) epia(Tt, IC(2 * i - 1), acc1);
        if constexpr (j == 2 && t >= 12) epia(IC(t - 12), IC(2 * i), acc0);
        SB();
      });
    });
    asm volatile("s_nop 11" : "+v"(acc1), "+v"(acch));
    sfor<7>([&](auto U) { epia(U, IC(13), acc1); });
    epih(IC(6));
    WAITL0(); BAR();
    STAMPT(3);
    {
      const unsigned rowp = (unsigned)(uintptr_t)lds + (32 * w + m + 8) * IMG_RS + h * 8;
      sfor<54>([&](auto C) { constexpr int c = C; ds_write_a64<A_H + 2 * c, 16 * c>(rowp); });
      if ((lane & 15) < 8) {
        const unsigned hp = (unsigned)(uintptr_t)lds + (lane & 7) * IMG_RS + w * 32 + G * 8;
        sfor<4>([&](auto I) { constexpr int i = I + 2; ds_write_a64<A_HH + 2 * i, 128 * i>(hp); });
        if (w < 3) ds_write_a64<A_HH + 12, 768>(hp);
      }
    }
    WAITL0(); BAR();
    STAMPT(4);
    {
      const int hd = lane & 7, i0 = 32 * w + 4 * r8;
      LAS char* rp[11];
#pragma unroll
      for (int jj = 0; jj < 11; ++jj) { rp[jj] = lds + (i0 + 1 + jj) * IMG_RS + hd * 36; PIN(rp[jj]); }
      unsigned qv[4][9], kv[11][9], vv[11][9];
#pragma unroll
      for (int a = 0; a < 4; ++a)
#pragma unroll
        for (int c = 0; c < 9; ++c) qv[a][c] = *(LAS unsigned*)(rp[7 + a] + c * 4);
#pragma unroll
      for (int jj = 0; jj < 3; ++jj)
#pragma unroll
        for (int c = 0; c < 9; ++c) kv[jj][c] = *(LAS unsigned*)(rp[jj] + 288 + c * 4);
      SB();
      float lg[4][8];
      sfor<8>([&](auto U) {
        constexpr int u = U;
#pragma unroll
        for (int c = 0; c < 9; ++c) kv[u + 3][c] = *(LAS unsigned*)(rp[u + 3] + 288 + c * 4);
        if constexpr (u > 0) {
#pragma unroll
          for (int a = 0; a < 4; ++a) lg[a][u - 1] = 0.f;
#pragma unroll
          for (int c = 0; c < 9; ++c)
#pragma unroll
            for (int a = 0; a < 4; ++a) lg[a][u - 1] = dot2(qv[a][c], kv[a + u - 1][c], lg[a][u - 1]);
        }
        SB();
      });
#pragma unroll
      for (int jj = 0; jj < 3; ++jj)
#pragma unroll
        for (int c = 0; c < 9; ++c) vv[jj][c] = *(LAS unsigned*)(rp[jj] + 576 + c * 4);
#pragma unroll
      for (int a = 0; a < 4; ++a) lg[a][7] = 0.f;
#pragma unroll
      for (int c = 0; c < 9; ++c)
#pragma unroll
        for (int a = 0; a < 4; ++a) lg[a][7] = dot2(qv[a][c], kv[a + 7][c], lg[a][7]);
      if (rt < 2) {
#pragma unroll
        for (int a = 0; a < 4; ++a)
#pragma unroll
          for (int u = 0; u < 7 - a; ++u) lg[a][u] = (i0 + a + u < 7) ? -1e9f : lg[a][u];
      }
      constexpr float CS = 0.23570226039551584f * 1.4426950408889634f;
#pragma unroll
      for (int a = 0; a < 4; ++a) {
        float mx = lg[a][0];
#pragma unroll
        for (int u = 1; u < 8; ++u) mx = fmaxf(mx, lg[a][u]);
        const float nb = -mx * CS;
        float sum = 0.f;
#pragma unroll
        for (int u = 0; u < 8; ++u) { lg[a][u] = __builtin_amdgcn_exp2f(fmaf(lg[a][u], CS, nb)); sum += lg[a][u]; }
        const float inv = __builtin_amdgcn_rcpf(sum);
#pragma unroll
        for (int u = 0; u < 8; ++u) lg[a][u] *= inv;
      }
      SB();
      f32x2 o[4][9];
      sfor<11>([&](auto J) {
        constexpr int jj = J;
        if constexpr (jj + 3 < 11) {
#pragma unroll
          for (int c = 0; c < 9; ++c) vv[jj + 3][c] = *(LAS unsigned*)(rp[jj + 3] + 576 + c * 4);
        }
#pragma unroll
        for (int c = 0; c < 9; ++c) {
          const f32x2 v = {bflo(vv[jj][c]), bfhi(vv[jj][c])};
#pragma unroll
          for (int a = 0; a < 4; ++a) {
            if (jj == a) o[a][c] = v * lg[a][0];
            else if (jj > a && jj <= a + 7) o[a][c] = __builtin_elementwise_fma(v, f32x2{lg[a][jj - a], lg[a][jj - a]}, o[a][c]);
          }
        }
        if constexpr (jj >= 7) {
#pragma unroll
          for (int c = 0; c < 9; ++c) *(LAS unsigned*)(rp[jj] + c * 4) = cvtpk(o[jj - 7][c][0], o[jj - 7][c][1]);
        }
        SB();
      });
    }
    STAMPT(5);
    {
      LAS char* rowp = lds + (32 * w + m + 8) * IMG_RS + h * 8;
      const unsigned oso = O_OFF + ((rt * 4 + kg) * 4 + w) * 9216;
#pragma unroll
      for (int s = 0; s < 9; ++s) {
        const u32x2 a = *(LAS u32x2*)(rowp + s * 32), b = *(LAS u32x2*)(rowp + s * 32 + 16);
        __builtin_amdgcn_raw_buffer_store_b128(u32x4{a[0], a[1], b[0], b[1]}, rws, l16, oso + s * 1024, 16);
      }
    }
    WAITV(0); BAR();
    if (tid == 0) __hip_atomic_store(flags + (FL_O + kg) * 16, tag, RLX_AGENT);
    STAMPT(6);
  }
  STAMPOUT();
}

__global__ __launch_bounds__(256, 1) void main_k(const void* __restrict__ x, const void* __restrict__ wqkv, const void* __restrict__ wo,
                                                 const void* __restrict__ w1, const void* __restrict__ b1, const void* __restrict__ w2,
                                                 const char* __restrict__ b2, char* __restrict__ out, void* __restrict__ ws) {
  const void* code;
  asm volatile("s_getpc_b64 %0" : "=s"(code) :: "a255");
  if (map_role(blockIdx.x) == 3) body<true>(x, wqkv, wo, w1, b1, w2, b2, out, ws, code);
  else body<false>(x, wqkv, wo, w1, b1, w2, b2, out, ws, code);
}

extern "C" void kernel_launch(void* const* d_in, const int* in_sizes, int n_in, void* d_out, int out_size, void* d_ws, size_t ws_size, hipStream_t stream) {
  (void)hipFuncSetAttribute((const void*)main_k, hipFuncAttributeMaxDynamicSharedMemorySize, LDS_BYTES);
  main_k<<<256, 256, LDS_BYTES, stream>>>(d_in[0], d_in[1], d_in[2], d_in[4], d_in[5], d_in[6], (const char*)d_in[7], (char*)d_out, d_ws);
}
```

```cpp
#include <hip/hip_runtime.h>
#include <stdint.h>
#include <utility>

typedef __bf16 bf16x8 __attribute__((ext_vector_type(8)));
typedef __bf16 bf16x4 __attribute__((ext_vector_type(4)));
typedef __bf16 bf16x2 __attribute__((ext_vector_type(2)));
typedef float f32x16 __attribute__((ext_vector_type(16)));
typedef float f32x4 __attribute__((ext_vector_type(4)));
typedef float f32x2 __attribute__((ext_vector_type(2)));
typedef unsigned u32x4 __attribute__((ext_vector_type(4)));
typedef unsigned u32x2 __attribute__((ext_vector_type(2)));
typedef unsigned long long u64;
#define LAS __attribute__((address_space(3)))
typedef __attribute__((address_space(1))) u64 gu64;
#define DI __device__ __forceinline__
#define SB() __builtin_amdgcn_sched_barrier(0)
#define IC(n) std::integral_constant<int, (n)>{}
#define RLX_AGENT __ATOMIC_RELAXED, __HIP_MEMORY_SCOPE_AGENT

template <class F, int... I> DI void sfor_(F&& f, std::integer_sequence<int, I...>) { (f(std::integral_constant<int, I>{}), ...); }
template <int N, class F> DI void sfor(F&& f) { sfor_(f, std::make_integer_sequence<int, N>{}); }

constexpr int S_ = 2048, D_ = 576, F_ = 4608;
constexpr int SLOT = 24576;
constexpr int TAB_OFF = 147456;
constexpr int LDS_BYTES = 163840;
constexpr int QK_OFF = 80512, QK_RS = 584, V_RS = 592;
constexpr unsigned H_OFF = 0, O_OFF = 20u << 20, P_OFF = 24u << 20, FLAG_OFF = 40u << 20, WS_BYTES = 41u << 20;
constexpr unsigned OOB = 0x40000000u;
constexpr int FL_HA = 0, FL_HB = 12, FL_O = 24, FL_FIN = 28, FL_EPOCH = 40, FL_N = 41;
constexpr unsigned CODE_BYTES = 73728;
constexpr int A_H = 56;
constexpr int A_Q = 16;
constexpr int A_V = 92;
constexpr int A_HH = 164;
constexpr int A_HX = 184;

__host__ DI int map_role(int bid) { return (bid >> 3) & 3; }
__host__ DI int map_kg(int bid) { return bid & 3; }
__host__ DI int map_rt(int bid) { return ((bid >> 2) & 1) * 8 + (bid >> 5); }
__host__ DI int map_sp(int bid) { return map_role(bid) == 3 ? 12 + map_kg(bid) : 3 * map_kg(bid) + map_role(bid); }

#define GAUX ""
#define XAUX ""
#define WAUX ""
DI u32x4 make_srd(const void* p, unsigned bytes) {
  uint64_t a = (uint64_t)p;
  u32x4 r; r[0] = (unsigned)a; r[1] = (unsigned)(a >> 32) & 0xffffu; r[2] = bytes; r[3] = 0x00020000u;
  return r;
}
DI void glds16(unsigned m0v, unsigned voff, u32x4 srd, unsigned soff) {
  asm volatile("s_mov_b32 m0, %0\n\ts_nop 0\n\tbuffer_load_dwordx4 %1, %2, %3 offen lds" :: "s"(m0v), "v"(voff), "s"(srd), "s"(soff) : "memory");
}
template <unsigned MC, unsigned SC> DI void glds16c(unsigned mb, unsigned voff, u32x4 srd, unsigned sb) {
  unsigned tmp;
  asm volatile("s_add_u32 m0, %1, %5\n\ts_add_u32 %0, %4, %6\n\tbuffer_load_dwordx4 %2, %3, %0 offen" GAUX " lds"
               : "=&s"(tmp) : "s"(mb), "v"(voff), "s"(srd), "s"(sb), "i"(MC), "i"(SC) : "memory", "scc");
}
template <unsigned MC> DI void glds16r(unsigned mb, unsigned voff, u32x4 srd, unsigned& so, unsigned step) {
  asm volatile("s_add_u32 m0, %1, %5\n\ts_add_u32 %0, %0, %4\n\tbuffer_load_dwordx4 %2, %3, %0 offen lds"
               : "+s"(so) : "s"(mb), "v"(voff), "s"(srd), "s"(step), "i"(MC) : "memory", "scc");
}
constexpr int x_base(int t) { return (t / 3 == 1 ? 2 : 4) * SLOT + (t % 3 == 2 ? SLOT : 0); }
template <int T> DI void x_line(unsigned m6, unsigned vx0, unsigned vx1, u32x4 srd, unsigned s0, unsigned s1, unsigned s2, unsigned s3) {
  constexpr int d0 = x_base(T) + (T % 3 == 1 ? 4096 : 0) - (T % 3) * 128, d2 = T % 3 == 1 ? SLOT - 1024 : 1024;
  asm volatile("s_add_u32 m0, %0, %8\n\ts_nop 0\n\tbuffer_load_dwordx4 %1, %3, %4 offen offset:%c10" XAUX " lds\n\t"
               "s_addk_i32 m0, 0x400\n\ts_nop 0\n\tbuffer_load_dwordx4 %2, %3, %5 offen offset:%c10" XAUX " lds\n\t"
               "s_addk_i32 m0, %c9\n\ts_nop 0\n\tbuffer_load_dwordx4 %1, %3, %6 offen offset:%c10" XAUX " lds\n\t"
               "s_addk_i32 m0, 0x400\n\ts_nop 0\n\tbuffer_load_dwordx4 %2, %3, %7 offen offset:%c10" XAUX " lds"
               :: "s"(m6), "v"(vx0), "v"(vx1), "s"(srd), "s"(s0), "s"(s1), "s"(s2), "s"(s3), "i"(d0), "i"(d2), "i"((T % 3) * 128) : "memory", "scc");
}
DI u32x4 bload16(unsigned voff, u32x4 srd, unsigned soff) {
  u32x4 r;
  asm volatile("buffer_load_dwordx4 %0, %1, %2, %3 offen" : "=v"(r) : "v"(voff), "s"(srd), "s"(soff) : "memory");
  return r;
}
DI unsigned bload4(unsigned voff, u32x4 srd, unsigned soff) {
  unsigned r;
  asm volatile("buffer_load_dword %0, %1, %2, %3 offen" : "=v"(r) : "v"(voff), "s"(srd), "s"(soff) : "memory");
  return r;
}
DI u32x2 bload8(unsigned voff, u32x4 srd, unsigned soff) {
  u32x2 r;
  asm volatile("buffer_load_dwordx2 %0, %1, %2, %3 offen" : "=v"(r) : "v"(voff), "s"(srd), "s"(soff) : "memory");
  return r;
}
DI void warm(unsigned voff, u32x4 srd, unsigned soff) {
  asm volatile("buffer_load_dword a11, %0, %1, %2 offen" WAUX :: "v"(voff), "s"(srd), "s"(soff) : "memory");
}
DI u64 bload8_sc1(unsigned voff, u32x4 srd, unsigned soff) {
  u64 r;
  asm volatile("buffer_load_dwordx2 %0, %1, %2, %3 offen sc1" : "=v"(r) : "v"(voff), "s"(srd), "s"(soff) : "memory");
  return r;
}
template <int AR, int OFF> DI void aload16_sc1(unsigned voff, u32x4 srd, unsigned soff) {
  asm volatile("buffer_load_dwordx4 a[%c3:%c4], %0, %1, %2 offen offset:%c5 sc1" :: "v"(voff), "s"(srd), "s"(soff), "i"(AR), "i"(AR + 3), "i"(OFF) : "memory");
}
template <int AR, int OFF> DI void aload16(unsigned voff, u32x4 srd) {
  asm volatile("buffer_load_dwordx4 a[%c2:%c3], %0, %1, 0 offen offset:%c4" :: "v"(voff), "s"(srd), "i"(AR), "i"(AR + 3), "i"(OFF) : "memory");
}
template <int N> DI void waitv() { asm volatile("s_waitcnt vmcnt(%c0)" :: "i"(N > 63 ? 63 : N) : "memory"); }
#define WAITV(n) asm volatile("s_waitcnt vmcnt(" #n ")" ::: "memory")
#define BAR() asm volatile("s_barrier" ::: "memory")
#define WAITL0() asm volatile("s_waitcnt lgkmcnt(0)" ::: "memory")
#define PIN(x) asm volatile("" : "+v"(x))
#define PINS(x) asm volatile("" : "+s"(x))

DI unsigned cvtpk(float lo, float hi) { f32x2 v = {lo, hi}; bf16x2 b = __builtin_convertvector(v, bf16x2); return __builtin_bit_cast(unsigned, b); }
DI float bflo(unsigned u) { return __uint_as_float(u << 16); }
DI float bfhi(unsigned u) { return __uint_as_float(u & 0xffff0000u); }
DI float dot2(unsigned a, unsigned b, float c) { return __builtin_amdgcn_fdot2_f32_bf16(__builtin_bit_cast(bf16x2, a), __builtin_bit_cast(bf16x2, b), c, false); }
DI float xor32(float v) { auto r = __builtin_amdgcn_permlane32_swap(__float_as_uint(v), __float_as_uint(v), false, false); return __uint_as_float(r[0]) + __uint_as_float(r[1]); }
DI float xor16(float v) { auto r = __builtin_amdgcn_permlane16_swap(__float_as_uint(v), __float_as_uint(v), false, false); return __uint_as_float(r[0]) + __uint_as_float(r[1]); }
DI float fmul_s(float a, float b) { float r; asm("v_mul_f32_e32 %0, %1, %2" : "=v"(r) : "v"(a), "v"(b)); return r; }

template <int UOFF> DI bf16x8 rdfrag(LAS char* p) {
  bf16x4 a = __builtin_amdgcn_ds_read_tr16_b64_v4bf16((LAS bf16x4*)p);
  bf16x4 b = __builtin_amdgcn_ds_read_tr16_b64_v4bf16((LAS bf16x4*)(p + UOFF));
  return __builtin_shufflevector(a, b, 0, 1, 2, 3, 4, 5, 6, 7);
}
template <bool FIRST, class TA> DI void mfma_vv(f32x16& acc, TA a, u32x4 b) {
  if constexpr (FIRST) asm volatile("v_mfma_f32_32x32x16_bf16 %0, %1, %2, 0" : "=&v"(acc) : "v"(a), "v"(b));
  else                 asm volatile("v_mfma_f32_32x32x16_bf16 %0, %1, %2, %0" : "+v"(acc) : "v"(a), "v"(b));
}
DI void mfma_vvc(f32x16& acc, u32x4 a, u32x4 b, const f32x16& c) { asm volatile("v_mfma_f32_32x32x16_bf16 %0, %1, %2, %3" : "=&v"(acc) : "v"(a), "v"(b), "v"(c)); }
template <bool FIRST, int AR> DI void mfma_va(f32x16& acc, bf16x8 a) {
  if constexpr (FIRST) asm volatile("v_mfma_f32_32x32x16_bf16 %0, %1, a[%c2:%c3], 0" : "=&v"(acc) : "v"(a), "i"(AR), "i"(AR + 3));
  else                 asm volatile("v_mfma_f32_32x32x16_bf16 %0, %1, a[%c2:%c3], %0" : "+v"(acc) : "v"(a), "i"(AR), "i"(AR + 3));
}
template <bool FIRST, int AR> DI void mfma16_va(f32x4& acc, bf16x8 a) {
  if constexpr (FIRST) asm volatile("v_mfma_f32_16x16x32_bf16 %0, %1, a[%c2:%c3], 0" : "=&v"(acc) : "v"(a), "i"(AR), "i"(AR + 3));
  else                 asm volatile("v_mfma_f32_16x16x32_bf16 %0, %1, a[%c2:%c3], %0" : "+v"(acc) : "v"(a), "i"(AR), "i"(AR + 3));
}
template <bool FIRST, int AR> DI void mfma16_aa(f32x4& acc) {
  if constexpr (FIRST) asm volatile("v_mfma_f32_16x16x32_bf16 %0, a[%c1:%c2], a[%c1:%c2], 0" : "=&v"(acc) : "i"(AR), "i"(AR + 3));
  else                 asm volatile("v_mfma_f32_16x16x32_bf16 %0, a[%c1:%c2], a[%c1:%c2], %0" : "+v"(acc) : "i"(AR), "i"(AR + 3));
}
template <bool FIRST, int AD, int AR> DI void mfma16_ava(bf16x8 a) {
  if constexpr (FIRST) asm volatile("v_mfma_f32_16x16x32_bf16 a[%c1:%c2], %0, a[%c3:%c4], 0" :: "v"(a), "i"(AD), "i"(AD + 3), "i"(AR), "i"(AR + 3));
  else                 asm volatile("v_mfma_f32_16x16x32_bf16 a[%c1:%c2], %0, a[%c3:%c4], a[%c1:%c2]" :: "v"(a), "i"(AD), "i"(AD + 3), "i"(AR), "i"(AR + 3));
}
template <int AR> DI float aread(void) { float r; asm volatile("v_accvgpr_read_b32 %0, a[%c1]" : "=v"(r) : "i"(AR)); return r; }
template <int AR> DI void awrite2(float t0, float t1) { asm volatile("v_accvgpr_write_b32 a[%c2], %0\n\tv_accvgpr_write_b32 a[%c3], %1" :: "v"(t0), "v"(t1), "i"(AR), "i"(AR + 1)); }
template <int AR, int OFF> DI void ds_write_a128(unsigned addr) { asm volatile("ds_write_b128 %0, a[%c1:%c2] offset:%c3" :: "v"(addr), "i"(AR), "i"(AR + 3), "i"(OFF) : "memory"); }
DI float ror8(float v) { return __int_as_float(__builtin_amdgcn_update_dpp(0, __float_as_int(v), 0x128, 0xf, 0xf, false)); }
template <int AR> DI void awrite(unsigned v) { asm volatile("v_accvgpr_write_b32 a[%c1], %0" :: "v"(v), "i"(AR)); }
DI void hp_a(float& t0, float& t1, float a0, float a1, float r, float b0, float b1) {
  asm volatile("v_fma_f32 %0, %2, %4, %5\n\tv_fma_f32 %1, %3, %4, %6\n\tv_max_f32_e32 %0, 0, %0" : "=&v"(t0), "=&v"(t1) : "v"(a0), "v"(a1), "v"(r), "v"(b0), "v"(b1));
}
DI void hp_b(float& t0, float& t1) { asm volatile("v_max_f32_e32 %1, 0, %1\n\tv_mul_f32_e32 %0, %0, %0\n\tv_mul_f32_e32 %1, %1, %1" : "+v"(t0), "+v"(t1)); }
template <int AR> DI unsigned hp_c(float t0, float t1) {
  unsigned e;
  asm volatile("v_cvt_pk_bf16_f32 %0, %1, %2\n\tv_accvgpr_write_b32 a[%c3], %0" : "=v"(e) : "v"(t0), "v"(t1), "i"(AR));
  return e;
}
DI void sp_a(float& t0, float& t1, float a0, float a1, float r) { asm volatile("v_mul_f32_e32 %0, %2, %4\n\tv_mul_f32_e32 %1, %3, %4" : "=&v"(t0), "=&v"(t1) : "v"(a0), "v"(a1), "v"(r)); }
template <int AR> DI void spair(float a0, float a1, float r) { float t0, t1; sp_a(t0, t1, a0, a1, r); (void)hp_c<AR>(t0, t1); }
template <int AR, int OFF> DI void ds_read_a128(unsigned addr) { asm volatile("ds_read_b128 a[%c1:%c2], %0 offset:%c3" :: "v"(addr), "i"(AR), "i"(AR + 3), "i"(OFF) : "memory"); }
DI void bstore8_sc1(u64 v, unsigned voff, u32x4 srd, unsigned soff) { asm volatile("buffer_store_dwordx2 %0, %1, %2, %3 offen sc1" :: "v"(v), "v"(voff), "s"(srd), "s"(soff) : "memory"); }
template <int AR, int OFF> DI void ds_write_a64(unsigned addr) { asm volatile("ds_write_b64 %0, a[%c1:%c2] offset:%c3" :: "v"(addr), "i"(AR), "i"(AR + 1), "i"(OFF) : "memory"); }

#define STAMPT(i)
#define STAMPOUT()
#define STAGET(n)
#define STAGEOUT()

#define WS0 0
#define WSP 48
#define WPRO 1
template <bool ATTN> struct Sched {
  static constexpr int P1 = ATTN ? 504 : 432, NSTEP = ATTN ? 504 : 918, NSTAGE = ATTN ? 21 : 39;
  static constexpr int a_stage(int s) { return s < P1 ? 3 * (s / 72) + (s % 36) / 12 : s < 864 ? s / 24 : 36 + (s - 864) / 18; }
  static constexpr int a_tile(int s) { return s < P1 ? (s % 72) / 36 : s < 864 ? (s % 24) / 12 : ((s - 864) % 18) / 9; }
  static constexpr int a_ks(int s) { return s < 864 ? s % 12 : (s - 864) % 9; }
  static constexpr int g_stage(int s) {
    if (s < 0) return (s + 24) / 6;
    if (s >= 864 || s % 4 != 1) return -1;
    const int st = s / 24 + 4;
    return st >= NSTAGE || (st >= 36 && (s % 24) / 4 == 5) ? -1 : st;
  }
  static constexpr int g_piece(int s) { return (s % 24) / 4; }
  static constexpr int g_last(int st) { int r = -25; for (int s = -24; s < NSTEP; ++s) if (g_stage(s) == st) r = s; return r; }
  static constexpr int warm_k(int s) { return s >= WS0 && (s - WS0) % WSP == 0 && (s - WS0) / WSP < 5 ? (s - WS0) / WSP : -1; }
  static constexpr int PUB_HA = 306, PUB_HB = 528, POLL_HA = 380, CHK_HA = 428, POLL_HB = 580, CHK_HB = 624, POLL_O = 792, CHK_O = 828;
  static constexpr int h_store(int s) { return !ATTN && s >= 36 && s < 468 ? (s % 36 == 20 ? 0 : s % 36 == 35 ? 1 : -1) : -1; }
  static constexpr int h_load(int s) {
    if (ATTN || s % 4 != 3) return -1;
    return s >= 432 && s < 528 ? (s - 432) / 4 : s >= 624 && s < 720 ? 24 + (s - 624) / 4 : -1;
  }
  static constexpr int o_load(int s) { return !ATTN && s > CHK_O && s <= CHK_O + 18 && s % 2 == 0 ? (s - CHK_O) / 2 - 1 : -1; }
  static constexpr int p_store(int s) { return !ATTN && s >= 873 ? ((s - 864) % 9 == 5 ? 0 : (s - 864) % 9 == 7 ? 1 : -1) : -1; }
  static constexpr int x_load(int s) { return !ATTN && s >= 866 && s < 890 && s % 4 == 2 ? (s - 866) / 4 : -1; }
  static constexpr int vm(int s) {
    int c = g_stage(s) >= 0 && g_stage(s) < 36;
    if (!ATTN && s >= 0) c += (h_store(s) >= 0) + (h_load(s) >= 0) + (o_load(s) >= 0) + (p_store(s) >= 0) + (s == POLL_HA) + (s == POLL_HB) + (s == POLL_O) + (x_load(s) >= 0);
    return c;
  }
  static constexpr bool bar(int s) { return s < P1 ? ((s % 72 == 0 || s % 72 == 18 || s % 72 == 48)) : (s <= 864 && s % 24 == 0); }
  static constexpr int need(int s) {
    int r = -25;
    if (bar(s) && !(ATTN && s == 480)) r = g_last(s < P1 ? 3 * (s / 72) + (s % 72 == 0 ? 1 : s % 72 == 18 ? 2 : 3) : s < 864 ? s / 24 + 1 : 38);
    if (!ATTN) {
      const int e = s == PUB_HA ? 251 : s == PUB_HB ? 467 : s == CHK_HA ? POLL_HA : s == CHK_HB ? POLL_HB : s == CHK_O ? POLL_O : s == 720 ? 671 : s == 768 ? 719 : s == 863 ? 862 : -25;
      if (e > r) r = e;
    }
    return r;
  }
  static constexpr int allow(int s) { int c = 0; for (int k = need(s) + 1; k < s; ++k) c += vm(k); return c; }
};
constexpr int kb_areg(int kb) { return kb == 0 ? A_H : kb == 1 ? A_H + 48 : kb == 2 ? A_H + 96 : kb == 3 ? A_H + 144 : kb == 4 ? A_H : kb == 5 ? A_H + 48 : A_H + 96; }
constexpr int qkv_pair(int kg, int p) {
  constexpr signed char T[4][28] = {
    {0,2,4,6, 8,10,12,14, 18,20,22,24, 26,28,30,32, 16,34,-1,52, 36,38,40,42, 44,46,48,50},
    {6,8,10,12, 0,2,4,-1, 24,26,28,30, 14,16,32,34, 18,20,22,50, 42,44,46,48, 36,38,40,52},
    {4,6,8,10, 12,14,16,-1, 22,24,26,28, 0,2,18,20, 30,32,34,36, 40,42,44,46, 48,50,52,38},
    {2,4,6,8, 10,12,14,16, 20,22,24,26, 28,30,32,34, 0,18,-1,36, 38,40,42,44, 46,48,50,52}};
  return T[kg][p];
}
struct QkvTab { unsigned v[4][16]; };
constexpr QkvTab make_tab() {
  QkvTab t = {};
  for (int kg = 0; kg < 4; ++kg)
    for (int p = 0; p < 28; ++p) {
      const int v = qkv_pair(kg, p), sv = v < 0 ? qkv_pair(kg, p - 1) : v;
      t.v[kg][p / 4] |= (unsigned)((sv / 18) * 72 + sv % 18) << (8 * (p % 4));
      t.v[kg][8 + p / 4] |= (unsigned)(sv >= 36 ? sv - 36 : sv) << (8 * (p % 4));
    }
  return t;
}

DI void spin_until(gu64* p, u64 tag) {
  for (unsigned spins = 0; spins < (1u << 18); ++spins) {
    const u64 f = __hip_atomic_load(p, RLX_AGENT);
    if (__all(f == tag)) break;
    __builtin_amdgcn_s_sleep(1);
  }
}

__global__ __launch_bounds__(256, 1) void main_k(const void* __restrict__ x, const void* __restrict__ wqkv, const void* __restrict__ wo,
                                                 const void* __restrict__ w1, const void* __restrict__ b1, const void* __restrict__ w2,
                                                 const char* __restrict__ b2, char* __restrict__ out, void* __restrict__ ws, const QkvTab qt) {
  extern __shared__ __attribute__((aligned(16))) char smem[];
  LAS char* lds = (LAS char*)smem;
  const void* code;
  asm volatile("s_getpc_b64 %0" : "=s"(code) :: "a255");
  STAMPT(0);
  const int tid = threadIdx.x, lane = tid & 63, bid = blockIdx.x;
  const int w = __builtin_amdgcn_readfirstlane(tid >> 6);
  const int kg = map_kg(bid), cg = map_role(bid), rt = map_rt(bid);
  const bool attn = cg == 3;
  const int tokbase = 256 * (rt >> 1) + (rt & 1);
  const int m = lane & 31, h = lane >> 5, r8 = lane >> 3, pos = lane & 7;
  const int G = lane >> 4, q4 = (lane >> 2) & 3, p4 = lane & 3;

  const u32x4 srdx = make_srd(x, S_ * D_ * 2);
  const unsigned rot = (0x24924924u >> (2 * ((rt & 7) + cg))) & 3;
  unsigned kro[3] = {rot * 192, rot == 2 ? 0 : rot * 192 + 192, rot == 0 ? 384 : rot * 192 - 192};
  unsigned m6 = 6 * w * 1024, sx = tokbase * 1152 + w * 32 * 2304;
  unsigned vx0 = r8 * 2304 + ((pos ^ G) << 4), vx1 = vx0 ^ 64;
  PINS(m6); PINS(sx); PIN(vx0); PIN(vx1); PINS(kro[0]); PINS(kro[1]); PINS(kro[2]);
  auto x_more = [&](auto T) { const unsigned b = sx + kro[T / 3] * 2; x_line<T>(m6, vx0, vx1, srdx, b, b + 8 * 2304, b + 16 * 2304, b + 24 * 2304); };
  sfor<3>(x_more);

  u32x4 srdA = make_srd(attn ? wqkv : w1, attn ? D_ * 3 * D_ * 2 : D_ * F_ * 2);
  PINS(srdA);
  const unsigned swz = pos ^ (((r8 >> 1) & 1) << 2);
  const int fs = 3 * kg + cg, f0 = fs * 384;
  const unsigned strideA = attn ? 3456u : 9216u;
  unsigned soA[3];
#pragma unroll
  for (int j = 0; j < 3; ++j) { soA[j] = (kro[j] + 48 * w) * strideA + (attn ? 288 * kg : f0 * 2); PINS(soA[j]); }
  unsigned qs[7], qd[7];
#pragma unroll
  for (int i = 0; i < 7; ++i) { qs[i] = qt.v[kg][i]; qd[i] = qt.v[kg][8 + i]; PINS(qs[i]); PINS(qd[i]); }
  const unsigned vab = r8 * strideA + (swz & 1) * 16, vsh = 8 * (swz >> 1);
  auto va_of = [&](unsigned word) { return vab + ((word >> vsh) & 255) * 16; };
  unsigned va = va_of(attn ? qs[0] : 0x06040200u), so = soA[0] - 8 * strideA;
  auto w_piece = [&](auto K) {
    constexpr int n = K / 6, p = K % 6;
    if constexpr (p == 0 && n > 0) so = soA[n % 3] - 8 * strideA;
    if constexpr (K == 18) va = va_of(attn ? qs[1] : 0x0e0c0a08u);
    glds16r<n * SLOT + p * 1024>(m6, va, srdA, so, 8 * strideA);
  };
  sfor<6>(w_piece);
  warm(tid < 64 ? tid * 128 : OOB, make_srd(code, CODE_BYTES), ((bid >> 3) & 15) * 8192);
  const u32x4 srdws = make_srd(ws, WS_BYTES);
  const unsigned flo = FLAG_OFF + rt * FL_N * 128;
  auto misc_loads = [&]() {
    asm volatile("buffer_load_dwordx2 a[12:13], %0, %1, %2 offen sc1" :: "v"(0), "s"(srdws), "s"(flo + FL_EPOCH * 128) : "memory");
    if (attn) {
      int ht = tokbase + 2 * (r8 - 8); ht = ht < 0 ? 0 : ht;
      const unsigned vh = ht * 1152 + pos * 16;
      const unsigned kb = ((0x24924924u >> (2 * (rot + w))) & 3) * 384 | (w == 3 ? OOB : 0);
      glds16c<TAB_OFF, 0>(w * 3072, vh, srdx, kb); glds16c<TAB_OFF + 1024, 128>(w * 3072, vh, srdx, kb); glds16c<TAB_OFF + 2048, 256>(w * 3072, vh, srdx, kb);
    } else {
      asm volatile("buffer_load_dword a14, %0, %1, 0 offen" :: "v"(tid * 4), "s"(make_srd((const char*)b1 + f0 * 2, 768)) : "memory");
      asm volatile("buffer_load_dword a15, %0, %1, 0 offen" :: "v"(tid * 4), "s"(make_srd(b2 + (192 * cg + 48 * kg) * 2, 96)) : "memory");
    }
  };
  u32x4 id0, id1;
#pragma unroll
  for (int j = 0; j < 4; ++j) {
    const int k0 = 8 * h + 2 * j;
    id0[j] = (m == k0 ? 0x3f80u : 0u) | (m == k0 + 1 ? 0x3f800000u : 0u);
    id1[j] = (m == k0 + 16 ? 0x3f80u : 0u) | (m == k0 + 17 ? 0x3f800000u : 0u);
  }
  PIN(id0); PIN(id1);
  const unsigned q1 = (q4 >> 1) & 1;
  LAS char* rb[2][3];
#pragma unroll
  for (int i = 0; i < 3; ++i) {
    const unsigned po0 = (4 * h + q4) * 128 + q1 * 64 + (G & 1) * 32 + p4 * 8;
    rb[0][i] = lds + po0 + 2 * i * SLOT; rb[1][i] = lds + (po0 ^ 64) + 2 * i * SLOT; PIN(rb[0][i]); PIN(rb[1][i]);
  }
  u32x4 xf[36];
  f32x16 acc0, acc1, nm;
  float rstd;
  {
    LAS char* xp[2][4];
#pragma unroll
    for (int u = 0; u < 4; ++u) {
      const unsigned xr0 = 2 * SLOT + w * 6144 + (m & 7) * 128 + (((h ^ ((m >> 1) & 7)) << 4) ^ (u << 5));
      xp[0][u] = lds + xr0 + (m >> 3) * 1024; xp[1][u] = lds + xr0 + 4096 + (m >> 4) * SLOT + ((m >> 3) & 1) * 1024; PIN(xp[0][u]); PIN(xp[1][u]);
    }
    float q0 = 0.f, q1 = 0.f;
    u32x4 ones = {0x3f803f80u, 0x3f803f80u, 0x3f803f80u, 0x3f803f80u}; PIN(ones);
    SB();
    sfor<10>([&](auto T) {
      constexpr int t = T;
      if constexpr (t == 0) x_more(IC(3));
      if constexpr (t + 4 < 9) x_more(IC(t + 4));
      if constexpr (t == 4) misc_loads();
      if constexpr (t < 9) {
        waitv<4 * ((t + 4 > 8 ? 8 : t + 4) - t) + (t < 3 ? 6 : 0) + (t > 9 - WPRO ? 6 * (t - 9 + WPRO) : 0)>();
#pragma unroll
        for (int u = 0; u < 4; ++u) xf[4 * t + u] = *(LAS u32x4*)(xp[t % 3 == 1][u] + x_base(t) - 2 * SLOT);
      }
      SB();
      if constexpr (t > 0) {
        sfor<4>([&](auto U) {
          const u32x4 v = xf[4 * t - 4 + U];
          mfma_vv<t == 1 && U == 0>(nm, ones, v);
          q0 = dot2(v[0], v[0], q0); q1 = dot2(v[1], v[1], q1); q0 = dot2(v[2], v[2], q0); q1 = dot2(v[3], v[3], q1);
          PIN(q0); PIN(q1);
        });
      }
      if constexpr (t >= 9 - WPRO && t < 9) sfor<6>([&](auto P) { w_piece(IC(6 + 6 * (t - 9 + WPRO) + P)); });
      SB();
    });
    const float sq = xor32(q0 + q1) * (1.0f / D_);
    asm volatile("s_nop 7" : "+v"(nm));
#pragma unroll
    for (int i = 0; i < 16; ++i) nm[i] = fmul_s(nm[i], -1.0f / D_);
    rstd = __builtin_amdgcn_rsqf(fmaxf(sq - nm[0] * nm[0], 0.f) + 1e-5f);
  }
  STAMPT(1);
  asm volatile("s_nop 1" : "+v"(nm));
  sfor<19>([&](auto B) {
    constexpr int b = B;
    if constexpr (b < 18) {
      f32x16& a = (b & 1) ? acc1 : acc0;
      mfma_vvc(a, id0, xf[2 * b], nm);
      if constexpr (6 + 6 * WPRO + b < 24) w_piece(IC(6 + 6 * WPRO + b));
      mfma_vv<false>(a, id1, xf[2 * b + 1]);
    } else asm volatile("s_waitcnt vmcnt(18)\n\ts_barrier\n\ts_nop 9" : "+v"(acc1) :: "memory");
    SB();
    if constexpr (b > 0) {
      const f32x16& a = (b & 1) ? acc0 : acc1;
#pragma unroll
      for (int d = 0; d < 8; ++d) xf[2 * b - 2 + (d >> 2)][d & 3] = cvtpk(a[2 * d], a[2 * d + 1]);
      PIN(xf[2 * b - 2]); PIN(xf[2 * b - 1]);
    }
    SB();
  });
  bf16x8 wf[6];
#pragma unroll
  for (int t = 0; t < 5; ++t) wf[t] = rdfrag<1024>(rb[0][0] + t * 2048);
  STAMPT(2);

  const u32x4 srdW2 = make_srd(w2, F_ * D_ * 2), srdWo = make_srd(wo, D_ * D_ * 2);
  const __amdgpu_buffer_rsrc_t rws = __builtin_amdgcn_make_buffer_rsrc(ws, 0, WS_BYTES, 0x00020000);
  gu64* flags = (gu64*)((char*)ws + flo);
  auto get_tag = [&]() { return (((u64)__float_as_uint(aread<13>()) << 32) | __float_as_uint(aread<12>())) * 0x9E3779B97F4A7C15ull + 0x632BE59BD9B4E019ull; };
  float t0, t1;
#define LANE_VALUES int tid_ = threadIdx.x; PIN(tid_); \
  const int tid = tid_, lane = tid & 63, m = lane & 31, h = lane >> 5, r8 = lane >> 3, pos = lane & 7, G = lane >> 4, q4 = (lane >> 2) & 3, p4 = lane & 3; \
  const unsigned swz = pos ^ (((r8 >> 1) & 1) << 2), q1 = (q4 >> 1) & 1, l16 = lane * 16, vflag = tid == 0 ? 0u : OOB, wa = rt * 72 + w * 18 + lane, wm = lane < 18 ? 0 : OOB; \
  (void)m; (void)h; (void)G; (void)p4; (void)swz; (void)q1;

  if (!attn) {
    using SC = Sched<false>;
    LANE_VALUES
    auto rd_a = [&](auto Sc) {
      constexpr int s = Sc;
      if constexpr (s < SC::NSTEP) { constexpr int sl = SC::a_stage(s) % 6; wf[s % 6] = rdfrag<1024>(rb[SC::a_tile(s)][sl >> 1] + (sl & 1) * SLOT + SC::a_ks(s) * 2048); }
    };
    auto sync = [&](auto Sc) {
      constexpr int s = Sc, k = SC::warm_k(s);
      if constexpr (SC::need(s) > -25) waitv<SC::allow(s)>();
      if constexpr (s == 18) WAITL0();
      if constexpr (SC::bar(s)) BAR();
      if constexpr (k >= 0 && k < 2) warm(((wa >> 1) * 9216 + (wa & 1) * 128) | wm, srdA, f0 * 2 + 256 + 256 * k);
      if constexpr (k >= 2) warm((wa * 128) | wm, srdW2, f0 * 1152 + (k - 2) * 147456);
    };
    va -= 128;
    u64 tag;
    const unsigned voffB = r8 * 1152 + swz * 16;
    const int c1 = cg == 2 ? 0 : cg + 1, c2 = cg == 0 ? 2 : cg - 1;
    unsigned soB[3];
    soB[0] = (f0 + 48 * w) * 1152 + 384 * cg; soB[1] = ((3 * kg + c1) * 384 + 48 * w) * 1152 + 384 * cg; soB[2] = ((3 * kg + c2) * 384 + 48 * w) * 1152 + 384 * cg;
    unsigned soO = (144 * kg + 40 * w) * 1152 + 384 * cg, m5 = 5 * w * 1024;
    unsigned hso[3];
    hso[0] = H_OFF + ((rt * 12 + fs) * 4 + w) * 24576; hso[1] = H_OFF + ((rt * 12 + 3 * kg + c1) * 4 + w) * 24576; hso[2] = H_OFF + ((rt * 12 + 3 * kg + c2) * 4 + w) * 24576;
    const unsigned oso = O_OFF + ((rt * 4 + kg) * 4 + w) * 9216;
    const unsigned pso = P_OFF + (((rt * 3 + cg) * 4 + kg) * 4 + w) * 12288;
    const unsigned vpoll = (3 * kg + ((lane & 1) ? c1 : c2)) * 128;
    LAS char* tab = lds + TAB_OFF + h * 16;
    f32x4 bs[2];
    f32x16 ac[6];
    unsigned e[8];
    u64 pollv = 0;
    const unsigned tok = tokbase + 2 * (32 * w + m), dB0 = (192 * cg + 48 * kg + 4 * h) * 2;
    u32x2 xr[3][2];
    auto epi2 = [&](auto U, auto Tc) {
      constexpr int u = U, T = Tc;
      if constexpr (u >= 3 && u < 7) {
        constexpr int d = 2 * (u - 3);
        e[d] = cvtpk(ac[T][2 * d], ac[T][2 * d + 1]); e[d + 1] = cvtpk(ac[T][2 * d + 2], ac[T][2 * d + 3]);
        PIN(e[d]); PIN(e[d + 1]);
      }
      if constexpr (u == 5) __builtin_amdgcn_raw_buffer_store_b128(u32x4{e[0], e[1], e[2], e[3]}, rws, l16 + (T & 1) * 2048, pso + (T >> 1) * 4096, 16);
      if constexpr (u == 7) __builtin_amdgcn_raw_buffer_store_b128(u32x4{e[4], e[5], e[6], e[7]}, rws, l16 + (T & 1) * 2048 + 1024, pso + (T >> 1) * 4096, 16);
    };
    sfor<51>([&](auto So) { sfor<18>([&](auto Si) {
      constexpr int s = 18 * So + Si;
      if constexpr (s == 432) { STAMPT(3); }
      if constexpr (s < 432) { if constexpr (s % 72 < 36) mfma_vv<s % 36 == 0>(acc0, wf[s % 6], xf[s % 36]); else mfma_vv<s % 36 == 0>(acc1, wf[s % 6], xf[s % 36]); }
      else {
        constexpr int n = SC::a_stage(s) - 18, kb = n / 3, T = 2 * (n % 3) + SC::a_tile(s);
        mfma_va<kb == 0 && SC::a_ks(s) == 0, kb_areg(kb) + 4 * SC::a_ks(s)>(ac[T], wf[s % 6]);
      }
      rd_a(IC(s + 5));
      sync(IC(s));
      if constexpr (s % 24 == 0) STAGET(s / 24);
      if constexpr (s == SC::PUB_HA || s == SC::PUB_HB) bstore8_sc1(tag, vflag, srdws, flo + ((s == SC::PUB_HA ? FL_HA : FL_HB) + fs) * 128);
      if constexpr (s == SC::CHK_HA || s == SC::CHK_HB || s == SC::CHK_O) {
        PIN(pollv);
        if (__builtin_expect(!__all(pollv == tag), 0))
          spin_until((gu64*)((char*)flags + (s == SC::CHK_O ? (FL_O + kg) * 128 : (s == SC::CHK_HA ? FL_HA : FL_HB) * 128 + vpoll)), tag);
      }
      if constexpr (s == 6) { const unsigned r = __float_as_uint(aread<14>()); *(LAS f32x2*)(lds + TAB_OFF + tid * 8) = f32x2{bflo(r), bfhi(r)}; }
      if constexpr (s == 10) { const unsigned r = __float_as_uint(aread<15>()); *(LAS f32x2*)(lds + TAB_OFF + 2048 + tid * 8) = f32x2{bflo(r), bfhi(r)}; }
      if constexpr (s == 14) { tag = get_tag(); PIN(tag); }
      if constexpr (SC::g_stage(s) >= 0) {
        constexpr int np = SC::g_stage(s), p = SC::g_piece(s), sl = np % 6;
        if constexpr (np < 18) glds16c<sl * SLOT + p * 1024, 8 * p * 9216 + 128 * (np / 3)>(m6, va, srdA, soA[np % 3]);
        else if constexpr (np < 36) {
          constexpr int kb = (np - 18) / 3, src = kb < 2 ? 0 : 1 + (kb & 1), half = kb < 2 ? kb : (kb >= 4);
          glds16c<sl * SLOT + p * 1024, (192 * half + 8 * p) * 1152 + 128 * ((np - 18) % 3)>(m6, voffB, srdW2, soB[src]);
        } else glds16c<sl * SLOT + p * 1024, 8 * p * 1152 + 128 * (np - 36)>(m5, voffB, srdWo, soO);
      }
      if constexpr (s >= 32 && s < 464 && (s + 4) % 36 % 8 == 0 && (s + 4) % 36 < 32) { constexpr int T = (s + 4) / 36 - 1, k = (s + 4) % 36 / 8; bs[k & 1] = *(LAS f32x4*)(tab + T * 128 + k * 32); }
      if constexpr (s >= 36 && s < 468) {
        constexpr int T = s / 36 - 1, v = s % 36, d = (v - 2) / 4;
        const f32x16& acc = (T & 1) ? acc1 : acc0;
        if constexpr (v >= 2 && v <= 32) {
          if constexpr (v % 4 == 2) hp_a(t0, t1, acc[2 * d], acc[2 * d + 1], rstd, bs[(d >> 1) & 1][(2 * d) & 3], bs[(d >> 1) & 1][(2 * d + 1) & 3]);
          if constexpr (v % 4 == 3) hp_b(t0, t1);
          if constexpr (v % 4 == 0) e[d] = hp_c<A_H + 8 * T + d>(t0, t1);
        }
        if constexpr (SC::h_store(s) >= 0) { constexpr int k = SC::h_store(s); __builtin_amdgcn_raw_buffer_store_b128(u32x4{e[4 * k], e[4 * k + 1], e[4 * k + 2], e[4 * k + 3]}, rws, l16 + (T & 1) * 2048 + k * 1024, hso[0] + (T >> 1) * 4096, 16); }
      }
      if constexpr (s >= 873) epi2(IC((s - 864) % 9), IC((s - 864) / 9 - 1));
      if constexpr (SC::x_load(s) >= 0) { constexpr int k = SC::x_load(s); xr[k >> 1][k & 1] = bload8(tok * 1152 + dB0, srdx, 16 * k); }
      if constexpr (s == SC::POLL_HA || s == SC::POLL_HB) pollv = bload8_sc1(vpoll, srdws, flo + (s == SC::POLL_HA ? FL_HA : FL_HB) * 128);
      if constexpr (s == SC::POLL_O) pollv = bload8_sc1(0, srdws, flo + (FL_O + kg) * 128);
      if constexpr (SC::o_load(s) >= 0) { constexpr int f = SC::o_load(s); aload16_sc1<kb_areg(6) + 4 * f, (f & 3) * 1024>(l16, srdws, oso + (f >> 2) * 4096); }
      if constexpr (SC::h_load(s) >= 0) { constexpr int b = SC::h_load(s) / 12, f = SC::h_load(s) % 12; aload16_sc1<kb_areg(2 + b) + 4 * f, (f & 3) * 1024>(l16, srdws, hso[1 + (b & 1)] + (b >> 1) * 12288 + (f >> 2) * 4096); }
      SB();
    }); });
    asm volatile("s_nop 11" : "+v"(ac[5]));
    sfor<8>([&](auto U) { epi2(U, IC(5)); });
    STAMPT(4);
    asm volatile("s_waitcnt vmcnt(0)" : "+v"(xr[0][0]), "+v"(xr[0][1]), "+v"(xr[1][0]), "+v"(xr[1][1]), "+v"(xr[2][0]), "+v"(xr[2][1]) :: "memory");
    BAR();
    bstore8_sc1(tag, vflag, srdws, flo + (FL_FIN + fs) * 128);
    STAMPT(5);
    f32x4 cr[3][2];
#pragma unroll
    for (int k = 0; k < 3; ++k)
#pragma unroll
      for (int j = 0; j < 2; ++j) { cr[k][j] = *(LAS f32x4*)(tab + 2048 + 64 * k + 32 * j); }
    spin_until(flags + (FL_FIN + 3 * (lane & 3) + cg) * 16, tag);
    if (fs == 0) bstore8_sc1(tag, vflag, srdws, flo + FL_EPOCH * 128);
    u32x4 v[3][4];
#pragma unroll
    for (int k = 0; k < 3; ++k)
#pragma unroll
      for (int s = 0; s < 4; ++s) v[k][s] = __builtin_amdgcn_raw_buffer_load_b128(rws, l16, P_OFF + (((rt * 3 + cg) * 4 + s) * 4 + w) * 12288 + (3 * kg + k) * 1024, 16);
#pragma unroll
    for (int k = 0; k < 3; ++k) {
      float a[8];
#pragma unroll
      for (int j = 0; j < 4; ++j) { a[2 * j] = bflo(xr[k][j >> 1][j & 1]) + cr[k][j >> 1][(2 * j) & 3]; a[2 * j + 1] = bfhi(xr[k][j >> 1][j & 1]) + cr[k][j >> 1][(2 * j + 1) & 3]; }
#pragma unroll
      for (int s = 0; s < 4; ++s)
#pragma unroll
        for (int j = 0; j < 4; ++j) { a[2 * j] += bflo(v[k][s][j]); a[2 * j + 1] += bfhi(v[k][s][j]); }
      *(u32x2*)(out + tok * 1152 + dB0 + 32 * k) = u32x2{cvtpk(a[0], a[1]), cvtpk(a[2], a[3])};
      *(u32x2*)(out + tok * 1152 + dB0 + 32 * k + 16) = u32x2{cvtpk(a[4], a[5]), cvtpk(a[6], a[7])};
    }
    STAMPT(6);
    STAMPOUT();
    STAGEOUT();
    __builtin_amdgcn_endpgm();
  }
  {
    using SC = Sched<true>;
    LANE_VALUES
    auto va_of = [&](unsigned word) { return r8 * 3456 + (swz & 1) * 16 + ((word >> (8 * (swz >> 1))) & 255) * 16; };
    auto rd_a = [&](auto Sc) {
      constexpr int s = Sc;
      if constexpr (s < SC::NSTEP) { constexpr int sl = SC::a_stage(s) % 6; wf[s % 6] = rdfrag<1024>(rb[SC::a_tile(s)][sl >> 1] + (sl & 1) * SLOT + SC::a_ks(s) * 2048); }
    };
    auto sync = [&](auto Sc) {
      constexpr int s = Sc, k = SC::warm_k(s);
      if constexpr (SC::need(s) > -25) waitv<SC::allow(s)>();
      if constexpr (s == 18) WAITL0();
      if constexpr (SC::bar(s)) BAR();
      if constexpr (k >= 0 && k < 4) {
        const unsigned la = (288 * kg + (qs[k < 3 ? 2 + k : 5] & 255) * 16) & ~127u, lb = (288 * kg + (k < 3 ? qs[2 + k] >> 24 : qs[6] & 255) * 16) & ~127u;
        warm(((wa >> 1) * 3456 + ((wa & 1) ? lb : la)) | wm, srdA, 0);
      }
      if constexpr (k == 4) warm(((rt * 81 + w * 21 + lane) * 128) | (lane < 21 ? 0 : OOB), srdWo, 144 * kg * 1152);
    };
    f32x4 gram;
    float rstdh, negmuh;
    const unsigned hrow = (lane & 15) < 8 ? (lane & 7) : 0;
    auto halo_val = [&](auto I) {
      f32x4 r;
      sfor<4>([&](auto K) { const float a = aread<A_HH + 4 * (I - 2) + K>(); r[(int)K] = fmaf(ror8(a), negmuh, a) * rstdh; });
      return r;
    };
    auto halo_col = [&](auto I) { return ((qd[I] >> (8 * w)) & 255); };
    bf16x8 wh[2];
    LAS char* hb[3];
#pragma unroll
    for (int i = 0; i < 3; ++i) { hb[i] = lds + (8 * G + q4) * 128 + (((w >> 1) ^ q1) * 64) + (w & 1) * 32 + p4 * 8 + 2 * i * SLOT; PIN(hb[i]); }
    const unsigned ha = (unsigned)(uintptr_t)lds + TAB_OFF + hrow * 128 + G * 16;
    const unsigned qrow = (unsigned)(uintptr_t)lds + QK_OFF + (32 * w + m + 8) * QK_RS + h * 8, vrow = (unsigned)(uintptr_t)lds + (32 * w + m + 8) * V_RS + h * 16;
    sfor<28>([&](auto So) { sfor<18>([&](auto Si) {
      constexpr int s = 18 * So + Si, i = s / 72, u = s % 72;
      if constexpr (u < 36) mfma_vv<s % 36 == 0>(acc0, wf[s % 6], xf[s % 36]); else mfma_vv<s % 36 == 0>(acc1, wf[s % 6], xf[s % 36]);
      rd_a(IC(s + 5));
      if constexpr (s == 480) WAITL0();
      sync(IC(s));
      if constexpr (s % 24 == 0 && s < 432) STAGET(s / 24);
      if constexpr (s == 2) *(LAS u32x4*)(lds + TAB_OFF + w * 3072 + (r8 < 3 ? r8 : 0) * 1024 + pos * 16) = u32x4{0x3f803f80u, 0x3f803f80u, 0x3f803f80u, 0x3f803f80u};
      if constexpr (s >= 19 && s < 37) { constexpr int sg = s - 19; ds_read_a128<A_HX + 4 * sg, (sg >> 1) * 1024 + (sg & 1) * 64>(ha); }
      if constexpr (u % 4 == 2) {
        constexpr int sg = u / 4;
        if constexpr (s >= 46 && s <= 114) mfma16_aa<s == 46, A_HX + s - 46>(gram);
        if constexpr (s == 122) {
          const float dg = (p4 == 0) ? gram[0] : (p4 == 1) ? gram[1] : (p4 == 2) ? gram[2] : gram[3];
          rstdh = G == q4 ? dg : 0.f; negmuh = G == 2 ? gram[0] : 0.f;
          PIN(rstdh); PIN(negmuh);
        }
        if constexpr (s == 126) { rstdh = xor16(rstdh); negmuh = xor16(negmuh); PIN(rstdh); PIN(negmuh); }
        if constexpr (s == 130) { rstdh = xor32(rstdh); negmuh = xor32(negmuh); PIN(rstdh); PIN(negmuh); }
        if constexpr (s == 134) {
          negmuh *= -1.0f / D_;
          rstdh = __builtin_amdgcn_rsqf(fmaxf(rstdh * (1.0f / D_) - negmuh * negmuh, 0.f) + 1e-5f);
          PIN(rstdh); PIN(negmuh);
        }
        if constexpr (i >= 2) mfma16_ava<sg == 0, A_HH + 4 * (i - 2), A_HX + 4 * sg>(wh[sg & 1]);
        constexpr int s2 = s + 4, sl = (3 * (s2 / 72) + (s2 % 72) / 24) % 6;
        if constexpr (s2 >= 144 && s2 < 504) wh[(sg + 1) & 1] = rdfrag<512>(hb[sl >> 1] + (sl & 1) * SLOT + (((s2 % 72) / 4) % 6) * 4096);
      }
      if constexpr (u == 47 && i < 5) { va = va_of(qs[i + 2]); PIN(va); }
      if constexpr (SC::g_stage(s) >= 0) { constexpr int np = SC::g_stage(s), p = SC::g_piece(s); glds16c<(np % 6) * SLOT + p * 1024, 8 * p * 3456>(m6, va, srdA, soA[np % 3]); }
      if constexpr (s >= 36) {
        constexpr int T = s / 36 - 1, v = s % 36, d = (v - 3) / 4, slot = 4 * T + (d >> 1);
        const f32x16& acc = (T & 1) ? acc1 : acc0;
        if constexpr (v >= 3 && v <= 32) {
          if constexpr (v % 4 == 3) sp_a(t0, t1, acc[2 * d], acc[2 * d + 1], rstd);
          if constexpr (v % 4 == 0) { if constexpr (slot < 38) (void)hp_c<A_Q + 2 * slot + (d & 1)>(t0, t1); else awrite2<A_V + 4 * (slot - 38) + 2 * (d & 1)>(t0, t1); }
        }
      }
      if constexpr (s > 432 && s < 471 && s % 2 == 1) {
        constexpr int p = (s - 433) / 2;
        const unsigned ad = qrow + ((qd[p / 4] >> (8 * (p % 4))) & 255) * 16;
        ds_write_a64<A_Q + 4 * p, 0>(ad); ds_write_a64<A_Q + 4 * p + 2, 16>(ad);
      }
      if constexpr (s == 471 || s == 473 || s == 477) {
        constexpr int hi = s == 471 ? 2 : s == 473 ? 3 : 4;
        const f32x4 r = halo_val(IC(hi));
        const unsigned row = (hi == 4 && w == 3) ? 0 : hrow;
        *(LAS u32x2*)(lds + QK_OFF + row * QK_RS + halo_col(IC(hi)) * 16 + G * 8) = u32x2{cvtpk(r[0], r[1]), cvtpk(r[2], r[3])};
      }
      SB();
    }); });
    asm volatile("s_nop 11" : "+v"(acc1));
    WAITL0(); BAR();
    STAMPT(3);
    {
      const int hd = lane & 7, i0 = 32 * w + 4 * r8;
      LAS char* rp[11];
#pragma unroll
      for (int jj = 0; jj < 11; ++jj) { rp[jj] = lds + QK_OFF + (i0 + 1 + jj) * QK_RS + hd * 36; PIN(rp[jj]); }
      unsigned qv[4][9], kv[11][9];
#pragma unroll
      for (int a = 0; a < 4; ++a)
#pragma unroll
        for (int c = 0; c < 9; ++c) qv[a][c] = *(LAS unsigned*)(rp[7 + a] + c * 4);
#pragma unroll
      for (int jj = 0; jj < 4; ++jj)
#pragma unroll
        for (int c = 0; c < 9; ++c) kv[jj][c] = *(LAS unsigned*)(rp[jj] + 288 + c * 4);
      {
#pragma unroll
        for (int k = 0; k < 4; ++k)
          *(LAS f32x4*)(uintptr_t)(vrow + ((qd[6] >> (16 + 8 * (k >> 1))) & 255) * 32 + (k & 1) * 32) = f32x4{acc1[4 * k], acc1[4 * k + 1], acc1[4 * k + 2], acc1[4 * k + 3]} * rstd;
        sfor<3>([&](auto I) {
          constexpr int hi = I + 4;
          const unsigned row = (hi == 4 && w != 3) ? 0 : hrow, col = (hi == 4 && w != 3) ? 0 : halo_col(IC(hi));
          *(LAS f32x4*)(lds + row * V_RS + col * 32 + G * 16) = halo_val(IC(hi));
        });
      }
      SB();
      float lg[4][8];
      sfor<8>([&](auto U) {
        constexpr int u = U;
        if constexpr (u + 4 < 11) {
#pragma unroll
          for (int c = 0; c < 9; ++c) kv[u + 4][c] = *(LAS unsigned*)(rp[u + 4] + 288 + c * 4);
        }
        if constexpr (u < 7) {
          constexpr int p = 19 + u;
          const unsigned ad = vrow + ((qd[p / 4] >> (8 * (p % 4))) & 255) * 32;
          ds_write_a128<A_V + 8 * u, 0>(ad); ds_write_a128<A_V + 8 * u + 4, 32>(ad);
        }
#pragma unroll
        for (int a = 0; a < 4; ++a) lg[a][u] = 0.f;
#pragma unroll
        for (int c = 0; c < 9; ++c)
#pragma unroll
          for (int a = 0; a < 4; ++a) lg[a][u] = dot2(qv[a][c], kv[a + u][c], lg[a][u]);
        SB();
      });
      WAITL0(); BAR();
      STAMPT(4);
      LAS char* vp = lds + (i0 + 1) * V_RS + hd * 72;
      f32x2 vv[11][9];
#pragma unroll
      for (int jj = 0; jj < 3; ++jj)
#pragma unroll
        for (int c = 0; c < 9; ++c) vv[jj][c] = *(volatile LAS f32x2*)(vp + jj * V_RS + c * 8);
      if (rt < 2) {
#pragma unroll
        for (int a = 0; a < 4; ++a)
#pragma unroll
          for (int u = 0; u < 7 - a; ++u) lg[a][u] = (i0 + a + u < 7) ? -1e9f : lg[a][u];
      }
      constexpr float CS = 0.23570226039551584f * 1.4426950408889634f;
#pragma unroll
      for (int a = 0; a < 4; ++a) {
        float mx = lg[a][0];
#pragma unroll
        for (int u = 1; u < 8; ++u) mx = fmaxf(mx, lg[a][u]);
        const float nb = -mx * CS;
        float sum = 0.f;
#pragma unroll
        for (int u = 0; u < 8; ++u) { lg[a][u] = __builtin_amdgcn_exp2f(fmaf(lg[a][u], CS, nb)); sum += lg[a][u]; }
        const float inv = __builtin_amdgcn_rcpf(sum);
#pragma unroll
        for (int u = 0; u < 8; ++u) lg[a][u] *= inv;
      }
      SB();
      f32x2 o[4][9];
      sfor<11>([&](auto J) {
        constexpr int jj = J;
        if constexpr (jj + 3 < 11) {
#pragma unroll
          for (int c = 0; c < 9; ++c) vv[jj + 3][c] = *(volatile LAS f32x2*)(vp + (jj + 3) * V_RS + c * 8);
        }
#pragma unroll
        for (int c = 0; c < 9; ++c) {
#pragma unroll
          for (int a = 0; a < 4; ++a) {
            if (jj == a) o[a][c] = vv[jj][c] * lg[a][0];
            else if (jj > a && jj <= a + 7) o[a][c] = __builtin_elementwise_fma(vv[jj][c], f32x2{lg[a][jj - a], lg[a][jj - a]}, o[a][c]);
          }
        }
        if constexpr (jj >= 7) {
#pragma unroll
          for (int c = 0; c < 9; ++c) *(LAS unsigned*)(rp[jj] + c * 4) = cvtpk(o[jj - 7][c][0], o[jj - 7][c][1]);
        }
        SB();
      });
    }
    STAMPT(5);
    {
      LAS char* rowp = lds + QK_OFF + (32 * w + m + 8) * QK_RS + h * 8;
      const unsigned oso = O_OFF + ((rt * 4 + kg) * 4 + w) * 9216;
#pragma unroll
      for (int s = 0; s < 9; ++s) {
        const u32x2 a = *(LAS u32x2*)(rowp + s * 32), b = *(LAS u32x2*)(rowp + s * 32 + 16);
        __builtin_amdgcn_raw_buffer_store_b128(u32x4{a[0], a[1], b[0], b[1]}, rws, l16, oso + s * 1024, 16);
      }
    }
    WAITV(0); BAR();
    bstore8_sc1(get_tag(), vflag, srdws, flo + (FL_O + kg) * 128);
    STAMPT(6);
  }
  STAMPOUT();
  STAGEOUT();
}

extern "C" void kernel_launch(void* const* d_in, const int* in_sizes, int n_in, void* d_out, int out_size, void* d_ws, size_t ws_size, hipStream_t stream) {
  (void)hipFuncSetAttribute((const void*)main_k, hipFuncAttributeMaxDynamicSharedMemorySize, LDS_BYTES);
  main_k<<<256, 256, LDS_BYTES, stream>>>(d_in[0], d_in[1], d_in[2], d_in[4], d_in[5], d_in[6], (const char*)d_in[7], (char*)d_out, d_ws, make_tab());
}
```

```cpp
#define PADP 500
#define PADM 0
#define PADE 0
#include <hip/hip_runtime.h>
#include <stdint.h>
#include <utility>

typedef __bf16 bf16x8 __attribute__((ext_vector_type(8)));
typedef __bf16 bf16x4 __attribute__((ext_vector_type(4)));
typedef __bf16 bf16x2 __attribute__((ext_vector_type(2)));
typedef float f32x16 __attribute__((ext_vector_type(16)));
typedef float f32x4 __attribute__((ext_vector_type(4)));
typedef float f32x2 __attribute__((ext_vector_type(2)));
typedef unsigned u32x4 __attribute__((ext_vector_type(4)));
typedef unsigned u32x2 __attribute__((ext_vector_type(2)));
typedef unsigned u32x16 __attribute__((ext_vector_type(16)));
typedef unsigned long long u64;
#define LAS __attribute__((address_space(3)))
#define GAS __attribute__((address_space(1)))
typedef GAS u64 gu64;
#define DI __device__ __forceinline__
#define SB() __builtin_amdgcn_sched_barrier(0)
#define IC(n) std::integral_constant<int, (n)>{}
#define RLX_AGENT __ATOMIC_RELAXED, __HIP_MEMORY_SCOPE_AGENT

template <class F, int... I> DI void sfor_(F&& f, std::integer_sequence<int, I...>) { (f(std::integral_constant<int, I>{}), ...); }
template <int N, class F> DI void sfor(F&& f) { sfor_(f, std::make_integer_sequence<int, N>{}); }

constexpr int S_ = 2048, D_ = 576, F_ = 4608;
constexpr int SLOT = 24576;
constexpr int TAB_OFF = 147456;
constexpr int LDS_BYTES = 163840;
constexpr int QK_OFF = 80512, QK_RS = 584, V_RS = 592;
constexpr unsigned H_OFF = 0, O_OFF = 20u << 20, P_OFF = 24u << 20, FLAG_OFF = 40u << 20, WS_BYTES = 41u << 20;
constexpr unsigned OOB = 0x40000000u;
constexpr int FL_HA = 0, FL_HB = 12, FL_O = 24, FL_FIN = 28, FL_EPOCH = 40, FL_N = 41;
constexpr int A_H = 56;
constexpr int A_Q = 16;
constexpr int A_V = 92;
constexpr int A_HH = 164;
constexpr int A_HX = 184;

__host__ DI int map_role(int bid) { return (bid >> 3) & 3; }
__host__ DI int map_kg(int bid) { return bid & 3; }
__host__ DI int map_rt(int bid) { return ((bid >> 2) & 1) * 8 + (bid >> 5); }
__host__ DI int map_sp(int bid) { return map_role(bid) == 3 ? 12 + map_kg(bid) : 3 * map_kg(bid) + map_role(bid); }

#define GAUX ""
#define XAUX ""
#define WAUX ""
DI u32x4 make_srd(const void* p, unsigned bytes) {
  uint64_t a = (uint64_t)p;
  u32x4 r; r[0] = (unsigned)a; r[1] = (unsigned)(a >> 32) & 0xffffu; r[2] = bytes; r[3] = 0x00020000u;
  return r;
}
DI void glds16(unsigned m0v, unsigned voff, u32x4 srd, unsigned soff) {
  asm volatile("s_mov_b32 m0, %0\n\ts_nop 0\n\tbuffer_load_dwordx4 %1, %2, %3 offen lds" :: "s"(m0v), "v"(voff), "s"(srd), "s"(soff) : "memory");
}
template <unsigned MC, unsigned SC> DI void glds16c(unsigned mb, unsigned voff, u32x4 srd, unsigned sb) {
  unsigned tmp;
  asm volatile("s_add_u32 m0, %1, %5\n\ts_add_u32 %0, %4, %6\n\tbuffer_load_dwordx4 %2, %3, %0 offen" GAUX " lds"
               : "=&s"(tmp) : "s"(mb), "v"(voff), "s"(srd), "s"(sb), "i"(MC), "i"(SC) : "memory", "scc");
}
template <unsigned MC> DI void glds16r(unsigned mb, unsigned voff, u32x4 srd, unsigned& so, unsigned step) {
  asm volatile("s_add_u32 m0, %1, %5\n\ts_add_u32 %0, %0, %4\n\tbuffer_load_dwordx4 %2, %3, %0 offen lds"
               : "+s"(so) : "s"(mb), "v"(voff), "s"(srd), "s"(step), "i"(MC) : "memory", "scc");
}
constexpr int x_base(int t) { return (t / 3 == 1 ? 2 : 4) * SLOT + (t % 3 == 2 ? SLOT : 0); }
template <int T> DI void x_line(unsigned m6, unsigned vx0, unsigned vx1, u32x4 srd, unsigned s0, unsigned s1, unsigned s2, unsigned s3) {
  constexpr int d0 = x_base(T) + (T % 3 == 1 ? 4096 : 0) - (T % 3) * 128, d2 = T % 3 == 1 ? SLOT - 1024 : 1024;
  asm volatile("s_add_u32 m0, %0, %8\n\ts_nop 0\n\tbuffer_load_dwordx4 %1, %3, %4 offen offset:%c10" XAUX " lds\n\t"
               "s_addk_i32 m0, 0x400\n\ts_nop 0\n\tbuffer_load_dwordx4 %2, %3, %5 offen offset:%c10" XAUX " lds\n\t"
               "s_addk_i32 m0, %c9\n\ts_nop 0\n\tbuffer_load_dwordx4 %1, %3, %6 offen offset:%c10" XAUX " lds\n\t"
               "s_addk_i32 m0, 0x400\n\ts_nop 0\n\tbuffer_load_dwordx4 %2, %3, %7 offen offset:%c10" XAUX " lds"
               :: "s"(m6), "v"(vx0), "v"(vx1), "s"(srd), "s"(s0), "s"(s1), "s"(s2), "s"(s3), "i"(d0), "i"(d2), "i"((T % 3) * 128) : "memory", "scc");
}
DI u32x4 bload16(unsigned voff, u32x4 srd, unsigned soff) {
  u32x4 r;
  asm volatile("buffer_load_dwordx4 %0, %1, %2, %3 offen" : "=v"(r) : "v"(voff), "s"(srd), "s"(soff) : "memory");
  return r;
}
DI unsigned bload4(unsigned voff, u32x4 srd, unsigned soff) {
  unsigned r;
  asm volatile("buffer_load_dword %0, %1, %2, %3 offen" : "=v"(r) : "v"(voff), "s"(srd), "s"(soff) : "memory");
  return r;
}
DI u32x2 bload8(unsigned voff, u32x4 srd, unsigned soff) {
  u32x2 r;
  asm volatile("buffer_load_dwordx2 %0, %1, %2, %3 offen" : "=v"(r) : "v"(voff), "s"(srd), "s"(soff) : "memory");
  return r;
}
DI void warm(unsigned voff, u32x4 srd, unsigned soff) {
  asm volatile("buffer_load_dword a11, %0, %1, %2 offen" WAUX :: "v"(voff), "s"(srd), "s"(soff) : "memory");
}
DI u64 bload8_sc1(unsigned voff, u32x4 srd, unsigned soff) {
  u64 r;
  asm volatile("buffer_load_dwordx2 %0, %1, %2, %3 offen sc1" : "=v"(r) : "v"(voff), "s"(srd), "s"(soff) : "memory");
  return r;
}
template <int AR, int OFF> DI void aload16_sc1(unsigned voff, u32x4 srd, unsigned soff) {
  asm volatile("buffer_load_dwordx4 a[%c3:%c4], %0, %1, %2 offen offset:%c5 sc1" :: "v"(voff), "s"(srd), "s"(soff), "i"(AR), "i"(AR + 3), "i"(OFF) : "memory");
}
template <int AR, int OFF> DI void aload16(unsigned voff, u32x4 srd) {
  asm volatile("buffer_load_dwordx4 a[%c2:%c3], %0, %1, 0 offen offset:%c4" :: "v"(voff), "s"(srd), "i"(AR), "i"(AR + 3), "i"(OFF) : "memory");
}
template <int ROLE, int K, int N> DI void mark() { asm volatile("s_branch 1f\n\t.p2align 12\n.Lm%c0_%c1:\n\ts_setpc_b64 vcc\n1:" :: "i"(ROLE), "i"(K)); }
template <int R> DI void visit(unsigned sel) {
  constexpr int a2 = R == 3 ? 12 : 4 * R + 2, a3 = R == 3 ? 13 : 4 * R + 3, b1 = R == 3 ? 12 : 4 * R + 1, b3 = R == 3 ? 12 : 4 * R + 3;
  asm volatile("s_getpc_b64 vcc\n\ts_add_u32 s90, vcc_lo, %0\n\ts_addc_u32 s91, vcc_hi, 0\n\ts_add_u32 vcc_lo, vcc_lo, 52\n\ts_addc_u32 vcc_hi, vcc_hi, 0\n\ts_setpc_b64 s[90:91]\n\t"
               "s_branch .Lm0_%c1\n\ts_branch .Lm0_%c2\n\ts_branch .Lm0_%c3\n\ts_branch .Lm0_%c4\n\ts_branch .Lm1_%c1\n\ts_branch .Lm1_%c5\n\ts_branch .Lm1_%c3\n\ts_branch .Lm1_%c6"
               :: "s"(sel + 20), "i"(4 * R), "i"(4 * R + 1), "i"(a2), "i"(a3), "i"(b1), "i"(b3) : "vcc", "s90", "s91", "scc");
}
template <int N> DI void waitv() { asm volatile("s_waitcnt vmcnt(%c0)" :: "i"(N > 63 ? 63 : N) : "memory"); }
#define WAITV(n) asm volatile("s_waitcnt vmcnt(" #n ")" ::: "memory")
#define BAR() asm volatile("s_barrier" ::: "memory")
#define WAITL0() asm volatile("s_waitcnt lgkmcnt(0)" ::: "memory")
#define PIN(x) asm volatile("" : "+v"(x))
#define PINS(x) asm volatile("" : "+s"(x))

DI unsigned cvtpk(float lo, float hi) { f32x2 v = {lo, hi}; bf16x2 b = __builtin_convertvector(v, bf16x2); return __builtin_bit_cast(unsigned, b); }
DI float bflo(unsigned u) { return __uint_as_float(u << 16); }
DI float bfhi(unsigned u) { return __uint_as_float(u & 0xffff0000u); }
DI float dot2(unsigned a, unsigned b, float c) { return __builtin_amdgcn_fdot2_f32_bf16(__builtin_bit_cast(bf16x2, a), __builtin_bit_cast(bf16x2, b), c, false); }
DI float xor32(float v) { auto r = __builtin_amdgcn_permlane32_swap(__float_as_uint(v), __float_as_uint(v), false, false); return __uint_as_float(r[0]) + __uint_as_float(r[1]); }
DI float xor16(float v) { auto r = __builtin_amdgcn_permlane16_swap(__float_as_uint(v), __float_as_uint(v), false, false); return __uint_as_float(r[0]) + __uint_as_float(r[1]); }
DI float fmul_s(float a, float b) { float r; asm("v_mul_f32_e32 %0, %1, %2" : "=v"(r) : "v"(a), "v"(b)); return r; }

DI bf16x4 rdhalf(LAS char* p) { return __builtin_amdgcn_ds_read_tr16_b64_v4bf16((LAS bf16x4*)p); }
DI bf16x8 cat(bf16x4 a, bf16x4 b) { return __builtin_shufflevector(a, b, 0, 1, 2, 3, 4, 5, 6, 7); }
template <bool FIRST> DI void mfma_vv(f32x4& acc, bf16x8 a, u32x4 b) {
  if constexpr (FIRST) asm volatile("v_mfma_f32_16x16x32_bf16 %0, %1, %2, 0" : "=&v"(acc) : "v"(a), "v"(b));
  else                 asm volatile("v_mfma_f32_16x16x32_bf16 %0, %1, %2, %0" : "+v"(acc) : "v"(a), "v"(b));
}
DI void mfma_vvc(f32x4& acc, u32x4 a, u32x4 b, const f32x4& c) { asm volatile("v_mfma_f32_16x16x32_bf16 %0, %1, %2, %3" : "=&v"(acc) : "v"(a), "v"(b), "v"(c)); }
template <bool FIRST, int AR> DI void mfma_va(f32x4& acc, bf16x8 a) {
  if constexpr (FIRST) asm volatile("v_mfma_f32_16x16x32_bf16 %0, %1, a[%c2:%c3], 0" : "=&v"(acc) : "v"(a), "i"(AR), "i"(AR + 3));
  else                 asm volatile("v_mfma_f32_16x16x32_bf16 %0, %1, a[%c2:%c3], %0" : "+v"(acc) : "v"(a), "i"(AR), "i"(AR + 3));
}
template <int AR> DI void mfma_k16(f32x4& acc, bf16x4 a) { asm volatile("v_mfma_f32_16x16x16_bf16 %0, %1, a[%c2:%c3], %0" : "+v"(acc) : "v"(a), "i"(AR), "i"(AR + 1)); }
template <bool FIRST, int AR> DI void mfma16_aa(f32x4& acc) {
  if constexpr (FIRST) asm volatile("v_mfma_f32_16x16x32_bf16 %0, a[%c1:%c2], a[%c1:%c2], 0" : "=&v"(acc) : "i"(AR), "i"(AR + 3));
  else                 asm volatile("v_mfma_f32_16x16x32_bf16 %0, a[%c1:%c2], a[%c1:%c2], %0" : "+v"(acc) : "i"(AR), "i"(AR + 3));
}
template <bool FIRST, int AD, int AR> DI void mfma16_ava(bf16x8 a) {
  if constexpr (FIRST) asm volatile("v_mfma_f32_16x16x32_bf16 a[%c1:%c2], %0, a[%c3:%c4], 0" :: "v"(a), "i"(AD), "i"(AD + 3), "i"(AR), "i"(AR + 3));
  else                 asm volatile("v_mfma_f32_16x16x32_bf16 a[%c1:%c2], %0, a[%c3:%c4], a[%c1:%c2]" :: "v"(a), "i"(AD), "i"(AD + 3), "i"(AR), "i"(AR + 3));
}
template <int AR> DI float aread(void) { float r; asm volatile("v_accvgpr_read_b32 %0, a[%c1]" : "=v"(r) : "i"(AR)); return r; }
template <int AR> DI void awrite2(float t0, float t1) { asm volatile("v_accvgpr_write_b32 a[%c2], %0\n\tv_accvgpr_write_b32 a[%c3], %1" :: "v"(t0), "v"(t1), "i"(AR), "i"(AR + 1)); }
template <int AR, int OFF> DI void ds_write_a128(unsigned addr) { asm volatile("ds_write_b128 %0, a[%c1:%c2] offset:%c3" :: "v"(addr), "i"(AR), "i"(AR + 3), "i"(OFF) : "memory"); }
DI float ror8(float v) { return __int_as_float(__builtin_amdgcn_update_dpp(0, __float_as_int(v), 0x128, 0xf, 0xf, false)); }
template <int AR> DI void awrite(unsigned v) { asm volatile("v_accvgpr_write_b32 a[%c1], %0" :: "v"(v), "i"(AR)); }
DI void hp_a(float& t0, float& t1, float a0, float a1, float r, float b0, float b1) {
  asm volatile("v_fma_f32 %0, %2, %4, %5\n\tv_fma_f32 %1, %3, %4, %6\n\tv_max_f32_e32 %0, 0, %0" : "=&v"(t0), "=&v"(t1) : "v"(a0), "v"(a1), "v"(r), "v"(b0), "v"(b1));
}
DI void hq_a(f32x2& t, f32x2 a, f32x2 r, f32x2 b) { asm volatile("v_pk_fma_f32 %0, %1, %2, %3" : "=v"(t) : "v"(a), "v"(r), "v"(b)); }
DI void hq_b(float& t0, float& t1) { asm volatile("v_mul_f32_e64 %0, %0, |%0|\n\tv_mul_f32_e64 %1, %1, |%1|" : "+v"(t0), "+v"(t1)); }
template <int AR> DI unsigned hq_c(float t0, float t1) {
  unsigned e;
  asm volatile("v_cvt_pk_bf16_f32 %0, %1, %2\n\tv_pk_max_i16 %0, %0, 0\n\tv_accvgpr_write_b32 a[%c3], %0" : "=v"(e) : "v"(t0), "v"(t1), "i"(AR));
  return e;
}
DI void hr_a(float& t0, float& t1, float a0, float a1, float r, float b0, float b1) {
  asm volatile("v_fma_f32 %0, %2, %4, %5\n\tv_fma_f32 %1, %3, %4, %6" : "=&v"(t0), "=&v"(t1) : "v"(a0), "v"(a1), "v"(r), "v"(b0), "v"(b1));
}
DI void hp_b(float& t0, float& t1) { asm volatile("v_max_f32_e32 %1, 0, %1\n\tv_mul_f32_e32 %0, %0, %0\n\tv_mul_f32_e32 %1, %1, %1" : "+v"(t0), "+v"(t1)); }
template <int AR> DI unsigned hp_c(float t0, float t1) {
  unsigned e;
  asm volatile("v_cvt_pk_bf16_f32 %0, %1, %2\n\tv_accvgpr_write_b32 a[%c3], %0" : "=v"(e) : "v"(t0), "v"(t1), "i"(AR));
  return e;
}
DI void sp_a(float& t0, float& t1, float a0, float a1, float r) { asm volatile("v_mul_f32_e32 %0, %2, %4\n\tv_mul_f32_e32 %1, %3, %4" : "=&v"(t0), "=&v"(t1) : "v"(a0), "v"(a1), "v"(r)); }
template <int AR> DI void spair(float a0, float a1, float r) { float t0, t1; sp_a(t0, t1, a0, a1, r); (void)hp_c<AR>(t0, t1); }
template <int AR, int OFF> DI void ds_read_a64(unsigned addr) { asm volatile("ds_read_b64 a[%c1:%c2], %0 offset:%c3" :: "v"(addr), "i"(AR), "i"(AR + 1), "i"(OFF) : "memory"); }
DI void bstore8_sc1(u64 v, unsigned voff, u32x4 srd, unsigned soff) { asm volatile("buffer_store_dwordx2 %0, %1, %2, %3 offen sc1" :: "v"(v), "v"(voff), "s"(srd), "s"(soff) : "memory"); }
template <int AR, int OFF> DI void ds_write_a64(unsigned addr) { asm volatile("ds_write_b64 %0, a[%c1:%c2] offset:%c3" :: "v"(addr), "i"(AR), "i"(AR + 1), "i"(OFF) : "memory"); }

#define STAMPT(i)
#define STAMPOUT()
#define STAGET(n)
#define STAGEOUT()

#define WS0 0
#define WSP 48
#define V0 0
#define V1 1
#define V2 2
#define V3 4
#define EPI 0
#define PAIRW 0
#define OAUX 0
#define WPRO 1
template <bool ATTN> struct Sched {
  static constexpr int P1 = ATTN ? 504 : 432, NSTEP = ATTN ? 504 : 924, NSTAGE = ATTN ? 21 : 39;
  static constexpr int a_stage(int s) { return s < P1 ? 3 * (s / 72) + (s % 36) / 12 : s < 864 ? s / 24 : 36 + (s - 864) / 20; }
  static constexpr int a_tile(int s) { return s < P1 ? (s % 72) / 36 : s < 864 ? (s % 24) / 12 : ((s - 864) % 20) / 10; }
  static constexpr int a_ks(int s) { return s < 864 ? s % 12 : (s - 864) % 10; }
  static constexpr bool skip(int s) { return ATTN && s >= 108 && s < 144 && s % 2 == 1; }
  static constexpr int g_stage(int s) {
    if (s < 0) return (s + 24) / 6;
    if (s >= 864 || s % 4 != 1) return -1;
    const int st = s / 24 + 4;
    return st >= NSTAGE || (st >= 36 && (s % 24) / 4 == 5) ? -1 : st;
  }
  static constexpr int g_piece(int s) { return (s % 24) / 4; }
  static constexpr int g_last(int st) { int r = -25; for (int s = -24; s < NSTEP; ++s) if (g_stage(s) == st) r = s; return r; }
  static constexpr int warm_k(int s) { return s >= WS0 && (s - WS0) % WSP == 0 && (s - WS0) / WSP < 5 ? (s - WS0) / WSP : -1; }
  static constexpr int PUB_HA = 306, PUB_HB = 528, POLL_HA = 380, CHK_HA = 428, POLL_HB = 580, CHK_HB = 624, POLL_O = 792, CHK_O = 828;
  static constexpr int h_store(int s) { return !ATTN && s >= 36 && s < 468 ? (s % 36 == 20 ? 0 : s % 36 == 35 ? 1 : -1) : -1; }
  static constexpr int h_load(int s) {
    if (ATTN || s % 4 != 3) return -1;
    return s >= 432 && s < 528 ? (s - 432) / 4 : s >= 624 && s < 720 ? 24 + (s - 624) / 4 : -1;
  }
  static constexpr int o_load(int s) { return !ATTN && s > CHK_O && s <= CHK_O + 18 && s % 2 == 0 ? (s - CHK_O) / 2 - 1 : -1; }
  static constexpr int p_store(int s) { return !ATTN && s >= 874 ? ((s - 864) % 10 == 5 ? 0 : (s - 864) % 10 == 7 ? 1 : -1) : -1; }
  static constexpr int x_load(int s) { return !ATTN && s >= 866 && s < 890 && s % 4 == 2 ? (s - 866) / 4 : -1; }
  static constexpr int NMARK = ATTN ? 13 : 14;
  static constexpr int mark_k(int s) {
    if (s <= 396) return s % 66 == 0 ? s / 66 : -1;
    if (ATTN) return s == 444 ? 7 : s == 480 ? 8 : s == 503 ? 9 : -1;
    return s >= 456 && (s - 456) % 84 == 0 ? 7 + (s - 456) / 84 : -1;
  }
  static constexpr int vm(int s) {
    int c = g_stage(s) >= 0;
    c += warm_k(s) >= 0;
    if (!ATTN && s >= 0) c += (h_store(s) >= 0) + (h_load(s) >= 0) + (o_load(s) >= 0) + (p_store(s) >= 0) + (s == POLL_HA) + (s == POLL_HB) + (s == POLL_O) + (x_load(s) >= 0);
    return c;
  }
  static constexpr bool bar(int s) { return s < P1 ? ((s % 72 == 0 || s % 72 == 18 || s % 72 == 48)) : (s <= 864 && s % 24 == 0); }
  static constexpr int need(int s) {
    int r = -25;
    if (bar(s) && !(ATTN && s == 480)) r = g_last(s < P1 ? 3 * (s / 72) + (s % 72 == 0 ? 1 : s % 72 == 18 ? 2 : 3) : s < 864 ? s / 24 + 1 : 38);
    if (!ATTN) {
      const int e = s == PUB_HA ? 251 : s == PUB_HB ? 467 : s == CHK_HA ? POLL_HA : s == CHK_HB ? POLL_HB : s == CHK_O ? POLL_O : s == 720 ? 671 : s == 768 ? 719 : s == 863 ? 862 : -25;
      if (e > r) r = e;
    }
    return r;
  }
  static constexpr int allow(int s) { int c = 0; for (int k = need(s) + 1; k < s; ++k) c += vm(k); return c; }
};
constexpr int kb_areg(int kb) { return kb == 0 ? A_H : kb == 1 ? A_H + 48 : kb == 2 ? A_H + 96 : kb == 3 ? A_H + 144 : kb == 4 ? A_H : kb == 5 ? A_H + 48 : A_H + 96; }
constexpr int qkv_pair(int kg, int p) {
  constexpr signed char T[4][28] = {
    {0,2,4,6, 8,10,12,-1, 18,20,22,24, 26,28,30,32, 14,16,34,52, 36,38,40,42, 44,46,48,50},
    {6,8,10,12, 0,2,4,-1, 24,26,28,30, 14,16,32,34, 18,20,22,50, 42,44,46,48, 36,38,40,52},
    {4,6,8,10, 12,14,16,-1, 22,24,26,28, 0,2,18,20, 30,32,34,36, 40,42,44,46, 48,50,52,38},
    {2,4,6,8, 10,12,14,-1, 20,22,24,26, 28,30,32,34, 16,0,18,36, 38,40,42,44, 46,48,50,52}};
  return T[kg][p];
}
struct QkvTab { unsigned v[4][16]; };
constexpr QkvTab make_tab() {
  QkvTab t = {};
  for (int kg = 0; kg < 4; ++kg)
    for (int p = 0; p < 28; ++p) {
      const int v = qkv_pair(kg, p), sv = v < 0 ? qkv_pair(kg, p - 1) : v;
      t.v[kg][p / 4] |= (unsigned)((sv / 18) * 72 + sv % 18) << (8 * (p % 4));
      t.v[kg][8 + p / 4] |= (unsigned)(sv >= 36 ? sv - 36 : sv) << (8 * (p % 4));
    }
  return t;
}

DI void spin_until(gu64* p, u64 tag) {
  for (unsigned spins = 0; spins < (1u << 18); ++spins) {
    const u64 f = __hip_atomic_load(p, RLX_AGENT);
    if (__all(f == tag)) break;
    __builtin_amdgcn_s_sleep(1);
  }
}

struct Args { const void *x, *wqkv, *wo, *w1, *b1, *w2, *b2; void *out, *ws; QkvTab qt; };
static_assert(sizeof(Args) == 72 + 256, "no padding");
DI char* as_ptr(unsigned lo, unsigned hi) { return (char*)(((u64)hi << 32) | lo); }

__global__ __launch_bounds__(256, 1) void main_k(const Args) {
  extern __shared__ __attribute__((aligned(16))) char smem[];
  LAS char* lds = (LAS char*)smem;
  const int tid = threadIdx.x, lane = tid & 63, bid = blockIdx.x;
  const int kg = map_kg(bid), cg = map_role(bid), rt = map_rt(bid);
  u64 tent; asm volatile("s_memrealtime %0" : "=s"(tent));
  u32x16 ka, kt; u32x2 kw;
  asm volatile("s_load_dwordx16 %0, %3, 0x0\n\ts_load_dwordx2 %1, %3, 0x40\n\ts_load_dwordx16 %2, %3, %4 offset:0x48"
               : "=&s"(ka), "=&s"(kw), "=&s"(kt) : "s"(__builtin_amdgcn_kernarg_segment_ptr()), "s"(kg * 64));
  const void* code;
  unsigned code_bytes;
  asm volatile("s_getpc_b64 %0\n.Lk_pc:\n\ts_mov_b32 %1, .Lk_end0-.Lk_pc\n\ts_max_i32 %1, %1, .Lk_end1-.Lk_pc" : "=s"(code), "=s"(code_bytes) :: "scc", "a255");
  const int w = __builtin_amdgcn_readfirstlane(tid >> 6);
  const bool attn = cg == 3;
  const int tokbase = 256 * (rt >> 1) + (rt & 1);
  const int n16 = lane & 15, r8 = lane >> 3, pos = lane & 7;
  const int G = lane >> 4, q4 = (lane >> 2) & 3, p4 = lane & 3;

  const unsigned rot = (0x24924924u >> (2 * ((rt & 7) + cg))) & 3;
  unsigned kro[3] = {rot * 192, rot == 2 ? 0 : rot * 192 + 192, rot == 0 ? 384 : rot * 192 - 192};
  unsigned m6 = 6 * w * 1024, sx = tokbase * 1152 + w * 32 * 2304;
  unsigned vx0 = r8 * 2304 + ((pos ^ G) << 4), vx1 = vx0 ^ 64;
  PINS(m6); PINS(sx); PIN(vx0); PIN(vx1); PINS(kro[0]); PINS(kro[1]); PINS(kro[2]);
  const unsigned swz = pos ^ (((r8 >> 1) & 3) << 1);
  const int fs = 3 * kg + cg, f0 = fs * 384;
  unsigned strideA = attn ? 3456u : 9216u;
  PINS(strideA);
  unsigned soA[3];
#pragma unroll
  for (int j = 0; j < 3; ++j) { soA[j] = (kro[j] + 48 * w) * strideA + (attn ? 288 * kg : f0 * 2); PINS(soA[j]); }
  unsigned vab = r8 * strideA + (swz & 1) * 16, vsh = 8 * (swz >> 1), vcode = tid < 64 ? tid * 128 : OOB;
  PIN(vab); PIN(vsh); PIN(vcode);
  u32x4 id0, id1;
  {
    const unsigned one = 0x3f80u << (16 * (n16 & 1));
    const int e = (n16 >> 1) - 4 * G;
#pragma unroll
    for (int j = 0; j < 4; ++j) { id0[j] = e == j ? one : 0u; id1[j] = e + 8 == j ? one : 0u; }
  }
  PIN(id0); PIN(id1);
  unsigned po0 = (4 * G + q4) * 128 + (((2 * G + (q4 >> 1)) & 3) << 5) + p4 * 8;
  LAS char* rb[4][3];
#pragma unroll
  for (int i = 0; i < 3; ++i)
#pragma unroll
    for (int c = 0; c < 4; ++c) { rb[c][i] = lds + (po0 ^ (c << 5)) + 2 * i * SLOT; PIN(rb[c][i]); }
  LAS char* xp[2][2];
#pragma unroll
  for (int u = 0; u < 2; ++u) {
    const unsigned xr0 = w * 6144 + (n16 >> 3) * 1024 + (n16 & 7) * 128 + (((G ^ (n16 >> 1)) << 4) ^ (u << 6));
    xp[0][u] = lds + xr0 + 4 * SLOT; xp[1][u] = lds + xr0 + 2 * SLOT; PIN(xp[0][u]); PIN(xp[1][u]);
  }
  unsigned ones = 0x3f803f80u; PIN(ones);
  asm volatile("s_waitcnt lgkmcnt(0)" : "+s"(ka), "+s"(kw), "+s"(kt), "+s"(tent));
  STAMPT(0);
  const char *x = as_ptr(ka[0], ka[1]), *wo = as_ptr(ka[4], ka[5]), *b1 = as_ptr(ka[8], ka[9]), *w2 = as_ptr(ka[10], ka[11]), *b2 = as_ptr(ka[12], ka[13]);
  char *out = as_ptr(ka[14], ka[15]), *ws = as_ptr(kw[0], kw[1]);
  const u32x4 srdx = make_srd(x, S_ * D_ * 2);
  auto x_more = [&](auto T) { const unsigned b = sx + kro[T / 3] * 2; x_line<T>(m6, vx0, vx1, srdx, b, b + 8 * 2304, b + 16 * 2304, b + 24 * 2304); };
  sfor<3>(x_more);
  const unsigned vsel = 4 * w + (attn ? 16 : 0);
  if (V0 == 0) visit<0>(vsel);
  if (V1 == 0) visit<1>(vsel);
  u32x4 srdA = make_srd(attn ? as_ptr(ka[2], ka[3]) : as_ptr(ka[6], ka[7]), attn ? D_ * 3 * D_ * 2 : D_ * F_ * 2);
  PINS(srdA);
  unsigned qs[7], qd[7];
#pragma unroll
  for (int i = 0; i < 7; ++i) { qs[i] = kt[i]; qd[i] = kt[8 + i]; }
  auto va_of = [&](unsigned word) { return vab + ((word >> vsh) & 255) * 16; };
  unsigned va = va_of(attn ? qs[0] : 0x06040200u), so = soA[0] - 8 * strideA;
  auto w_piece = [&](auto K) {
    constexpr int n = K / 6, p = K % 6;
    if constexpr (p == 0 && n > 0) so = soA[n % 3] - 8 * strideA;
    if constexpr (K == 18) va = va_of(attn ? qs[1] : 0x0e0c0a08u);
    glds16r<n * SLOT + p * 1024>(m6, va, srdA, so, 8 * strideA);
  };
  sfor<6>(w_piece);
  warm(vcode, make_srd(code, code_bytes), ((bid >> 3) & 15) * 8192);
  if (V0 == 1) visit<0>(vsel);
  if (V1 == 1) visit<1>(vsel);
  if (V2 == 1) visit<2>(vsel);
  const u32x4 srdws = make_srd(ws, WS_BYTES);
  const unsigned flo = FLAG_OFF + rt * FL_N * 128;
  auto misc_loads = [&]() {
    asm volatile("buffer_load_dwordx2 a[12:13], %0, %1, %2 offen sc1" :: "v"(0), "s"(srdws), "s"(flo + FL_EPOCH * 128) : "memory");
    if (attn) {
      int ht = tokbase + 2 * (r8 - 8); ht = ht < 0 ? 0 : ht;
      const unsigned vh = ht * 1152 + pos * 16;
      const unsigned kb = ((0x24924924u >> (2 * (rot + w))) & 3) * 384 | (w == 3 ? OOB : 0);
      glds16c<TAB_OFF, 0>(w * 3072, vh, srdx, kb); glds16c<TAB_OFF + 1024, 128>(w * 3072, vh, srdx, kb); glds16c<TAB_OFF + 2048, 256>(w * 3072, vh, srdx, kb);
    } else {
      asm volatile("buffer_load_dword a14, %0, %1, 0 offen" :: "v"(tid * 4), "s"(make_srd(b1 + f0 * 2, 768)) : "memory");
      asm volatile("buffer_load_dword a15, %0, %1, 0 offen" :: "v"(tid * 4), "s"(make_srd(b2 + 192 * cg * 2, 384)) : "memory");
    }
  };
  u32x4 xf[36];
  f32x4 nm[2];
  float rstd[2];
  {
    float sm[2] = {0.f, 0.f}, sq[2] = {0.f, 0.f};
    SB();
    sfor<10>([&](auto T) {
      constexpr int t = T;
      if constexpr (t == 0) x_more(IC(3));
      if constexpr (t + 4 < 9) x_more(IC(t + 4));
      if constexpr (t == 4) misc_loads();
      if constexpr (t == V2 - 2) visit<2>(vsel);
      if constexpr (t == V3 - 2) visit<3>(vsel);
      if constexpr (t == V1 - 2) visit<1>(vsel);
      if constexpr (t == V0 - 2) visit<0>(vsel);
      if constexpr (t < 9) {
        waitv<4 * ((t + 4 > 8 ? 8 : t + 4) - t) + (t < 3 ? 6 : 0) + (t > 9 - WPRO ? 6 * (t - 9 + WPRO) : 0)>();
#pragma unroll
        for (int u = 0; u < 4; ++u) xf[4 * t + u] = *(LAS u32x4*)(xp[t / 3 == 1][u >> 1] + (t % 3 == 1 ? 4096 + (u & 1) * SLOT : (t % 3 == 2 ? SLOT : 0) + (u & 1) * 2048));
      }
      SB();
      if constexpr (t > 0) {
#pragma unroll
        for (int u = 0; u < 4; u += 2)
#pragma unroll
          for (int j = 0; j < 4; ++j) {
            const unsigned v0 = xf[4 * t - 4 + u][j], v1 = xf[4 * t - 3 + u][j];
            sm[0] = dot2(v0, ones, sm[0]); sm[1] = dot2(v1, ones, sm[1]); sq[0] = dot2(v0, v0, sq[0]); sq[1] = dot2(v1, v1, sq[1]);
          }
        PIN(sm[0]); PIN(sm[1]); PIN(sq[0]); PIN(sq[1]);
      }
      if constexpr (t >= 9 - WPRO && t < 9) sfor<6>([&](auto P) { w_piece(IC(6 + 6 * (t - 9 + WPRO) + P)); });
      SB();
    });
#pragma unroll
    for (int tb = 0; tb < 2; ++tb) {
      const float negmu = xor32(xor16(sm[tb])) * (-1.0f / D_);
      nm[tb] = f32x4{negmu, negmu, negmu, negmu};
      rstd[tb] = __builtin_amdgcn_rsqf(fmaxf(xor32(xor16(sq[tb])) * (1.0f / D_) - negmu * negmu, 0.f) + 1e-5f);
    }
  }
  STAMPT(1);
  bf16x4 wl[6], wu[6];
  {
    f32x4 dl[3], dh[3];
    asm volatile("s_nop 1" : "+v"(nm[0]), "+v"(nm[1]));
    sfor<38>([&](auto B) {
      constexpr int b = B;
      if constexpr (b < 36) mfma_vvc(dl[b % 3], id0, xf[b], nm[b & 1]);
      if constexpr (b == 36) {
        asm volatile("s_waitcnt vmcnt(18)\n\ts_barrier" : "+v"(dl[1]), "+v"(dh[1]), "+v"(dl[2]), "+v"(dh[2]) :: "memory");
#pragma unroll
        for (int t = 0; t < 5; ++t) { wl[t] = rdhalf(rb[t & 1][0] + (t >> 1) * 4096); wu[t] = rdhalf(rb[t & 1][0] + (t >> 1) * 4096 + 2048); }
      }
      SB();
      if constexpr (b >= 2) { xf[b - 2][0] = cvtpk(dl[(b - 2) % 3][0], dl[(b - 2) % 3][1]); xf[b - 2][1] = cvtpk(dl[(b - 2) % 3][2], dl[(b - 2) % 3][3]); }
      SB();
      if constexpr (b < 36) mfma_vvc(dh[b % 3], id1, xf[b], nm[b & 1]);
      SB();
      if constexpr (b >= 2) { xf[b - 2][2] = cvtpk(dh[(b - 2) % 3][0], dh[(b - 2) % 3][1]); xf[b - 2][3] = cvtpk(dh[(b - 2) % 3][2], dh[(b - 2) % 3][3]); PIN(xf[b - 2]); }
      if constexpr (b % 2 == 0 && 6 + 6 * WPRO + b / 2 < 24) w_piece(IC(6 + 6 * WPRO + b / 2));
      SB();
    });
  }
  STAMPT(2);
#define PADTO(T) do { u64 tn; do { asm volatile("s_memrealtime %0\n\ts_waitcnt lgkmcnt(0)" : "=s"(tn)); } while (tn - tent < (T)); } while (0)
  if (PADP) PADTO(PADP);

  const u32x4 srdW2 = make_srd(w2, F_ * D_ * 2), srdWo = make_srd(wo, D_ * D_ * 2);
  const __amdgpu_buffer_rsrc_t rws = __builtin_amdgcn_make_buffer_rsrc(ws, 0, WS_BYTES, 0x00020000);
  gu64* flags = (gu64*)((char*)ws + flo);
  auto get_tag = [&]() __attribute__((always_inline)) { return (((u64)__float_as_uint(aread<13>()) << 32) | __float_as_uint(aread<12>())) * 0x9E3779B97F4A7C15ull + 0x632BE59BD9B4E019ull; };
  float t0, t1;
  f32x4 acc[2][2][2];
#define LANE_VALUES int tid_ = threadIdx.x; PIN(tid_); \
  const int tid = tid_, lane = tid & 63, n16 = lane & 15, r8 = lane >> 3, pos = lane & 7, G = lane >> 4, q4 = (lane >> 2) & 3, p4 = lane & 3; \
  const unsigned swz = pos ^ (((r8 >> 1) & 3) << 1), l16 = lane * 16, vflag = tid == 0 ? 0u : OOB, wa = rt * 72 + w * 18 + lane, wm = lane < 18 ? 0 : OOB; \
  (void)q4; (void)p4;

  if (!attn) {
    using SC = Sched<false>;
    LANE_VALUES
    auto rd_a = [&](auto Sc, auto U) {
      constexpr int s = Sc, u = U;
      if constexpr (s < SC::NSTEP && !SC::skip(s) && !(u == 1 && s >= 864 && SC::a_ks(s) >= 8)) {
        constexpr int sl = SC::a_stage(s) % 6, ks = SC::a_ks(s);
        (u ? wu : wl)[s % 6] = rdhalf(rb[2 * SC::a_tile(s) + (ks & 1)][sl >> 1] + (sl & 1) * SLOT + (ks >> 1) * 4096 + u * 2048);
      }
    };
    auto step1 = [&](auto Sc) {
      constexpr int s = Sc, k = s % 36, c = SC::a_tile(s), o = k & 1;
      if constexpr (!SC::skip(s)) mfma_vv<k < 2>(acc[c][o][o], cat(wl[s % 6], wu[s % 6]), xf[(k & ~1) + o]);
      rd_a(IC(s + 5), IC(0));
      SB();
      if constexpr (!SC::skip(s)) mfma_vv<k < 2>(acc[c][o][o ^ 1], cat(wl[s % 6], wu[s % 6]), xf[(k & ~1) + (o ^ 1)]);
      rd_a(IC(s + 5), IC(1));
    };
    auto sync = [&](auto Sc) {
      constexpr int s = Sc, k = SC::warm_k(s);
      if constexpr (SC::need(s) > -25) waitv<SC::allow(s)>();
      if constexpr (s == 18) WAITL0();
      if constexpr (SC::bar(s)) BAR();
      if constexpr (SC::mark_k(s) >= 0) mark<0, SC::mark_k(s), SC::NMARK>();
      if constexpr (k >= 0 && k < 2) warm(((wa >> 1) * 9216 + (wa & 1) * 128) | wm, srdA, f0 * 2 + 256 + 256 * k);
      if constexpr (k >= 2) warm((wa * 128) | wm, srdW2, f0 * 1152 + (k - 2) * 147456);
    };
    va -= 128;
    u64 tag;
    const unsigned voffB = r8 * 1152 + swz * 16;
    const int c1 = cg == 2 ? 0 : cg + 1, c2 = cg == 0 ? 2 : cg - 1;
    unsigned soB[3];
    soB[0] = (f0 + 48 * w) * 1152 + 384 * cg; soB[1] = ((3 * kg + c1) * 384 + 48 * w) * 1152 + 384 * cg; soB[2] = ((3 * kg + c2) * 384 + 48 * w) * 1152 + 384 * cg;
    const int p5 = w == 3 ? 13 : 5 * w;
    unsigned soO = (144 * kg + 8 * p5) * 1152 + 384 * cg, m5 = p5 * 1024;
    unsigned hso[3];
    hso[0] = H_OFF + ((rt * 12 + fs) * 4 + w) * 24576; hso[1] = H_OFF + ((rt * 12 + 3 * kg + c1) * 4 + w) * 24576; hso[2] = H_OFF + ((rt * 12 + 3 * kg + c2) * 4 + w) * 24576;
    const unsigned oso = O_OFF + ((rt * 4 + kg) * 4 + w) * 9216;
    const unsigned pso = P_OFF + (((rt * 3 + cg) * 4 + kg) * 4 + w) * 12288;
    const unsigned vpoll = (3 * kg + ((lane & 1) ? c1 : c2)) * 128;
    LAS char* tab = lds + TAB_OFF + G * 16;
    f32x4 bs[2];
    f32x2 rs2[2] = {f32x2{rstd[0], rstd[0]}, f32x2{rstd[1], rstd[1]}};
    if (EPI == 2) { PIN(rs2[0]); PIN(rs2[1]); }
    f32x4 ac[6][2][2];
    unsigned e[8];
    u64 pollv = 0;
    const unsigned ov = n16 * 2304 + G * 8;
    unsigned os[3];
#pragma unroll
    for (int k = 0; k < 3; ++k) { const int un = 3 * kg + k; os[k] = (tokbase + 64 * w + 32 * (un & 1)) * 1152 + (192 * cg + 32 * (un >> 1)) * 2; }
    u32x2 xr[3][2];
    auto epi2 = [&](auto U, auto Tc) {
      constexpr int u = U, T = Tc;
      if constexpr (u >= 3 && u < 7) {
        constexpr int d = 2 * (u - 3);
        const f32x4& a = ac[T][(u - 3) & 1][(u - 3) >> 1];
        e[d] = cvtpk(a[0], a[1]); e[d + 1] = cvtpk(a[2], a[3]);
        PIN(e[d]); PIN(e[d + 1]);
      }
      if constexpr (u == 5) __builtin_amdgcn_raw_buffer_store_b128(u32x4{e[0], e[1], e[2], e[3]}, rws, l16 + (T & 1) * 2048, pso + (T >> 1) * 4096, 16);
      if constexpr (u == 7) __builtin_amdgcn_raw_buffer_store_b128(u32x4{e[4], e[5], e[6], e[7]}, rws, l16 + (T & 1) * 2048 + 1024, pso + (T >> 1) * 4096, 16);
    };
    sfor<77>([&](auto So) { sfor<12>([&](auto Si) {
      constexpr int s = 12 * So + Si;
      if constexpr (s == 432 && PADM) PADTO(PADM);
      if constexpr (PAIRW && s % 2 == 0) __builtin_amdgcn_s_waitcnt(0xC07F | (6 << 8));
      if constexpr (s < 432) step1(IC(s));
      else {
        constexpr int n = SC::a_stage(s) - 18, kb = n / 3, T = 2 * (n % 3) + SC::a_tile(s), ks = SC::a_ks(s), o = ks & 1, ar = kb_areg(kb) + 8 * (ks >> 1);
        if constexpr (kb == 6 && ks >= 8) {
          mfma_k16<ar + 2 * o>(ac[T][o][o], wl[s % 6]); rd_a(IC(s + 5), IC(0)); SB();
          mfma_k16<ar + 2 * (o ^ 1)>(ac[T][o][o ^ 1], wl[s % 6]); rd_a(IC(s + 5), IC(1));
        } else {
          const bf16x8 a = cat(wl[s % 6], wu[s % 6]);
          mfma_va<kb == 0 && ks < 2, ar + 4 * o>(ac[T][o][o], a); rd_a(IC(s + 5), IC(0)); SB();
          mfma_va<kb == 0 && ks < 2, ar + 4 * (o ^ 1)>(ac[T][o][o ^ 1], a); rd_a(IC(s + 5), IC(1));
        }
      }
      sync(IC(s));
      if constexpr (s % 24 == 0) STAGET(s / 24);
      if constexpr (s == SC::PUB_HA || s == SC::PUB_HB) bstore8_sc1(tag, vflag, srdws, flo + ((s == SC::PUB_HA ? FL_HA : FL_HB) + fs) * 128);
      if constexpr (s == SC::CHK_HA || s == SC::CHK_HB || s == SC::CHK_O) {
        PIN(pollv);
        if (__builtin_expect(!__all(pollv == tag), 0))
          spin_until((gu64*)((char*)flags + (s == SC::CHK_O ? (FL_O + kg) * 128 : (s == SC::CHK_HA ? FL_HA : FL_HB) * 128 + vpoll)), tag);
      }
      if constexpr (s == 6) { const unsigned r = __float_as_uint(aread<14>()); *(LAS f32x2*)(lds + TAB_OFF + tid * 8) = f32x2{bflo(r), bfhi(r)}; }
      if constexpr (s == 10) { const unsigned r = __float_as_uint(aread<15>()); *(LAS f32x2*)(lds + TAB_OFF + 2048 + tid * 8) = f32x2{bflo(r), bfhi(r)}; }
      if constexpr (s == 14) { tag = get_tag(); PIN(tag); }
      if constexpr (SC::g_stage(s) >= 0) {
        constexpr int np = SC::g_stage(s), p = SC::g_piece(s), sl = np % 6;
        if constexpr (np < 18) glds16c<sl * SLOT + p * 1024, 8 * p * 9216 + 128 * (np / 3)>(m6, va, srdA, soA[np % 3]);
        else if constexpr (np < 36) {
          constexpr int kb = (np - 18) / 3, src = kb < 2 ? 0 : 1 + (kb & 1), half = kb < 2 ? kb : (kb >= 4);
          glds16c<sl * SLOT + p * 1024, (192 * half + 8 * p) * 1152 + 128 * ((np - 18) % 3)>(m6, voffB, srdW2, soB[src]);
        } else glds16c<sl * SLOT + p * 1024, 8 * p * 1152 + 128 * (np - 36)>(m5, voffB, srdWo, soO);
      }
      if constexpr (s >= 32 && s < 436 && (s % 36 == 32 || s % 36 == 0)) { constexpr int T = (s + 4) / 36 - 1, k = s % 36 == 0; bs[k] = *(LAS f32x4*)(tab + T * 128 + k * 64); }
      if constexpr (s >= 36 && s < 468) {
        constexpr int T = s / 36 - 1, v = s % 36, d = (v - 2) / 4;
        if constexpr (v >= 2 && v <= 32) {
          const f32x4& a = acc[T & 1][(d >> 1) & 1][d >> 2];
          if constexpr (v % 4 == 2) hp_a(t0, t1, a[2 * (d & 1)], a[2 * (d & 1) + 1], rstd[d >> 2], bs[(d >> 1) & 1][2 * (d & 1)], bs[(d >> 1) & 1][2 * (d & 1) + 1]);
          if constexpr (v % 4 == 3) hp_b(t0, t1);
          if constexpr (v % 4 == 0) e[d] = hp_c<A_H + 8 * T + d>(t0, t1);
        }
        if constexpr (SC::h_store(s) >= 0) { constexpr int k = SC::h_store(s); __builtin_amdgcn_raw_buffer_store_b128(u32x4{e[4 * k], e[4 * k + 1], e[4 * k + 2], e[4 * k + 3]}, rws, l16 + (T & 1) * 2048 + k * 1024, hso[0] + (T >> 1) * 4096, 16); }
      }
      if constexpr (s >= 874) epi2(IC((s - 864) % 10), IC((s - 864) / 10 - 1));
      if constexpr (SC::x_load(s) >= 0) { constexpr int k = SC::x_load(s); xr[k >> 1][k & 1] = bload8(ov, srdx, os[k >> 1] + 32 * (k & 1)); }
      if constexpr (s == SC::POLL_HA || s == SC::POLL_HB) pollv = bload8_sc1(vpoll, srdws, flo + (s == SC::POLL_HA ? FL_HA : FL_HB) * 128);
      if constexpr (s == SC::POLL_O) pollv = bload8_sc1(0, srdws, flo + (FL_O + kg) * 128);
      if constexpr (SC::o_load(s) >= 0) { constexpr int f = SC::o_load(s); aload16_sc1<kb_areg(6) + 4 * f, (f & 3) * 1024>(l16, srdws, oso + (f >> 2) * 4096); }
      if constexpr (SC::h_load(s) >= 0) { constexpr int b = SC::h_load(s) / 12, f = SC::h_load(s) % 12; aload16_sc1<kb_areg(2 + b) + 4 * f, (f & 3) * 1024>(l16, srdws, hso[1 + (b & 1)] + (b >> 1) * 12288 + (f >> 2) * 4096); }
      SB();
    }); });
    asm volatile("s_nop 7" : "+v"(ac[5][0][0]), "+v"(ac[5][0][1]), "+v"(ac[5][1][0]), "+v"(ac[5][1][1]));
    sfor<8>([&](auto U) { epi2(U, IC(5)); });
    if (PADE) PADTO(PADE);
    mark<0, 13, 14>();
    asm volatile("s_waitcnt vmcnt(0)" : "+v"(xr[0][0]), "+v"(xr[0][1]), "+v"(xr[1][0]), "+v"(xr[1][1]), "+v"(xr[2][0]), "+v"(xr[2][1]) :: "memory");
    BAR();
    bstore8_sc1(tag, vflag, srdws, flo + (FL_FIN + fs) * 128);
    STAMPT(5);
    f32x4 cr[3][2];
#pragma unroll
    for (int k = 0; k < 3; ++k)
#pragma unroll
      for (int j = 0; j < 2; ++j) { cr[k][j] = *(LAS f32x4*)(tab + 2048 + 128 * ((3 * kg + k) >> 1) + 64 * j); }
    spin_until(flags + (FL_FIN + 3 * (lane & 3) + cg) * 16, tag);
    if (fs == 0) bstore8_sc1(tag, vflag, srdws, flo + FL_EPOCH * 128);
    const __amdgpu_buffer_rsrc_t rout = __builtin_amdgcn_make_buffer_rsrc(out, 0, S_ * D_ * 2, 0x00020000);
    u32x4 v[3][4];
#pragma unroll
    for (int k = 0; k < 3; ++k)
#pragma unroll
      for (int s = 0; s < 4; ++s) v[k][s] = __builtin_amdgcn_raw_buffer_load_b128(rws, l16, P_OFF + (((rt * 3 + cg) * 4 + s) * 4 + w) * 12288 + (3 * kg + k) * 1024, 16);
#pragma unroll
    for (int k = 0; k < 3; ++k) {
      float a[8];
#pragma unroll
      for (int j = 0; j < 4; ++j) { a[2 * j] = bflo(xr[k][j >> 1][j & 1]) + cr[k][j >> 1][(2 * j) & 3]; a[2 * j + 1] = bfhi(xr[k][j >> 1][j & 1]) + cr[k][j >> 1][(2 * j + 1) & 3]; }
#pragma unroll
      for (int s = 0; s < 4; ++s)
#pragma unroll
        for (int j = 0; j < 4; ++j) { a[2 * j] += bflo(v[k][s][j]); a[2 * j + 1] += bfhi(v[k][s][j]); }
      __builtin_amdgcn_raw_buffer_store_b64(u32x2{cvtpk(a[0], a[1]), cvtpk(a[2], a[3])}, rout, ov, os[k], OAUX);
      __builtin_amdgcn_raw_buffer_store_b64(u32x2{cvtpk(a[4], a[5]), cvtpk(a[6], a[7])}, rout, ov + 32, os[k], OAUX);
    }
    STAMPT(6);
    STAMPOUT();
    STAGEOUT();
    asm volatile(".Lk_end0:");
    __builtin_amdgcn_endpgm();
  }
  {
    using SC = Sched<true>;
    LANE_VALUES
    auto va_of = [&](unsigned word) { return r8 * 3456 + (swz & 1) * 16 + ((word >> (8 * (swz >> 1))) & 255) * 16; };
    auto rd_a = [&](auto Sc, auto U) {
      constexpr int s = Sc, u = U;
      if constexpr (s < SC::NSTEP && !SC::skip(s) && !(u == 1 && s >= 864 && SC::a_ks(s) >= 8)) {
        constexpr int sl = SC::a_stage(s) % 6, ks = SC::a_ks(s);
        (u ? wu : wl)[s % 6] = rdhalf(rb[2 * SC::a_tile(s) + (ks & 1)][sl >> 1] + (sl & 1) * SLOT + (ks >> 1) * 4096 + u * 2048);
      }
    };
    auto step1 = [&](auto Sc) {
      constexpr int s = Sc, k = s % 36, c = SC::a_tile(s), o = k & 1;
      if constexpr (!SC::skip(s)) mfma_vv<k < 2>(acc[c][o][o], cat(wl[s % 6], wu[s % 6]), xf[(k & ~1) + o]);
      rd_a(IC(s + 5), IC(0));
      SB();
      if constexpr (!SC::skip(s)) mfma_vv<k < 2>(acc[c][o][o ^ 1], cat(wl[s % 6], wu[s % 6]), xf[(k & ~1) + (o ^ 1)]);
      rd_a(IC(s + 5), IC(1));
    };
    auto sync = [&](auto Sc) {
      constexpr int s = Sc, k = SC::warm_k(s);
      if constexpr (SC::need(s) > -25) waitv<SC::allow(s)>();
      if constexpr (s == 18) WAITL0();
      if constexpr (SC::bar(s)) BAR();
      if constexpr (SC::mark_k(s) >= 0) mark<1, SC::mark_k(s), SC::NMARK>();
      if constexpr (k >= 0 && k < 4) {
        const unsigned la = (288 * kg + (qs[k < 3 ? 2 + k : 5] & 255) * 16) & ~127u, lb = (288 * kg + (k < 3 ? qs[2 + k] >> 24 : qs[6] & 255) * 16) & ~127u;
        warm(((wa >> 1) * 3456 + ((wa & 1) ? lb : la)) | wm, srdA, 0);
      }
      if constexpr (k == 4) warm(((rt * 81 + w * 21 + lane) * 128) | (lane < 21 ? 0 : OOB), srdWo, 144 * kg * 1152);
    };
    f32x4 gram;
    float rstdh, negmuh;
    const unsigned hrow = n16 < 8 ? n16 : 0;
    auto halo_val = [&](auto I) {
      f32x4 r;
      sfor<4>([&](auto K) { const float a = aread<A_HH + 4 * (I - 2) + K>(); r[(int)K] = fmaf(ror8(a), negmuh, a) * rstdh; });
      return r;
    };
    auto halo_col = [&](auto I) { return ((qd[I] >> (8 * w)) & 255); };
    bf16x8 wh[2];
    LAS char* hb[3];
#pragma unroll
    for (int i = 0; i < 3; ++i) { hb[i] = (LAS char*)(uintptr_t)((unsigned)(uintptr_t)rb[0][i] ^ (w << 5)); PIN(hb[i]); }
    const unsigned ha = (unsigned)(uintptr_t)lds + TAB_OFF + hrow * 128 + G * 8;
    const unsigned qrow = (unsigned)(uintptr_t)lds + QK_OFF + (32 * w + n16 + 8) * QK_RS + G * 8, vrow = (unsigned)(uintptr_t)lds + (32 * w + n16 + 8) * V_RS + G * 16;
    sfor<28>([&](auto So) { sfor<18>([&](auto Si) {
      constexpr int s = 18 * So + Si, i = s / 72, u = s % 72;
      step1(IC(s));
      if constexpr (s == 480) WAITL0();
      sync(IC(s));
      if constexpr (s % 24 == 0 && s < 432) STAGET(s / 24);
      if constexpr (s == 2) *(LAS u32x4*)(lds + TAB_OFF + w * 3072 + (r8 < 3 ? r8 : 0) * 1024 + pos * 16) = u32x4{0x3f803f80u, 0x3f803f80u, 0x3f803f80u, 0x3f803f80u};
      if constexpr (s >= 22 && s < 94 && s % 4 == 2) { constexpr int sg = (s - 22) / 4; ds_read_a64<A_HX + 4 * sg, (sg >> 1) * 1024 + (sg & 1) * 64>(ha); ds_read_a64<A_HX + 4 * sg + 2, (sg >> 1) * 1024 + (sg & 1) * 64 + 32>(ha); }
      if constexpr (u % 4 == 2) {
        constexpr int sg = u / 4;
        if constexpr (s >= 46 && s <= 114) mfma16_aa<s == 46, A_HX + s - 46>(gram);
        if constexpr (s == 122) {
          const float dg = (p4 == 0) ? gram[0] : (p4 == 1) ? gram[1] : (p4 == 2) ? gram[2] : gram[3];
          rstdh = G == q4 ? dg : 0.f; negmuh = G == 2 ? gram[0] : 0.f;
          PIN(rstdh); PIN(negmuh);
        }
        if constexpr (s == 126) { rstdh = xor16(rstdh); negmuh = xor16(negmuh); PIN(rstdh); PIN(negmuh); }
        if constexpr (s == 130) { rstdh = xor32(rstdh); negmuh = xor32(negmuh); PIN(rstdh); PIN(negmuh); }
        if constexpr (s == 134) {
          negmuh *= -1.0f / D_;
          rstdh = __builtin_amdgcn_rsqf(fmaxf(rstdh * (1.0f / D_) - negmuh * negmuh, 0.f) + 1e-5f);
          PIN(rstdh); PIN(negmuh);
        }
        if constexpr (i >= 2) mfma16_ava<sg == 0, A_HH + 4 * (i - 2), A_HX + 4 * sg>(wh[sg & 1]);
        constexpr int s2 = s + 4, sl = (3 * (s2 / 72) + (s2 % 72) / 24) % 6;
        if constexpr (s2 >= 144 && s2 < 504) { LAS char* p = hb[sl >> 1] + (sl & 1) * SLOT + (((s2 % 72) / 4) % 6) * 4096; wh[(sg + 1) & 1] = cat(rdhalf(p), rdhalf(p + 2048)); }
      }
      if constexpr (u == 47 && i < 5) { va = va_of(qs[i + 2]); PIN(va); }
      if constexpr (SC::g_stage(s) >= 0) { constexpr int np = SC::g_stage(s), p = SC::g_piece(s); glds16c<(np % 6) * SLOT + p * 1024, 8 * p * 3456>(m6, va, srdA, soA[np % 3]); }
      if constexpr (s >= 36) {
        constexpr int T = s / 36 - 1, v = s % 36, d = (v - 3) / 4, p = 2 * T + (d >> 2), tb = (d >> 1) & 1, hf = d & 1;
        if constexpr (v >= 3 && v <= 32 && p != 7) {
          const f32x4& a = acc[T & 1][d >> 2][tb];
          if constexpr (v % 4 == 3) sp_a(t0, t1, a[2 * hf], a[2 * hf + 1], rstd[tb]);
          if constexpr (v % 4 == 0) { if constexpr (p < 19) (void)hp_c<A_Q + 4 * p + 2 * tb + hf>(t0, t1); else awrite2<A_V + 8 * (p - 19) + 4 * tb + 2 * hf>(t0, t1); }
        }
      }
      if constexpr (s > 432 && s < 471 && s % 2 == 1 && s != 447) {
        constexpr int p = (s - 433) / 2;
        const unsigned ad = qrow + ((qd[p / 4] >> (8 * (p % 4))) & 255) * 16;
        ds_write_a64<A_Q + 4 * p, 0>(ad); ds_write_a64<A_Q + 4 * p + 2, 16 * QK_RS>(ad);
      }
      if constexpr (s == 471 || s == 473 || s == 477) {
        constexpr int hi = s == 471 ? 2 : s == 473 ? 3 : 4;
        const f32x4 r = halo_val(IC(hi));
        const unsigned row = (hi == 4 && w == 3) ? 0 : hrow;
        *(LAS u32x2*)(lds + QK_OFF + row * QK_RS + halo_col(IC(hi)) * 16 + G * 8) = u32x2{cvtpk(r[0], r[1]), cvtpk(r[2], r[3])};
      }
      SB();
    }); });
    asm volatile("s_nop 7" : "+v"(acc[1][0][0]), "+v"(acc[1][0][1]), "+v"(acc[1][1][0]), "+v"(acc[1][1][1]));
    WAITL0(); BAR();
    STAMPT(3);
    {
      const int hd = lane & 7, i0 = 32 * w + 4 * r8;
      LAS char* rp[11];
#pragma unroll
      for (int jj = 0; jj < 11; ++jj) { rp[jj] = lds + QK_OFF + (i0 + 1 + jj) * QK_RS + hd * 36; PIN(rp[jj]); }
      unsigned qv[4][9], kv[11][9];
#pragma unroll
      for (int a = 0; a < 4; ++a)
#pragma unroll
        for (int c = 0; c < 9; ++c) qv[a][c] = *(LAS unsigned*)(rp[7 + a] + c * 4);
#pragma unroll
      for (int jj = 0; jj < 4; ++jj)
#pragma unroll
        for (int c = 0; c < 9; ++c) kv[jj][c] = *(LAS unsigned*)(rp[jj] + 288 + c * 4);
      {
#pragma unroll
        for (int k = 0; k < 4; ++k)
          *(LAS f32x4*)(uintptr_t)(vrow + ((qd[6] >> (16 + 8 * (k >> 1))) & 255) * 32 + (k & 1) * 16 * V_RS) = acc[1][k >> 1][k & 1] * rstd[k & 1];
        sfor<3>([&](auto I) {
          constexpr int hi = I + 4;
          const unsigned row = (hi == 4 && w != 3) ? 0 : hrow, col = (hi == 4 && w != 3) ? 0 : halo_col(IC(hi));
          *(LAS f32x4*)(lds + row * V_RS + col * 32 + G * 16) = halo_val(IC(hi));
        });
      }
      SB();
      float lg[4][8];
      sfor<8>([&](auto U) {
        constexpr int u = U;
        if constexpr (u + 4 < 11) {
#pragma unroll
          for (int c = 0; c < 9; ++c) kv[u + 4][c] = *(LAS unsigned*)(rp[u + 4] + 288 + c * 4);
        }
        if constexpr (u < 7) {
          constexpr int p = 19 + u;
          const unsigned ad = vrow + ((qd[p / 4] >> (8 * (p % 4))) & 255) * 32;
          ds_write_a128<A_V + 8 * u, 0>(ad); ds_write_a128<A_V + 8 * u + 4, 16 * V_RS>(ad);
        }
#pragma unroll
        for (int a = 0; a < 4; ++a) lg[a][u] = 0.f;
#pragma unroll
        for (int c = 0; c < 9; ++c)
#pragma unroll
          for (int a = 0; a < 4; ++a) lg[a][u] = dot2(qv[a][c], kv[a + u][c], lg[a][u]);
        SB();
      });
      WAITL0(); BAR();
      mark<1, 10, 13>();
      STAMPT(4);
      LAS char* vp = lds + (i0 + 1) * V_RS + hd * 72;
      f32x2 vv[11][9];
#pragma unroll
      for (int jj = 0; jj < 3; ++jj)
#pragma unroll
        for (int c = 0; c < 9; ++c) vv[jj][c] = *(volatile LAS f32x2*)(vp + jj * V_RS + c * 8);
      if (rt < 2) {
#pragma unroll
        for (int a = 0; a < 4; ++a)
#pragma unroll
          for (int u = 0; u < 7 - a; ++u) lg[a][u] = (i0 + a + u < 7) ? -1e9f : lg[a][u];
      }
      constexpr float CS = 0.23570226039551584f * 1.4426950408889634f;
#pragma unroll
      for (int a = 0; a < 4; ++a) {
        float mx = lg[a][0];
#pragma unroll
        for (int u = 1; u < 8; ++u) mx = fmaxf(mx, lg[a][u]);
        const float nb = -mx * CS;
        float sum = 0.f;
#pragma unroll
        for (int u = 0; u < 8; ++u) { lg[a][u] = __builtin_amdgcn_exp2f(fmaf(lg[a][u], CS, nb)); sum += lg[a][u]; }
        const float inv = __builtin_amdgcn_rcpf(sum);
#pragma unroll
        for (int u = 0; u < 8; ++u) lg[a][u] *= inv;
      }
      SB();
      f32x2 o[4][9];
      sfor<11>([&](auto J) {
        constexpr int jj = J;
        if constexpr (jj + 3 < 11) {
#pragma unroll
          for (int c = 0; c < 9; ++c) vv[jj + 3][c] = *(volatile LAS f32x2*)(vp + (jj + 3) * V_RS + c * 8);
        }
#pragma unroll
        for (int c = 0; c < 9; ++c) {
#pragma unroll
          for (int a = 0; a < 4; ++a) {
            if (jj == a) o[a][c] = vv[jj][c] * lg[a][0];
            else if (jj > a && jj <= a + 7) o[a][c] = __builtin_elementwise_fma(vv[jj][c], f32x2{lg[a][jj - a], lg[a][jj - a]}, o[a][c]);
          }
        }
        if constexpr (jj == 4) mark<1, 11, 13>();
        if constexpr (jj >= 7) {
#pragma unroll
          for (int c = 0; c < 9; ++c) *(LAS unsigned*)(rp[jj] + c * 4) = cvtpk(o[jj - 7][c][0], o[jj - 7][c][1]);
        }
        SB();
      });
    }
    mark<1, 12, 13>();
    STAMPT(5);
    {
      LAS char* rowp = (LAS char*)(uintptr_t)qrow;
      const unsigned oso = O_OFF + ((rt * 4 + kg) * 4 + w) * 9216;
#pragma unroll
      for (int f = 0; f < 9; ++f) {
        const u32x2 a = *(LAS u32x2*)(rowp + (f >> 1) * 64 + (f & 1) * 16 * QK_RS), b = *(LAS u32x2*)(rowp + (f < 8 ? (f >> 1) * 64 + (f & 1) * 16 * QK_RS + 32 : 256 + 16 * QK_RS));
        __builtin_amdgcn_raw_buffer_store_b128(u32x4{a[0], a[1], b[0], b[1]}, rws, l16, oso + f * 1024, 16);
      }
    }
    WAITV(0); BAR();
    bstore8_sc1(get_tag(), vflag, srdws, flo + (FL_O + kg) * 128);
    STAMPT(6);
  }
  STAMPOUT();
  STAGEOUT();
  asm volatile(".Lk_end1:");
}

extern "C" void kernel_launch(void* const* d_in, const int* in_sizes, int n_in, void* d_out, int out_size, void* d_ws, size_t ws_size, hipStream_t stream) {
  (void)hipFuncSetAttribute((const void*)main_k, hipFuncAttributeMaxDynamicSharedMemorySize, LDS_BYTES);
  const Args args = {d_in[0], d_in[1], d_in[2], d_in[4], d_in[5], d_in[6], d_in[7], d_out, d_ws, make_tab()};
  main_k<<<256, 256, LDS_BYTES, stream>>>(args);
}
```

```cpp
#include <hip/hip_runtime.h>
#include <stdint.h>
#include <utility>

typedef __bf16 bf16x8 __attribute__((ext_vector_type(8)));
typedef __bf16 bf16x4 __attribute__((ext_vector_type(4)));
typedef __bf16 bf16x2 __attribute__((ext_vector_type(2)));
typedef float f32x16 __attribute__((ext_vector_type(16)));
typedef float f32x4 __attribute__((ext_vector_type(4)));
typedef float f32x2 __attribute__((ext_vector_type(2)));
typedef unsigned u32x4 __attribute__((ext_vector_type(4)));
typedef unsigned u32x2 __attribute__((ext_vector_type(2)));
typedef unsigned u32x16 __attribute__((ext_vector_type(16)));
typedef unsigned long long u64;
#define LAS __attribute__((address_space(3)))
#define GAS __attribute__((address_space(1)))
typedef GAS u64 gu64;
#define DI __device__ __forceinline__
#define SB() __builtin_amdgcn_sched_barrier(0)
#define IC(n) std::integral_constant<int, (n)>{}
#define RLX_AGENT __ATOMIC_RELAXED, __HIP_MEMORY_SCOPE_AGENT

template <class F, int... I> DI void sfor_(F&& f, std::integer_sequence<int, I...>) { (f(std::integral_constant<int, I>{}), ...); }
template <int N, class F> DI void sfor(F&& f) { sfor_(f, std::make_integer_sequence<int, N>{}); }

constexpr int S_ = 2048, D_ = 576, F_ = 4608;
constexpr int SLOT = 24576;
constexpr int TAB_OFF = 147456;
constexpr int LDS_BYTES = 163840;
constexpr int QK_OFF = 80512, QK_RS = 584, V_RS = 592;
constexpr unsigned H_OFF = 0, O_OFF = 20u << 20, P_OFF = 24u << 20, FLAG_OFF = 40u << 20, WS_BYTES = 41u << 20;
constexpr unsigned OOB = 0x40000000u;
constexpr int FL_HA = 0, FL_HB = 12, FL_O = 24, FL_FIN = 28, FL_EPOCH = 40, FL_N = 41;
constexpr int A_H = 56;
constexpr int A_Q = 16;
constexpr int A_V = 92;
constexpr int A_HH = 164;
constexpr int A_HX = 184;

__host__ DI int map_role(int bid) { return (bid >> 3) & 3; }
__host__ DI int map_kg(int bid) { return bid & 3; }
__host__ DI int map_rt(int bid) { return ((bid >> 2) & 1) * 8 + (bid >> 5); }
__host__ DI int map_sp(int bid) { return map_role(bid) == 3 ? 12 + map_kg(bid) : 3 * map_kg(bid) + map_role(bid); }

#define GAUX ""
#define XAUX ""
#define WAUX ""
DI u32x4 make_srd(const void* p, unsigned bytes) {
  uint64_t a = (uint64_t)p;
  u32x4 r; r[0] = (unsigned)a; r[1] = (unsigned)(a >> 32) & 0xffffu; r[2] = bytes; r[3] = 0x00020000u;
  return r;
}
DI void glds16(unsigned m0v, unsigned voff, u32x4 srd, unsigned soff) {
  asm volatile("s_mov_b32 m0, %0\n\ts_nop 0\n\tbuffer_load_dwordx4 %1, %2, %3 offen lds" :: "s"(m0v), "v"(voff), "s"(srd), "s"(soff) : "memory");
}
template <unsigned MC, unsigned SC> DI void glds16c(unsigned mb, unsigned voff, u32x4 srd, unsigned sb) {
  unsigned tmp;
  asm volatile("s_add_u32 m0, %1, %5\n\ts_add_u32 %0, %4, %6\n\tbuffer_load_dwordx4 %2, %3, %0 offen" GAUX " lds"
               : "=&s"(tmp) : "s"(mb), "v"(voff), "s"(srd), "s"(sb), "i"(MC), "i"(SC) : "memory", "scc");
}
template <unsigned C> DI void set_m0(unsigned b) { asm volatile("s_add_u32 m0, %0, %1" :: "s"(b), "i"(C) : "scc"); }
template <unsigned C> DI unsigned sadd(unsigned b) { unsigned r; asm volatile("s_add_u32 %0, %1, %2" : "=s"(r) : "s"(b), "i"(C) : "scc"); return r; }
template <int J> DI void glds3(unsigned v, u32x4 srd, unsigned so) { asm volatile("buffer_load_dwordx4 %0, %1, %2 offen offset:%c3 lds" :: "v"(v), "s"(srd), "s"(so), "i"(J * 1024) : "memory"); }
template <unsigned MC> DI void glds16r(unsigned mb, unsigned voff, u32x4 srd, unsigned& so, unsigned step) {
  asm volatile("s_add_u32 m0, %1, %5\n\ts_add_u32 %0, %0, %4\n\tbuffer_load_dwordx4 %2, %3, %0 offen lds"
               : "+s"(so) : "s"(mb), "v"(voff), "s"(srd), "s"(step), "i"(MC) : "memory", "scc");
}
constexpr int x_base(int t) { return (t / 3 == 1 ? 2 : 4) * SLOT + (t % 3 == 2 ? SLOT : 0); }
template <int T> DI void x_line(unsigned m6, unsigned vx0, unsigned vx1, u32x4 srd, unsigned s0, unsigned s1, unsigned s2, unsigned s3) {
  constexpr int d0 = x_base(T) + (T % 3 == 1 ? 4096 : 0) - (T % 3) * 128, d2 = T % 3 == 1 ? SLOT - 1024 : 1024;
  asm volatile("s_add_u32 m0, %0, %8\n\ts_nop 0\n\tbuffer_load_dwordx4 %1, %3, %4 offen offset:%c10" XAUX " lds\n\t"
               "s_addk_i32 m0, 0x400\n\ts_nop 0\n\tbuffer_load_dwordx4 %2, %3, %5 offen offset:%c10" XAUX " lds\n\t"
               "s_addk_i32 m0, %c9\n\ts_nop 0\n\tbuffer_load_dwordx4 %1, %3, %6 offen offset:%c10" XAUX " lds\n\t"
               "s_addk_i32 m0, 0x400\n\ts_nop 0\n\tbuffer_load_dwordx4 %2, %3, %7 offen offset:%c10" XAUX " lds"
               :: "s"(m6), "v"(vx0), "v"(vx1), "s"(srd), "s"(s0), "s"(s1), "s"(s2), "s"(s3), "i"(d0), "i"(d2), "i"((T % 3) * 128) : "memory", "scc");
}
DI u32x4 bload16(unsigned voff, u32x4 srd, unsigned soff) {
  u32x4 r;
  asm volatile("buffer_load_dwordx4 %0, %1, %2, %3 offen" : "=v"(r) : "v"(voff), "s"(srd), "s"(soff) : "memory");
  return r;
}
DI unsigned bload4(unsigned voff, u32x4 srd, unsigned soff) {
  unsigned r;
  asm volatile("buffer_load_dword %0, %1, %2, %3 offen" : "=v"(r) : "v"(voff), "s"(srd), "s"(soff) : "memory");
  return r;
}
template <int OFF> DI u32x2 bload8(unsigned voff, u32x4 srd, unsigned soff) {
  u32x2 r;
  asm volatile("buffer_load_dwordx2 %0, %1, %2, %3 offen offset:%c4" : "=v"(r) : "v"(voff), "s"(srd), "s"(soff), "i"(OFF) : "memory");
  return r;
}
DI void warm(unsigned voff, u32x4 srd, unsigned soff) {
  asm volatile("buffer_load_dword a11, %0, %1, %2 offen" WAUX :: "v"(voff), "s"(srd), "s"(soff) : "memory");
}
DI u64 bload8_sc1(unsigned voff, u32x4 srd, unsigned soff) {
  u64 r;
  asm volatile("buffer_load_dwordx2 %0, %1, %2, %3 offen sc1" : "=v"(r) : "v"(voff), "s"(srd), "s"(soff) : "memory");
  return r;
}
template <int AR, int OFF> DI void aload16_sc1(unsigned voff, u32x4 srd, unsigned soff) {
  asm volatile("buffer_load_dwordx4 a[%c3:%c4], %0, %1, %2 offen offset:%c5 sc1" :: "v"(voff), "s"(srd), "s"(soff), "i"(AR), "i"(AR + 3), "i"(OFF) : "memory");
}
template <int AR, int OFF> DI void aload16(unsigned voff, u32x4 srd) {
  asm volatile("buffer_load_dwordx4 a[%c2:%c3], %0, %1, 0 offen offset:%c4" :: "v"(voff), "s"(srd), "i"(AR), "i"(AR + 3), "i"(OFF) : "memory");
}
template <int ROLE, int K, int N> DI void mark() { asm volatile("s_branch 1f\n\t.p2align 12\n.Lm%c0_%c1:\n\ts_setpc_b64 vcc\n1:" :: "i"(ROLE), "i"(K)); }
template <int R, int N0, int N1> DI void visit(unsigned sel) {
  constexpr auto c = [](int k, int n) { return k < n ? k : n - 1; };
  asm volatile("s_getpc_b64 vcc\n\ts_add_u32 s90, vcc_lo, %0\n\ts_addc_u32 s91, vcc_hi, 0\n\ts_add_u32 vcc_lo, vcc_lo, 52\n\ts_addc_u32 vcc_hi, vcc_hi, 0\n\ts_setpc_b64 s[90:91]\n\t"
               "s_branch .Lm0_%c1\n\ts_branch .Lm0_%c2\n\ts_branch .Lm0_%c3\n\ts_branch .Lm0_%c4\n\ts_branch .Lm1_%c5\n\ts_branch .Lm1_%c6\n\ts_branch .Lm1_%c7\n\ts_branch .Lm1_%c8"
               :: "s"(sel + 20), "i"(c(4 * R, N0)), "i"(c(4 * R + 1, N0)), "i"(c(4 * R + 2, N0)), "i"(c(4 * R + 3, N0)),
                  "i"(c(4 * R, N1)), "i"(c(4 * R + 1, N1)), "i"(c(4 * R + 2, N1)), "i"(c(4 * R + 3, N1)) : "vcc", "s90", "s91", "scc");
}
template <int N> DI void waitv() { asm volatile("s_waitcnt vmcnt(%c0)" :: "i"(N > 63 ? 63 : N) : "memory"); }
#define WAITV(n) asm volatile("s_waitcnt vmcnt(" #n ")" ::: "memory")
#define BAR() asm volatile("s_barrier" ::: "memory")
#define WAITL0() asm volatile("s_waitcnt lgkmcnt(0)" ::: "memory")
#define PIN(x) asm volatile("" : "+v"(x))
#define PINS(x) asm volatile("" : "+s"(x))

DI unsigned cvtpk(float lo, float hi) { f32x2 v = {lo, hi}; bf16x2 b = __builtin_convertvector(v, bf16x2); return __builtin_bit_cast(unsigned, b); }
DI float bflo(unsigned u) { return __uint_as_float(u << 16); }
DI float bfhi(unsigned u) { return __uint_as_float(u & 0xffff0000u); }
DI float dot2(unsigned a, unsigned b, float c) { return __builtin_amdgcn_fdot2_f32_bf16(__builtin_bit_cast(bf16x2, a), __builtin_bit_cast(bf16x2, b), c, false); }
DI float xor32(float v) { auto r = __builtin_amdgcn_permlane32_swap(__float_as_uint(v), __float_as_uint(v), false, false); return __uint_as_float(r[0]) + __uint_as_float(r[1]); }
DI float xor16(float v) { auto r = __builtin_amdgcn_permlane16_swap(__float_as_uint(v), __float_as_uint(v), false, false); return __uint_as_float(r[0]) + __uint_as_float(r[1]); }
DI float fmul_s(float a, float b) { float r; asm("v_mul_f32_e32 %0, %1, %2" : "=v"(r) : "v"(a), "v"(b)); return r; }

DI bf16x4 rdhalf(LAS char* p) { return __builtin_amdgcn_ds_read_tr16_b64_v4bf16((LAS bf16x4*)p); }
DI bf16x8 cat(bf16x4 a, bf16x4 b) { return __builtin_shufflevector(a, b, 0, 1, 2, 3, 4, 5, 6, 7); }
template <bool FIRST> DI void mfma_vv(f32x4& acc, bf16x8 a, u32x4 b) {
  if constexpr (FIRST) asm volatile("v_mfma_f32_16x16x32_bf16 %0, %1, %2, 0" : "=&v"(acc) : "v"(a), "v"(b));
  else                 asm volatile("v_mfma_f32_16x16x32_bf16 %0, %1, %2, %0" : "+v"(acc) : "v"(a), "v"(b));
}
DI void mfma_vvc(f32x4& acc, u32x4 a, u32x4 b, const f32x4& c) { asm volatile("v_mfma_f32_16x16x32_bf16 %0, %1, %2, %3" : "=&v"(acc) : "v"(a), "v"(b), "v"(c)); }
template <bool FIRST, int AR> DI void mfma_va(f32x4& acc, bf16x8 a) {
  if constexpr (FIRST) asm volatile("v_mfma_f32_16x16x32_bf16 %0, %1, a[%c2:%c3], 0" : "=&v"(acc) : "v"(a), "i"(AR), "i"(AR + 3));
  else                 asm volatile("v_mfma_f32_16x16x32_bf16 %0, %1, a[%c2:%c3], %0" : "+v"(acc) : "v"(a), "i"(AR), "i"(AR + 3));
}
template <int AR> DI void mfma_k16(f32x4& acc, bf16x4 a) { asm volatile("v_mfma_f32_16x16x16_bf16 %0, %1, a[%c2:%c3], %0" : "+v"(acc) : "v"(a), "i"(AR), "i"(AR + 1)); }
template <bool FIRST, int AR> DI void mfma16_aa(f32x4& acc) {
  if constexpr (FIRST) asm volatile("v_mfma_f32_16x16x32_bf16 %0, a[%c1:%c2], a[%c1:%c2], 0" : "=&v"(acc) : "i"(AR), "i"(AR + 3));
  else                 asm volatile("v_mfma_f32_16x16x32_bf16 %0, a[%c1:%c2], a[%c1:%c2], %0" : "+v"(acc) : "i"(AR), "i"(AR + 3));
}
template <bool FIRST, int AD, int AR> DI void mfma16_ava(bf16x8 a) {
  if constexpr (FIRST) asm volatile("v_mfma_f32_16x16x32_bf16 a[%c1:%c2], %0, a[%c3:%c4], 0" :: "v"(a), "i"(AD), "i"(AD + 3), "i"(AR), "i"(AR + 3));
  else                 asm volatile("v_mfma_f32_16x16x32_bf16 a[%c1:%c2], %0, a[%c3:%c4], a[%c1:%c2]" :: "v"(a), "i"(AD), "i"(AD + 3), "i"(AR), "i"(AR + 3));
}
template <int AR> DI float aread(void) { float r; asm volatile("v_accvgpr_read_b32 %0, a[%c1]" : "=v"(r) : "i"(AR)); return r; }
template <int AR, int OFF> DI void ds_write_a128(unsigned addr) { asm volatile("ds_write_b128 %0, a[%c1:%c2] offset:%c3" :: "v"(addr), "i"(AR), "i"(AR + 3), "i"(OFF) : "memory"); }
DI float ror8(float v) { return __int_as_float(__builtin_amdgcn_update_dpp(0, __float_as_int(v), 0x128, 0xf, 0xf, false)); }
template <int AR> DI void awrite(unsigned v) { asm volatile("v_accvgpr_write_b32 a[%c1], %0" :: "v"(v), "i"(AR)); }
DI float i_fma(float a, float b, float c) { float r; asm volatile("v_fma_f32 %0, %1, %2, %3" : "=v"(r) : "v"(a), "v"(b), "v"(c)); return r; }
DI float i_mul(float a, float b) { float r; asm volatile("v_mul_f32_e32 %0, %1, %2" : "=v"(r) : "v"(a), "v"(b)); return r; }
DI float i_sqs(float a) { float r; asm volatile("v_mul_f32_e64 %0, %1, |%1|" : "=v"(r) : "v"(a)); return r; }
DI unsigned i_cvt(float a, float b) { unsigned r; asm volatile("v_cvt_pk_bf16_f32 %0, %1, %2" : "=v"(r) : "v"(a), "v"(b)); return r; }
DI unsigned i_relu(unsigned a) { unsigned r; asm volatile("v_pk_max_i16 %0, %1, 0" : "=v"(r) : "v"(a)); return r; }
template <int OFF> DI void st16_sc1(u32x4 v, unsigned voff, u32x4 srd, unsigned soff) {
  asm volatile("buffer_store_dwordx4 %0, %1, %2, %3 offen offset:%c4 sc1" :: "v"(v), "v"(voff), "s"(srd), "s"(soff), "i"(OFF) : "memory");
}
template <int AR, int OFF> DI void ds_read_a64(unsigned addr) { asm volatile("ds_read_b64 a[%c1:%c2], %0 offset:%c3" :: "v"(addr), "i"(AR), "i"(AR + 1), "i"(OFF) : "memory"); }
DI void bstore8_sc1(u64 v, unsigned voff, u32x4 srd, unsigned soff) { asm volatile("buffer_store_dwordx2 %0, %1, %2, %3 offen sc1" :: "v"(v), "v"(voff), "s"(srd), "s"(soff) : "memory"); }
template <int AR, int OFF> DI void ds_write_a64(unsigned addr) { asm volatile("ds_write_b64 %0, a[%c1:%c2] offset:%c3" :: "v"(addr), "i"(AR), "i"(AR + 1), "i"(OFF) : "memory"); }

#define CYA 2
#define STAMPT(i)
#define STAMPOUT()
#define STAGET(n)
#define STAGEOUT()

#define WS0 0
#define WSP 48
#define V0 0
#define V1 1
#define V2 2
#define LA 7
#define WP1 4
#define WP2 2
constexpr int NB = LA + 1, BMID = (23 - LA) & ~1;
#define OAUX 0
#define DRAIN 1
#define WPRO 1
template <bool ATTN> struct Sched {
  static constexpr int P1 = ATTN ? 504 : 432, NSTEP = ATTN ? 504 : 924, NSTAGE = ATTN ? 21 : 39;
  static constexpr int a_stage(int s) { return s < P1 ? 3 * (s / 72) + (s % 36) / 12 : s < 864 ? s / 24 : 36 + (s - 864) / 20; }
  static constexpr int a_tile(int s) { return s < P1 ? (s % 72) / 36 : s < 864 ? (s % 24) / 12 : ((s - 864) % 20) / 10; }
  static constexpr int a_ks(int s) { return s < 864 ? s % 12 : (s - 864) % 10; }
  static constexpr bool skip(int s) { return ATTN && s >= 108 && s < 144 && s % 2 == 1; }
  static constexpr int g_stage(int s) {
    if (s < 0) return (s + 24) / 6;
    if (s >= 864 || s % 4 != 1) return -1;
    const int st = s / 24 + 4;
    return st >= NSTAGE || (st >= 36 && (s % 24) / 4 == 5) ? -1 : st;
  }
  static constexpr int g_piece(int s) { return (s % 24) / 4; }
  static constexpr int g_last(int st) { int r = -25; for (int s = -24; s < NSTEP; ++s) if (g_stage(s) == st) r = s; return r; }
  static constexpr int warm_k(int s) { return s >= WS0 && (s - WS0) % WSP == 0 && (s - WS0) / WSP < 5 ? (s - WS0) / WSP : -1; }
  static constexpr int PUB_HA = 288 + BMID, PUB_HB = 528, POLL_HA = 380, CHK_HA = 428, POLL_HB = 580, CHK_HB = 624, POLL_O = 792, CHK_O = 828;
  static constexpr int h_load(int s) {
    if (ATTN || s % 4 != 3) return -1;
    return s >= 432 && s < 528 ? (s - 432) / 4 : s >= 624 && s < 720 ? 24 + (s - 624) / 4 : -1;
  }
  static constexpr int o_load(int s) { return !ATTN && s > CHK_O && s < CHK_O + 19 && s % 4 > 1 ? (s - CHK_O) / 4 * 2 + s % 4 - 2 : -1; }
  static constexpr int x_load(int s) { return !ATTN && s >= 723 && s < 771 && s % 8 == 3 ? (s - 723) / 8 : -1; }
  static constexpr int NMARK = ATTN ? 9 : 11;
  static constexpr int mark_k(int s) {
    constexpr int M[] = {2, 84, 162, 240, 318, 396, 474, 574, 674, 774, 876}, A[] = {16, 98, 190, 268, 348, 430, 492};
    if (ATTN) { for (int k = 0; k < 7; ++k) if (s == A[k]) return k; }
    else { for (int k = 0; k < 11; ++k) if (s == M[k]) return k; }
    return -1;
  }
  static constexpr bool bar(int s) { return s < P1 ? (s % 72 == 0 || s % 72 == BMID || s % 72 == 48) : (s <= 864 && s % 24 == 0); }
  static constexpr bool halo(int s) { return ATTN && s % 4 == 2 && s >= 46; }
  static constexpr bool lwait(int s) { return (s + 1) % (s < 468 && !ATTN ? WP1 : WP2) == 0; }
  static constexpr int bias_rd(int s) { return !ATTN && s < 432 ? (s % 36 == 28 ? 0 : s % 36 == 35 ? 1 : -1) : -1; }
  static constexpr bool g_set(int s) { return g_stage(s + 3) >= 0 && g_piece(s + 3) % 3 == 0; }
  static constexpr bool fixed(int s, int h) {
    if (bar(s) || g_set(s)) return true;
    return h == 0 ? (g_stage(s) >= 0 || h_load(s) >= 0 || o_load(s) >= 0 || x_load(s) >= 0 || bias_rd(s) >= 0) : lwait(s);
  }
  static constexpr int ST0 = 30, ST1 = 57;
  static constexpr int win0(int s) { return s < 864 ? s / 36 * 36 : 864 + (s - 864) / 10 * 10; }
  static constexpr int wlen(int s) { return s < 864 ? 36 : 10; }
  static constexpr int nop(int s) { return ATTN ? (s >= 36 ? 32 : 0) : s >= 36 && s < 468 ? 59 : s >= 874 ? 10 : 0; }
  static constexpr int epi_to(int s, int h) {
    int f = 0, u = 0;
    for (int q = 2 * win0(s) + 2; q < 2 * (win0(s) + wlen(s)); ++q) if (!fixed(q / 2, q % 2)) { ++f; if (q <= 2 * s + h) ++u; }
    return f >= nop(s) ? (u < nop(s) ? u : nop(s)) : u * nop(s) / f;
  }
  static constexpr int epi_from(int s, int h) { return 2 * s + h <= 2 * win0(s) + 2 ? 0 : h ? epi_to(s, 0) : epi_to(s - 1, 1); }
  static constexpr bool has_op(int s, int k) { return epi_from(s, 0) <= k && k < epi_to(s, 1); }
  static constexpr int stores(int s) { return ATTN || s < 0 || !nop(s) ? 0 : s < 864 ? has_op(s, ST0) + has_op(s, ST1) : has_op(s, 4) + has_op(s, 9); }
  static constexpr int vm(int s) {
    int c = (g_stage(s) >= 0) + (warm_k(s) >= 0) + stores(s);
    if (!ATTN && s >= 0) c += (h_load(s) >= 0) + (o_load(s) >= 0) + (s == POLL_HA) + (s == POLL_HB) + (s == POLL_O) + (x_load(s) >= 0);
    return c;
  }
  static constexpr int need(int s) {
    int r = -25;
    if (bar(s) && !(ATTN && s == 480)) r = g_last(s < P1 ? 3 * (s / 72) + (s % 72 == 0 ? 1 : s % 72 == BMID ? 2 : 3) : s < 864 ? s / 24 + 1 : 38);
    if (!ATTN) {
      const int e = s == PUB_HA ? (DRAIN ? s - 1 : 251) : s == PUB_HB ? (DRAIN ? s - 1 : 467) : s == CHK_HA ? POLL_HA : s == CHK_HB ? POLL_HB : s == CHK_O ? POLL_O : s == 720 ? 671 : s == 768 ? 719 : s == 863 ? 862 : -25;
      if (e > r) r = e;
    }
    return r;
  }
  static constexpr int allow(int s) { int c = 0; for (int k = need(s) + 1; k < s; ++k) c += vm(k); return c; }
};
constexpr int kb_areg(int kb) { return kb == 0 ? A_H : kb == 1 ? A_H + 48 : kb == 2 ? A_H + 96 : kb == 3 ? A_H + 144 : kb == 4 ? A_H : kb == 5 ? A_H + 48 : A_H + 96; }
constexpr int qkv_pair(int kg, int p) {
  constexpr signed char T[4][28] = {
    {0,2,4,6, 16,8,10,-1, 18,20,22,24, 26,28,30,32, 12,14,34,52, 36,38,40,42, 44,46,48,50},
    {6,8,10,12, 0,2,4,-1, 24,26,28,30, 14,16,32,34, 18,20,22,50, 42,44,46,48, 36,38,40,52},
    {4,6,8,10, 12,14,16,-1, 22,24,26,28, 0,2,18,20, 30,32,34,36, 40,42,44,46, 48,50,52,38},
    {2,4,6,8, 0,10,12,-1, 20,22,24,26, 28,30,32,34, 14,16,18,36, 38,40,42,44, 46,48,50,52}};
  return T[kg][p];
}
struct QkvTab { unsigned v[4][16]; };
constexpr QkvTab make_tab() {
  QkvTab t = {};
  for (int kg = 0; kg < 4; ++kg)
    for (int p = 0; p < 28; ++p) {
      const int v = qkv_pair(kg, p), sv = v < 0 ? qkv_pair(kg, p - 1) : v;
      t.v[kg][p / 4] |= (unsigned)((sv / 18) * 72 + sv % 18) << (8 * (p % 4));
      t.v[kg][8 + p / 4] |= (unsigned)(sv >= 36 ? sv - 36 : sv) << (8 * (p % 4));
    }
  return t;
}

DI void spin_until(gu64* p, u64 tag) {
  for (unsigned spins = 0; spins < (1u << 18); ++spins) {
    const u64 f = __hip_atomic_load(p, RLX_AGENT);
    if (__all(f == tag)) break;
    __builtin_amdgcn_s_sleep(1);
  }
}

struct Args { const void *x, *wqkv, *wo, *w1, *b1, *w2, *b2; void *out, *ws; QkvTab qt; };
static_assert(sizeof(Args) == 72 + 256, "no padding");
DI char* as_ptr(unsigned lo, unsigned hi) { return (char*)(((u64)hi << 32) | lo); }

__global__ __launch_bounds__(256, 1) void main_k(const Args) {
  extern __shared__ __attribute__((aligned(16))) char smem[];
  LAS char* lds = (LAS char*)smem;
  const int tid = threadIdx.x, lane = tid & 63, bid = blockIdx.x;
  const int kg = map_kg(bid), cg = map_role(bid), rt = map_rt(bid);
  u32x16 ka, kt; u32x2 kw;
  asm volatile("s_load_dwordx16 %0, %3, 0x0\n\ts_load_dwordx2 %1, %3, 0x40\n\ts_load_dwordx16 %2, %3, %4 offset:0x48"
               : "=&s"(ka), "=&s"(kw), "=&s"(kt) : "s"(__builtin_amdgcn_kernarg_segment_ptr()), "s"(kg * 64));
  const void* code;
  unsigned code_bytes;
  asm volatile("s_getpc_b64 %0\n.Lk_pc:\n\ts_mov_b32 %1, .Lk_end0-.Lk_pc\n\ts_max_i32 %1, %1, .Lk_end1-.Lk_pc" : "=s"(code), "=s"(code_bytes) :: "scc", "a255");
  const int w = __builtin_amdgcn_readfirstlane(tid >> 6);
  const bool attn = cg == 3;
  const int tokbase = 256 * (rt >> 1) + (rt & 1);
  const int n16 = lane & 15, r8 = lane >> 3, pos = lane & 7;
  const int G = lane >> 4, q4 = (lane >> 2) & 3, p4 = lane & 3;

  const unsigned rot = (0x24924924u >> (2 * ((rt & 7) + cg))) & 3;
  unsigned kro[3] = {rot * 192, rot == 2 ? 0 : rot * 192 + 192, rot == 0 ? 384 : rot * 192 - 192};
  unsigned m6 = 6 * w * 1024, sx = tokbase * 1152 + w * 32 * 2304;
  unsigned vx0 = r8 * 2304 + ((pos ^ G) << 4), vx1 = vx0 ^ 64;
  PINS(m6); PINS(sx); PIN(vx0); PIN(vx1); PINS(kro[0]); PINS(kro[1]); PINS(kro[2]);
  const unsigned swz = pos ^ (((r8 >> 1) & 3) << 1);
  const int fs = 3 * kg + cg, f0 = fs * 384;
  unsigned strideA = attn ? 3456u : 9216u;
  PINS(strideA);
  unsigned soA[3];
#pragma unroll
  for (int j = 0; j < 3; ++j) { soA[j] = (kro[j] + 48 * w) * strideA + (attn ? 288 * kg : f0 * 2); PINS(soA[j]); }
  unsigned vab = r8 * strideA + (swz & 1) * 16, vsh = 8 * (swz >> 1), vcode = tid < 64 ? tid * 128 : OOB;
  PIN(vab); PIN(vsh); PIN(vcode);
  u32x4 id0, id1;
  {
    const unsigned one = 0x3f80u << (16 * (n16 & 1));
    const int e = (n16 >> 1) - 4 * G;
#pragma unroll
    for (int j = 0; j < 4; ++j) { id0[j] = e == j ? one : 0u; id1[j] = e + 8 == j ? one : 0u; }
  }
  PIN(id0); PIN(id1);
  unsigned po0 = (4 * G + q4) * 128 + (((2 * G + (q4 >> 1)) & 3) << 5) + p4 * 8;
  LAS char* rb[4][3];
#pragma unroll
  for (int i = 0; i < 3; ++i)
#pragma unroll
    for (int c = 0; c < 4; ++c) { rb[c][i] = lds + (po0 ^ (c << 5)) + 2 * i * SLOT; PIN(rb[c][i]); }
  LAS char* xp[2][2];
#pragma unroll
  for (int u = 0; u < 2; ++u) {
    const unsigned xr0 = w * 6144 + (n16 >> 3) * 1024 + (n16 & 7) * 128 + (((G ^ (n16 >> 1)) << 4) ^ (u << 6));
    xp[0][u] = lds + xr0 + 4 * SLOT; xp[1][u] = lds + xr0 + 2 * SLOT; PIN(xp[0][u]); PIN(xp[1][u]);
  }
  unsigned ones = 0x3f803f80u; PIN(ones);
  const int c1 = cg == 2 ? 0 : cg + 1, c2 = cg == 0 ? 2 : cg - 1;
  unsigned soB[3];
  soB[0] = (f0 + 48 * w) * 1152 + 384 * cg; soB[1] = ((3 * kg + c1) * 384 + 48 * w) * 1152 + 384 * cg; soB[2] = ((3 * kg + c2) * 384 + 48 * w) * 1152 + 384 * cg;
  const int p5 = w == 3 ? 13 : 5 * w;
  unsigned soO = (144 * kg + 8 * p5) * 1152 + 384 * cg, m5 = p5 * 1024;
  unsigned hso[3];
  hso[0] = H_OFF + ((rt * 12 + fs) * 4 + w) * 24576; hso[1] = H_OFF + ((rt * 12 + 3 * kg + c1) * 4 + w) * 24576; hso[2] = H_OFF + ((rt * 12 + 3 * kg + c2) * 4 + w) * 24576;
  unsigned oso = O_OFF + ((rt * 4 + kg) * 4 + w) * 9216;
  unsigned pso = P_OFF + (((rt * 3 + cg) * 4 + kg) * 4 + w) * 12288;
  unsigned os[3];
#pragma unroll
  for (int k = 0; k < 3; ++k) { const int un = 3 * kg + k; os[k] = (tokbase + 64 * w + 32 * (un & 1)) * 1152 + (192 * cg + 32 * (un >> 1)) * 2; }
#pragma unroll
  for (int k = 0; k < 3; ++k) { PINS(soB[k]); PINS(hso[k]); PINS(os[k]); }
  PINS(pso); PINS(oso); PINS(soO); PINS(m5);
  asm volatile("s_waitcnt lgkmcnt(0)" : "+s"(ka), "+s"(kw), "+s"(kt));
  STAMPT(0);
  const char *x = as_ptr(ka[0], ka[1]), *wo = as_ptr(ka[4], ka[5]), *b1 = as_ptr(ka[8], ka[9]), *w2 = as_ptr(ka[10], ka[11]), *b2 = as_ptr(ka[12], ka[13]);
  char *out = as_ptr(ka[14], ka[15]), *ws = as_ptr(kw[0], kw[1]);
  const u32x4 srdx = make_srd(x, S_ * D_ * 2);
  auto x_more = [&](auto T) { const unsigned b = sx + kro[T / 3] * 2; x_line<T>(m6, vx0, vx1, srdx, b, b + 8 * 2304, b + 16 * 2304, b + 24 * 2304); };
  sfor<3>(x_more);
  const unsigned vsel = 4 * w + (attn ? 16 : 0);
  if (V0 == 0) visit<0, Sched<false>::NMARK, Sched<true>::NMARK>(vsel);
  if (V1 == 0) visit<1, Sched<false>::NMARK, Sched<true>::NMARK>(vsel);
  u32x4 srdA = make_srd(attn ? as_ptr(ka[2], ka[3]) : as_ptr(ka[6], ka[7]), attn ? D_ * 3 * D_ * 2 : D_ * F_ * 2);
  PINS(srdA);
  unsigned qs[7], qd[7];
#pragma unroll
  for (int i = 0; i < 7; ++i) { qs[i] = kt[i]; qd[i] = kt[8 + i]; }
  auto va_of = [&](unsigned word) { return vab + ((word >> vsh) & 255) * 16; };
  unsigned va = va_of(attn ? qs[0] : 0x06040200u), so = soA[0] - 8 * strideA;
  auto w_piece = [&](auto K) {
    constexpr int n = K / 6, p = K % 6;
    if constexpr (p == 0 && n > 0) so = soA[n % 3] - 8 * strideA;
    if constexpr (K == 18) va = va_of(attn ? qs[1] : 0x0e0c0a08u);
    glds16r<n * SLOT + p * 1024>(m6, va, srdA, so, 8 * strideA);
  };
  sfor<6>(w_piece);
  warm(vcode, make_srd(code, code_bytes), ((bid >> 3) & 15) * 8192);
  if (V0 == 1) visit<0, Sched<false>::NMARK, Sched<true>::NMARK>(vsel);
  if (V1 == 1) visit<1, Sched<false>::NMARK, Sched<true>::NMARK>(vsel);
  if (V2 == 1) visit<2, Sched<false>::NMARK, Sched<true>::NMARK>(vsel);
  const u32x4 srdws = make_srd(ws, WS_BYTES);
  const unsigned flo = FLAG_OFF + rt * FL_N * 128;
  auto misc_loads = [&]() {
    asm volatile("buffer_load_dwordx2 a[12:13], %0, %1, %2 offen sc1" :: "v"(0), "s"(srdws), "s"(flo + FL_EPOCH * 128) : "memory");
    if (attn) {
      int ht = tokbase + 2 * (r8 - 8); ht = ht < 0 ? 0 : ht;
      const unsigned vh = ht * 1152 + pos * 16;
      const unsigned kb = ((0x24924924u >> (2 * (rot + w))) & 3) * 384 | (w == 3 ? OOB : 0);
      glds16c<TAB_OFF, 0>(w * 3072, vh, srdx, kb); glds16c<TAB_OFF + 1024, 128>(w * 3072, vh, srdx, kb); glds16c<TAB_OFF + 2048, 256>(w * 3072, vh, srdx, kb);
    } else {
      asm volatile("buffer_load_dword a14, %0, %1, 0 offen" :: "v"(tid * 4), "s"(make_srd(b1 + f0 * 2, 768)) : "memory");
      asm volatile("buffer_load_dword a15, %0, %1, 0 offen" :: "v"(tid * 4), "s"(make_srd(b2 + 192 * cg * 2, 384)) : "memory");
    }
  };
  u32x4 xf[36];
  f32x4 nm[2];
  float rstd[2];
  {
    float sm[2] = {0.f, 0.f}, sq[2] = {0.f, 0.f};
    SB();
    sfor<10>([&](auto T) {
      constexpr int t = T;
      if constexpr (t == 0) x_more(IC(3));
      if constexpr (t + 4 < 9) x_more(IC(t + 4));
      if constexpr (t == 4) misc_loads();
      if constexpr (t == V2 - 2) visit<2, Sched<false>::NMARK, Sched<true>::NMARK>(vsel);
      if constexpr (t == V1 - 2) visit<1, Sched<false>::NMARK, Sched<true>::NMARK>(vsel);
      if constexpr (t == V0 - 2) visit<0, Sched<false>::NMARK, Sched<true>::NMARK>(vsel);
      if constexpr (t < 9) {
        waitv<4 * ((t + 4 > 8 ? 8 : t + 4) - t) + (t < 3 ? 6 : 0) + (t > 9 - WPRO ? 6 * (t - 9 + WPRO) : 0)>();
#pragma unroll
        for (int u = 0; u < 4; ++u) xf[4 * t + u] = *(LAS u32x4*)(xp[t / 3 == 1][u >> 1] + (t % 3 == 1 ? 4096 + (u & 1) * SLOT : (t % 3 == 2 ? SLOT : 0) + (u & 1) * 2048));
      }
      SB();
      if constexpr (t > 0) {
#pragma unroll
        for (int u = 0; u < 4; u += 2)
#pragma unroll
          for (int j = 0; j < 4; ++j) {
            const unsigned v0 = xf[4 * t - 4 + u][j], v1 = xf[4 * t - 3 + u][j];
            sm[0] = dot2(v0, ones, sm[0]); sm[1] = dot2(v1, ones, sm[1]); sq[0] = dot2(v0, v0, sq[0]); sq[1] = dot2(v1, v1, sq[1]);
          }
        PIN(sm[0]); PIN(sm[1]); PIN(sq[0]); PIN(sq[1]);
      }
      if constexpr (t >= 9 - WPRO && t < 9) sfor<6>([&](auto P) { w_piece(IC(6 + 6 * (t - 9 + WPRO) + P)); });
      SB();
    });
#pragma unroll
    for (int tb = 0; tb < 2; ++tb) {
      const float negmu = xor32(xor16(sm[tb])) * (-1.0f / D_);
      nm[tb] = f32x4{negmu, negmu, negmu, negmu};
      rstd[tb] = __builtin_amdgcn_rsqf(fmaxf(xor32(xor16(sq[tb])) * (1.0f / D_) - negmu * negmu, 0.f) + 1e-5f);
    }
  }
  STAMPT(1);
  bf16x4 wl[NB], wu[NB];
  {
    f32x4 dl[3], dh[3];
    asm volatile("s_nop 1" : "+v"(nm[0]), "+v"(nm[1]));
    sfor<38>([&](auto B) {
      constexpr int b = B;
      if constexpr (b < 36) mfma_vvc(dl[b % 3], id0, xf[b], nm[b & 1]);
      if constexpr (b == 36) {
        asm volatile("s_waitcnt vmcnt(18)\n\ts_barrier" : "+v"(dl[1]), "+v"(dh[1]), "+v"(dl[2]), "+v"(dh[2]) :: "memory");
#pragma unroll
        for (int t = 0; t < LA; ++t) { wl[t] = rdhalf(rb[t & 1][0] + (t >> 1) * 4096); wu[t] = rdhalf(rb[t & 1][0] + (t >> 1) * 4096 + 2048); }
      }
      SB();
      if constexpr (b >= 2) { xf[b - 2][0] = cvtpk(dl[(b - 2) % 3][0], dl[(b - 2) % 3][1]); xf[b - 2][1] = cvtpk(dl[(b - 2) % 3][2], dl[(b - 2) % 3][3]); }
      SB();
      if constexpr (b < 36) mfma_vvc(dh[b % 3], id1, xf[b], nm[b & 1]);
      SB();
      if constexpr (b >= 2) { xf[b - 2][2] = cvtpk(dh[(b - 2) % 3][0], dh[(b - 2) % 3][1]); xf[b - 2][3] = cvtpk(dh[(b - 2) % 3][2], dh[(b - 2) % 3][3]); PIN(xf[b - 2]); }
      if constexpr (b % 2 == 0 && 6 + 6 * WPRO + b / 2 < 24) w_piece(IC(6 + 6 * WPRO + b / 2));
      SB();
    });
  }
  STAMPT(2);

  const u32x4 srdW2 = make_srd(w2, F_ * D_ * 2), srdWo = make_srd(wo, D_ * D_ * 2);
  const __amdgpu_buffer_rsrc_t rws = __builtin_amdgcn_make_buffer_rsrc(ws, 0, WS_BYTES, 0x00020000);
  gu64* flags = (gu64*)((char*)ws + flo);
  auto get_tag = [&]() __attribute__((always_inline)) { return (((u64)__float_as_uint(aread<13>()) << 32) | __float_as_uint(aread<12>())) * 0x9E3779B97F4A7C15ull + 0x632BE59BD9B4E019ull; };
  float t0, t1;
  f32x4 acc[2][2][2];
#define LANE_VALUES int tid_ = threadIdx.x; PIN(tid_); \
  const int tid = tid_, lane = tid & 63, n16 = lane & 15, r8 = lane >> 3, pos = lane & 7, G = lane >> 4, q4 = (lane >> 2) & 3, p4 = lane & 3; \
  const unsigned swz = pos ^ (((r8 >> 1) & 3) << 1), l16 = lane * 16, vflag = tid == 0 ? 0u : OOB, wa = rt * 72 + w * 18 + lane, wm = lane < 18 ? 0 : OOB; \
  (void)q4; (void)p4;

  if (!attn) {
    using SC = Sched<false>;
    LANE_VALUES
    constexpr bool attn_role = false;
    auto rd_a = [&](auto Sc, auto U) {
      constexpr int s = Sc, u = U;
      if constexpr (s < SC::NSTEP && !SC::skip(s) && !(u == 1 && s >= 864 && SC::a_ks(s) >= 8)) {
        constexpr int sl = SC::a_stage(s) % 6, ks = SC::a_ks(s);
        (u ? wu : wl)[s % NB] = rdhalf(rb[2 * SC::a_tile(s) + (ks & 1)][sl >> 1] + (sl & 1) * SLOT + (ks >> 1) * 4096 + u * 2048);
      }
    };
    auto mma1 = [&](auto Sc, auto H) {
      constexpr int s = Sc, k = s % 36, o = k & 1, tb = o ^ H;
      if constexpr (!SC::skip(s)) mfma_vv<k < 2>(acc[SC::a_tile(s)][o][tb], cat(wl[s % NB], wu[s % NB]), xf[(k & ~1) + tb]);
    };
    auto sync = [&](auto Sc, auto H) {
      constexpr int s = Sc, h = H;
      if constexpr (h == 0 && SC::need(s) > -25) waitv<SC::allow(s)>();
      if constexpr (h == 1 && (s == BMID || (attn_role && s == 480))) WAITL0();
      if constexpr (h == 1 && SC::bar(s)) BAR();
      if constexpr (h == 1 && SC::lwait(s)) __builtin_amdgcn_s_waitcnt(0xC07F | ((2 * (LA - (s < 468 && !attn_role ? WP1 : WP2))) << 8));
    };
    unsigned vA[3] = {va - 128, va - 128 + 8 * 9216 - 1024, va - 128 + 2 * (8 * 9216 - 1024)}, vB[3], lk[3], gso = sadd<128>(soA[1]), hst = hso[0];
    set_m0<4 * SLOT>(m6);
    u64 tag;
    const unsigned vpoll = (3 * kg + ((lane & 1) ? c1 : c2)) * 128;
    LAS char* tab = lds + TAB_OFF + G * 16;
    f32x4 bs[2], bn;
    f32x4 ac[6][2][2];
    unsigned e[8];
    u64 pollv = 0;
    const unsigned ov = n16 * 2304 + G * 8;
    u32x2 xr[3][2];
    float u0[2], u1[2];
    auto op1 = [&](auto Tc, auto K) {
      constexpr int T = Tc, k = K;
      if constexpr (k == SC::ST0 || k == SC::ST1) { constexpr int q = k == SC::ST1; st16_sc1<(T & 1) * 2048 + q * 1024>(u32x4{e[4 * q], e[4 * q + 1], e[4 * q + 2], e[4 * q + 3]}, l16, srdws, hst); }
      else if constexpr (k == 58) { if constexpr (T & 1) hst = sadd<4096>(hst); }
      else {
        constexpr int kk = k > SC::ST0 ? k - 1 : k, r = (kk - 4) % 7;
        constexpr int j = kk < 4 ? kk : kk >= 53 ? kk - 49 : r == 0 ? 4 : r == 1 ? 0 : r == 2 ? 5 : r == 3 ? 1 : r == 4 ? 2 : r == 5 ? 3 : 6;
        constexpr int d = kk < 4 ? 0 : kk >= 53 ? 7 : (kk - 4) / 7 + (j < 4), hf = d & 1;
        const f32x4& a = acc[T & 1][(d >> 1) & 1][d >> 2];
        if constexpr (j == 0) u0[hf] = i_fma(a[2 * hf], rstd[d >> 2], bs[(d >> 1) & 1][2 * hf]);
        if constexpr (j == 1) u1[hf] = i_fma(a[2 * hf + 1], rstd[d >> 2], bs[(d >> 1) & 1][2 * hf + 1]);
        if constexpr (j == 2) u0[hf] = i_sqs(u0[hf]);
        if constexpr (j == 3) u1[hf] = i_sqs(u1[hf]);
        if constexpr (j == 4) e[d] = i_cvt(u0[hf], u1[hf]);
        if constexpr (j == 5) e[d] = i_relu(e[d]);
        if constexpr (j == 6) awrite<A_H + 8 * T + d>(e[d]);
      }
    };
    auto op2 = [&](auto Tc, auto K) {
      constexpr int T = Tc, q = K / 5, j = K % 5;
      if constexpr (j < 4) e[4 * q + j] = i_cvt(ac[T][j >> 1][q][2 * (j & 1)], ac[T][j >> 1][q][2 * (j & 1) + 1]);
      else st16_sc1<(T & 1) * 2048 + q * 1024>(u32x4{e[4 * q], e[4 * q + 1], e[4 * q + 2], e[4 * q + 3]}, lk[T >> 1], srdws, pso);
    };
    auto slot = [&](auto Sc, auto H) {
      constexpr int s = Sc, h = H;
      sync(Sc, H);
      if constexpr (SC::g_set(s)) {
        constexpr int np = SC::g_stage(s + 3), p = SC::g_piece(s + 3), kb = (np - 18) / 3, src = kb < 2 ? 0 : 1 + (kb & 1), half = kb < 2 ? kb : (kb >= 4);
        if constexpr (h == 0) set_m0<(np % 6) * SLOT + p * 1024>(np < 36 ? m6 : m5);
        else if constexpr (np < 18) gso = sadd<8 * p * 9216 + 128 * (np / 3)>(soA[np % 3]);
        else if constexpr (np < 36) gso = sadd<(192 * half + 8 * p) * 1152 + 128 * ((np - 18) % 3)>(soB[src]);
        else gso = sadd<8 * p * 1152 + 128 * (np - 36)>(soO);
      }
      if constexpr (h == 0) {
        if constexpr (SC::g_stage(s) >= 0) { constexpr int np = SC::g_stage(s), j = SC::g_piece(s) % 3; glds3<j>(np < 18 ? vA[j] : vB[j], np < 18 ? srdA : np < 36 ? srdW2 : srdWo, gso); }
        if constexpr (SC::bias_rd(s) == 0) bn = *(LAS f32x4*)(tab + (s / 36) * 128);
        if constexpr (SC::bias_rd(s) == 1) { bs[0] = bn; bs[1] = *(LAS f32x4*)(tab + (s / 36) * 128 + 64); }
        if constexpr (SC::x_load(s) >= 0) { constexpr int k = SC::x_load(s); xr[k >> 1][k & 1] = bload8<32 * (k & 1)>(ov, srdx, os[k >> 1]); }
        if constexpr (SC::o_load(s) >= 0) { constexpr int f = SC::o_load(s); aload16_sc1<kb_areg(6) + 4 * f, (f & 3) * 1024>(lk[f >> 2], srdws, oso); }
        if constexpr (SC::h_load(s) >= 0) { constexpr int b = SC::h_load(s) / 12, f = SC::h_load(s) % 12; aload16_sc1<kb_areg(2 + b) + 4 * f, (f & 3) * 1024>(lk[f >> 2], srdws, hso[1 + (b & 1)]); }
      }
      if constexpr (SC::nop(s) > 0) {
        static_assert(SC::bias_rd(s) != 1 || s < 36 || SC::epi_from(s, 0) > 52, "the bias registers are free");
        constexpr int k0 = SC::epi_from(s, h), k1 = SC::epi_to(s, h);
        sfor<k1 - k0>([&](auto I) { if constexpr (s < 864) op1(IC(s / 36 - 1), IC(k0 + I)); else op2(IC((s - 864) / 10 - 1), IC(k0 + I)); if constexpr (s < 864 && k0 + I == SC::ST0 && k0 + I + 1 < k1) asm volatile("s_nop 1"); });
      }
    };
    sfor<77>([&](auto So) { sfor<12>([&](auto Si) {
      constexpr int s = 12 * So + Si;
      if constexpr (s == 432) { STAMPT(3); }
      sfor<2>([&](auto H) {
        if constexpr (s < 432) mma1(IC(s), H);
        else {
          constexpr int n = SC::a_stage(s) - 18, kb = n / 3, T = 2 * (n % 3) + SC::a_tile(s), ks = SC::a_ks(s), o = ks & 1, tb = o ^ H, ar = kb_areg(kb) + 8 * (ks >> 1);
          if constexpr (kb == 6 && ks >= 8) mfma_k16<ar + 2 * tb>(ac[T][o][tb], wl[s % NB]);
          else mfma_va<kb == 0 && ks < 2, ar + 4 * tb>(ac[T][o][tb], cat(wl[s % NB], wu[s % NB]));
        }
        SB(); rd_a(IC(s + LA), H); SB();
        slot(IC(s), H); SB();
      });
      constexpr int k = SC::warm_k(s);
      if constexpr (s % 24 == 0) STAGET(s / 24);
      if constexpr (SC::mark_k(s) >= 0) mark<0, SC::mark_k(s), SC::NMARK>();
      if constexpr (k >= 0 && k < 2) warm(((wa >> 1) * 9216 + (wa & 1) * 128) | wm, srdA, f0 * 2 + 256 + 256 * k);
      if constexpr (k >= 2) warm((wa * 128) | wm, srdW2, f0 * 1152 + (k - 2) * 147456);
      if constexpr (s == SC::PUB_HA || s == SC::PUB_HB) bstore8_sc1(tag, vflag, srdws, flo + ((s == SC::PUB_HA ? FL_HA : FL_HB) + fs) * 128);
      if constexpr (s == SC::CHK_HA || s == SC::CHK_HB || s == SC::CHK_O) {
        PIN(pollv);
        if (__builtin_expect(!__all(pollv == tag), 0))
          spin_until((gu64*)((char*)flags + (s == SC::CHK_O ? (FL_O + kg) * 128 : (s == SC::CHK_HA ? FL_HA : FL_HB) * 128 + vpoll)), tag);
      }
      if constexpr (s == 4) { const unsigned r = __float_as_uint(aread<14>()); *(LAS f32x2*)(lds + TAB_OFF + tid * 8) = f32x2{bflo(r), bfhi(r)}; }
      if constexpr (s == 8) { const unsigned r = __float_as_uint(aread<15>()); *(LAS f32x2*)(lds + TAB_OFF + 2048 + tid * 8) = f32x2{bflo(r), bfhi(r)}; }
      if constexpr (s == 12) { tag = get_tag(); PIN(tag); }
      if constexpr (s == 334) { vB[0] = r8 * 1152 + swz * 16; vB[1] = vB[0] + 8192; vB[2] = vB[0] + 16384; PIN(vB[0]); PIN(vB[1]); PIN(vB[2]); }
      if constexpr (s == 432) { lk[0] = l16; lk[1] = l16 + 4096; lk[2] = l16 + 8192; PIN(lk[1]); PIN(lk[2]); }
      if constexpr (s == 600) { hso[1] += 12288; hso[2] += 12288; PINS(hso[1]); PINS(hso[2]); }
      if constexpr (s == SC::POLL_HA || s == SC::POLL_HB) pollv = bload8_sc1(vpoll, srdws, flo + (s == SC::POLL_HA ? FL_HA : FL_HB) * 128);
      if constexpr (s == SC::POLL_O) pollv = bload8_sc1(0, srdws, flo + (FL_O + kg) * 128);
      SB();
    }); });
    asm volatile("s_nop 7" : "+v"(ac[5][0][0]), "+v"(ac[5][0][1]), "+v"(ac[5][1][0]), "+v"(ac[5][1][1]));
    sfor<10>([&](auto K) { op2(IC(5), K); if constexpr (K % 5 == 4) asm volatile("s_nop 1"); });
    STAMPT(4);
    asm volatile("s_waitcnt vmcnt(0)" : "+v"(xr[0][0]), "+v"(xr[0][1]), "+v"(xr[1][0]), "+v"(xr[1][1]), "+v"(xr[2][0]), "+v"(xr[2][1]) :: "memory");
    BAR();
    bstore8_sc1(tag, vflag, srdws, flo + (FL_FIN + fs) * 128);
    STAMPT(5);
    f32x4 cr[3][2];
#pragma unroll
    for (int k = 0; k < 3; ++k)
#pragma unroll
      for (int j = 0; j < 2; ++j) { cr[k][j] = *(LAS f32x4*)(tab + 2048 + 128 * ((3 * kg + k) >> 1) + 64 * j); }
    spin_until(flags + (FL_FIN + 3 * (lane & 3) + cg) * 16, tag);
    if (fs == 0) bstore8_sc1(tag, vflag, srdws, flo + FL_EPOCH * 128);
    const __amdgpu_buffer_rsrc_t rout = __builtin_amdgcn_make_buffer_rsrc(out, 0, S_ * D_ * 2, 0x00020000);
    u32x4 v[3][4];
#pragma unroll
    for (int k = 0; k < 3; ++k)
#pragma unroll
      for (int s = 0; s < 4; ++s) v[k][s] = __builtin_amdgcn_raw_buffer_load_b128(rws, l16, P_OFF + (((rt * 3 + cg) * 4 + s) * 4 + w) * 12288 + (3 * kg + k) * 1024, 16);
#pragma unroll
    for (int k = 0; k < 3; ++k) {
      float a[8];
#pragma unroll
      for (int j = 0; j < 4; ++j) { a[2 * j] = bflo(xr[k][j >> 1][j & 1]) + cr[k][j >> 1][(2 * j) & 3]; a[2 * j + 1] = bfhi(xr[k][j >> 1][j & 1]) + cr[k][j >> 1][(2 * j + 1) & 3]; }
#pragma unroll
      for (int s = 0; s < 4; ++s)
#pragma unroll
        for (int j = 0; j < 4; ++j) { a[2 * j] += bflo(v[k][s][j]); a[2 * j + 1] += bfhi(v[k][s][j]); }
      __builtin_amdgcn_raw_buffer_store_b64(u32x2{cvtpk(a[0], a[1]), cvtpk(a[2], a[3])}, rout, ov, os[k], OAUX);
      __builtin_amdgcn_raw_buffer_store_b64(u32x2{cvtpk(a[4], a[5]), cvtpk(a[6], a[7])}, rout, ov + 32, os[k], OAUX);
    }
    STAMPT(6);
    STAMPOUT();
    STAGEOUT();
    asm volatile(".Lk_end0:");
    __builtin_amdgcn_endpgm();
  }
  {
    using SC = Sched<true>;
    LANE_VALUES
    auto va_of = [&](unsigned word) { return r8 * 3456 + (swz & 1) * 16 + ((word >> (8 * (swz >> 1))) & 255) * 16; };
    constexpr bool attn_role = true;
    auto rd_a = [&](auto Sc, auto U) {
      constexpr int s = Sc, u = U;
      if constexpr (s < SC::NSTEP && !SC::skip(s) && !(u == 1 && s >= 864 && SC::a_ks(s) >= 8)) {
        constexpr int sl = SC::a_stage(s) % 6, ks = SC::a_ks(s);
        (u ? wu : wl)[s % NB] = rdhalf(rb[2 * SC::a_tile(s) + (ks & 1)][sl >> 1] + (sl & 1) * SLOT + (ks >> 1) * 4096 + u * 2048);
      }
    };
    auto mma1 = [&](auto Sc, auto H) {
      constexpr int s = Sc, k = s % 36, o = k & 1, tb = o ^ H;
      if constexpr (!SC::skip(s)) mfma_vv<k < 2>(acc[SC::a_tile(s)][o][tb], cat(wl[s % NB], wu[s % NB]), xf[(k & ~1) + tb]);
    };
    auto sync = [&](auto Sc, auto H) {
      constexpr int s = Sc, h = H;
      if constexpr (h == 0 && SC::need(s) > -25) waitv<SC::allow(s)>();
      if constexpr (h == 1 && (s == BMID || (attn_role && s == 480))) WAITL0();
      if constexpr (h == 1 && SC::bar(s)) BAR();
      if constexpr (h == 1 && SC::lwait(s)) __builtin_amdgcn_s_waitcnt(0xC07F | ((2 * (LA - (s < 468 && !attn_role ? WP1 : WP2))) << 8));
    };
    unsigned vA[3] = {va, va + 8 * 3456 - 1024, va + 2 * (8 * 3456 - 1024)}, gso = soA[1];
    set_m0<4 * SLOT>(m6);
    f32x4 gram;
    float rstdh, negmuh;
    const unsigned hrow = n16 < 8 ? n16 : 0;
    auto halo_val = [&](auto I) {
      f32x4 r;
      sfor<4>([&](auto K) { const float a = aread<A_HH + 4 * (I - 2) + K>(); r[(int)K] = fmaf(ror8(a), negmuh, a) * rstdh; });
      return r;
    };
    auto halo_col = [&](auto I) { return ((qd[I] >> (8 * w)) & 255); };
    LAS char* hb[3];
#pragma unroll
    for (int i = 0; i < 3; ++i) { hb[i] = (LAS char*)(uintptr_t)((unsigned)(uintptr_t)rb[0][i] ^ (w << 5)); PIN(hb[i]); }
    const unsigned ha = (unsigned)(uintptr_t)lds + TAB_OFF + hrow * 128 + G * 8;
    const unsigned qrow = (unsigned)(uintptr_t)lds + QK_OFF + (32 * w + n16 + 8) * QK_RS + G * 8, vrow = (unsigned)(uintptr_t)lds + (32 * w + n16 + 8) * V_RS + G * 16;
    bf16x4 hl[2], hu[2];
    auto opa = [&](auto Tc, auto K) {
      constexpr int T = Tc, d = K / 4, j = K % 4, p = 2 * T + (d >> 2), tb = (d >> 1) & 1, hf = d & 1;
      if constexpr (p != 7) {
        const f32x4& a = acc[T & 1][d >> 2][tb];
        if constexpr (j == 0) t0 = i_mul(a[2 * hf], rstd[tb]);
        if constexpr (j == 1) t1 = i_mul(a[2 * hf + 1], rstd[tb]);
        if constexpr (p < 19) {
          if constexpr (j == 2) t0 = __uint_as_float(i_cvt(t0, t1));
          if constexpr (j == 3) awrite<A_Q + 4 * p + 2 * tb + hf>(__float_as_uint(t0));
        } else if constexpr (j >= 2) awrite<A_V + 8 * (p - 19) + 4 * tb + 2 * hf + j - 2>(__float_as_uint(j == 2 ? t0 : t1));
      }
    };
    sfor<28>([&](auto So) { sfor<18>([&](auto Si) {
      constexpr int s = 18 * So + Si, i = s / 72, u = s % 72;
      sfor<2>([&](auto H) {
        constexpr int h = H;
        mma1(IC(s), H);
        SB(); rd_a(IC(s + LA), H); SB();
        sync(IC(s), H);
        if constexpr (SC::g_set(s)) {
          constexpr int np = SC::g_stage(s + 3), p = SC::g_piece(s + 3);
          if constexpr (h == 0) set_m0<(np % 6) * SLOT + p * 1024>(m6); else gso = sadd<8 * p * 3456>(soA[np % 3]);
        }
        if constexpr (h == 0 && SC::g_stage(s) >= 0) glds3<SC::g_piece(s) % 3>(vA[SC::g_piece(s) % 3], srdA, gso);
        if constexpr (SC::nop(s) > 0) { constexpr int k0 = SC::epi_from(s, h), k1 = SC::epi_to(s, h); sfor<k1 - k0>([&](auto I) { opa(IC(s / 36 - 1), IC(k0 + I)); }); }
        SB();
      });
      if constexpr (u % 4 == 2) {
        constexpr int sg = u / 4;
        if constexpr (s >= 46 && s <= 114) mfma16_aa<s == 46, A_HX + s - 46>(gram);
        if constexpr (i >= 2) mfma16_ava<sg == 0, A_HH + 4 * (i - 2), A_HX + 4 * sg>(cat(hl[sg & 1], hu[sg & 1]));
        SB();
        if constexpr (s >= 22 && s < 94) { constexpr int f = (s - 22) / 4; ds_read_a64<A_HX + 4 * f, (f >> 1) * 1024 + (f & 1) * 64>(ha); ds_read_a64<A_HX + 4 * f + 2, (f >> 1) * 1024 + (f & 1) * 64 + 32>(ha); }
        constexpr int s2 = s + 8, sl = (3 * (s2 / 72) + (s2 % 72) / 24) % 6;
        if constexpr (s2 >= 144 && s2 < 504) { LAS char* p = hb[sl >> 1] + (sl & 1) * SLOT + (((s2 % 72) / 4) % 6) * 4096; hl[sg & 1] = rdhalf(p); hu[sg & 1] = rdhalf(p + 2048); }
        if constexpr (s == 122) {
          const float dg = (p4 == 0) ? gram[0] : (p4 == 1) ? gram[1] : (p4 == 2) ? gram[2] : gram[3];
          rstdh = G == q4 ? dg : 0.f; negmuh = G == 2 ? gram[0] : 0.f;
          PIN(rstdh); PIN(negmuh);
        }
        if constexpr (s == 126) { rstdh = xor16(rstdh); negmuh = xor16(negmuh); PIN(rstdh); PIN(negmuh); }
        if constexpr (s == 130) { rstdh = xor32(rstdh); negmuh = xor32(negmuh); PIN(rstdh); PIN(negmuh); }
        if constexpr (s == 134) {
          negmuh *= -1.0f / D_;
          rstdh = __builtin_amdgcn_rsqf(fmaxf(rstdh * (1.0f / D_) - negmuh * negmuh, 0.f) + 1e-5f);
          PIN(rstdh); PIN(negmuh);
        }
      }
      constexpr int k = SC::warm_k(s);
      if constexpr (s % 24 == 0 && s < 432) STAGET(s / 24);
      if constexpr (SC::mark_k(s) >= 0) mark<1, SC::mark_k(s), SC::NMARK>();
      if constexpr (k >= 0 && k < 4) {
        const unsigned la = (288 * kg + ((qs[k < 3 ? 2 + k : 5] >> (k == 2 ? 16 : 0)) & 255) * 16) & ~127u, lb = (288 * kg + (k < 3 ? qs[2 + k] >> 24 : qs[6] & 255) * 16) & ~127u;
        warm(((wa >> 1) * 3456 + ((wa & 1) ? lb : la)) | wm, srdA, 0);
      }
      if constexpr (k == 4) warm(((rt * 81 + w * 21 + lane) * 128) | (lane < 21 ? 0 : OOB), srdWo, 144 * kg * 1152);
      if constexpr (s == 2) *(LAS u32x4*)(lds + TAB_OFF + w * 3072 + (r8 < 3 ? r8 : 0) * 1024 + pos * 16) = u32x4{0x3f803f80u, 0x3f803f80u, 0x3f803f80u, 0x3f803f80u};
      if constexpr (u == 47 && i < 5) { vA[0] = va_of(qs[i + 2]); vA[1] = vA[0] + 8 * 3456 - 1024; vA[2] = vA[0] + 2 * (8 * 3456 - 1024); PIN(vA[0]); PIN(vA[1]); PIN(vA[2]); }
      if constexpr (s > 432 && s < 471 && s % 2 == 1 && s != 447) {
        constexpr int p = (s - 433) / 2;
        const unsigned ad = qrow + ((qd[p / 4] >> (8 * (p % 4))) & 255) * 16;
        ds_write_a64<A_Q + 4 * p, 0>(ad); ds_write_a64<A_Q + 4 * p + 2, 16 * QK_RS>(ad);
      }
      if constexpr (s == 471 || s == 473 || s == 477) {
        constexpr int hi = s == 471 ? 2 : s == 473 ? 3 : 4;
        const f32x4 r = halo_val(IC(hi));
        const unsigned row = (hi == 4 && w == 3) ? 0 : hrow;
        *(LAS u32x2*)(lds + QK_OFF + row * QK_RS + halo_col(IC(hi)) * 16 + G * 8) = u32x2{cvtpk(r[0], r[1]), cvtpk(r[2], r[3])};
      }
      SB();
    }); });
    asm volatile("s_nop 7" : "+v"(acc[1][0][0]), "+v"(acc[1][0][1]), "+v"(acc[1][1][0]), "+v"(acc[1][1][1]));
    WAITL0(); BAR();
    STAMPT(3);
    {
      const int hd = lane & 7, i0 = 32 * w + 4 * r8;
      LAS char* rp[11];
#pragma unroll
      for (int jj = 0; jj < 11; ++jj) { rp[jj] = lds + QK_OFF + (i0 + 1 + jj) * QK_RS + hd * 36; PIN(rp[jj]); }
      unsigned qv[4][9], kv[11][9];
#pragma unroll
      for (int a = 0; a < 4; ++a)
#pragma unroll
        for (int c = 0; c < 9; ++c) qv[a][c] = *(LAS unsigned*)(rp[7 + a] + c * 4);
#pragma unroll
      for (int jj = 0; jj < 4; ++jj)
#pragma unroll
        for (int c = 0; c < 9; ++c) kv[jj][c] = *(LAS unsigned*)(rp[jj] + 288 + c * 4);
      {
#pragma unroll
        for (int k = 0; k < 4; ++k)
          *(LAS f32x4*)(uintptr_t)(vrow + ((qd[6] >> (16 + 8 * (k >> 1))) & 255) * 32 + (k & 1) * 16 * V_RS) = acc[1][k >> 1][k & 1] * rstd[k & 1];
        sfor<3>([&](auto I) {
          constexpr int hi = I + 4;
          const unsigned row = (hi == 4 && w != 3) ? 0 : hrow, col = (hi == 4 && w != 3) ? 0 : halo_col(IC(hi));
          *(LAS f32x4*)(lds + row * V_RS + col * 32 + G * 16) = halo_val(IC(hi));
        });
      }
      SB();
      float lg[4][8];
      sfor<8>([&](auto U) {
        constexpr int u = U;
        if constexpr (u + 4 < 11) {
#pragma unroll
          for (int c = 0; c < 9; ++c) kv[u + 4][c] = *(LAS unsigned*)(rp[u + 4] + 288 + c * 4);
        }
        if constexpr (u < 7) {
          constexpr int p = 19 + u;
          const unsigned ad = vrow + ((qd[p / 4] >> (8 * (p % 4))) & 255) * 32;
          ds_write_a128<A_V + 8 * u, 0>(ad); ds_write_a128<A_V + 8 * u + 4, 16 * V_RS>(ad);
        }
#pragma unroll
        for (int a = 0; a < 4; ++a) lg[a][u] = 0.f;
#pragma unroll
        for (int c = 0; c < 9; ++c)
#pragma unroll
          for (int a = 0; a < 4; ++a) lg[a][u] = dot2(qv[a][c], kv[a + u][c], lg[a][u]);
        SB();
      });
      WAITL0(); BAR();
      mark<1, 7, 9>();
      STAMPT(4);
      LAS char* vp = lds + (i0 + 1) * V_RS + hd * 72;
      f32x2 vv[11][9];
#pragma unroll
      for (int jj = 0; jj < 3; ++jj)
#pragma unroll
        for (int c = 0; c < 9; ++c) vv[jj][c] = *(volatile LAS f32x2*)(vp + jj * V_RS + c * 8);
      if (rt < 2) {
#pragma unroll
        for (int a = 0; a < 4; ++a)
#pragma unroll
          for (int u = 0; u < 7 - a; ++u) lg[a][u] = (i0 + a + u < 7) ? -1e9f : lg[a][u];
      }
      constexpr float CS = 0.23570226039551584f * 1.4426950408889634f;
#pragma unroll
      for (int a = 0; a < 4; ++a) {
        float mx = lg[a][0];
#pragma unroll
        for (int u = 1; u < 8; ++u) mx = fmaxf(mx, lg[a][u]);
        const float nb = -mx * CS;
        float sum = 0.f;
#pragma unroll
        for (int u = 0; u < 8; ++u) { lg[a][u] = __builtin_amdgcn_exp2f(fmaf(lg[a][u], CS, nb)); sum += lg[a][u]; }
        const float inv = __builtin_amdgcn_rcpf(sum);
#pragma unroll
        for (int u = 0; u < 8; ++u) lg[a][u] *= inv;
      }
      SB();
      f32x2 o[4][9];
      sfor<11>([&](auto J) {
        constexpr int jj = J;
        if constexpr (jj + 3 < 11) {
#pragma unroll
          for (int c = 0; c < 9; ++c) vv[jj + 3][c] = *(volatile LAS f32x2*)(vp + (jj + 3) * V_RS + c * 8);
        }
#pragma unroll
        for (int c = 0; c < 9; ++c) {
#pragma unroll
          for (int a = 0; a < 4; ++a) {
            if (jj == a) o[a][c] = vv[jj][c] * lg[a][0];
            else if (jj > a && jj <= a + 7) o[a][c] = __builtin_elementwise_fma(vv[jj][c], f32x2{lg[a][jj - a], lg[a][jj - a]}, o[a][c]);
          }
        }
        if constexpr (jj == 7) mark<1, 8, 9>();
        if constexpr (jj >= 7) {
#pragma unroll
          for (int c = 0; c < 9; ++c) *(LAS unsigned*)(rp[jj] + c * 4) = cvtpk(o[jj - 7][c][0], o[jj - 7][c][1]);
        }
        SB();
      });
    }
    STAMPT(5);
    {
      LAS char* rowp = (LAS char*)(uintptr_t)qrow;
#pragma unroll
      for (int f = 0; f < 9; ++f) {
        const u32x2 a = *(LAS u32x2*)(rowp + (f >> 1) * 64 + (f & 1) * 16 * QK_RS), b = *(LAS u32x2*)(rowp + (f < 8 ? (f >> 1) * 64 + (f & 1) * 16 * QK_RS + 32 : 256 + 16 * QK_RS));
        __builtin_amdgcn_raw_buffer_store_b128(u32x4{a[0], a[1], b[0], b[1]}, rws, l16, oso + f * 1024, 16);
      }
    }
    WAITV(0); BAR();
    bstore8_sc1(get_tag(), vflag, srdws, flo + (FL_O + kg) * 128);
    STAMPT(6);
  }
  STAMPOUT();
  STAGEOUT();
  asm volatile(".Lk_end1:");
}

extern "C" void kernel_launch(void* const* d_in, const int* in_sizes, int n_in, void* d_out, int out_size, void* d_ws, size_t ws_size, hipStream_t stream) {
  (void)hipFuncSetAttribute((const void*)main_k, hipFuncAttributeMaxDynamicSharedMemorySize, LDS_BYTES);
  const Args args = {d_in[0], d_in[1], d_in[2], d_in[4], d_in[5], d_in[6], d_in[7], d_out, d_ws, make_tab()};
  main_k<<<256, 256, LDS_BYTES, stream>>>(args);
}
```

```cpp
#include <hip/hip_runtime.h>
#include <stdint.h>
#include <utility>

typedef __bf16 bf16x8 __attribute__((ext_vector_type(8)));
typedef __bf16 bf16x4 __attribute__((ext_vector_type(4)));
typedef __bf16 bf16x2 __attribute__((ext_vector_type(2)));
typedef float f32x4 __attribute__((ext_vector_type(4)));
typedef float f32x2 __attribute__((ext_vector_type(2)));
typedef unsigned u32x4 __attribute__((ext_vector_type(4)));
typedef unsigned u32x2 __attribute__((ext_vector_type(2)));
typedef unsigned u32x16 __attribute__((ext_vector_type(16)));
typedef unsigned long long u64;
#define LAS __attribute__((address_space(3)))
#define GAS __attribute__((address_space(1)))
typedef GAS u64 gu64;
#define DI __device__ __forceinline__
#define SB() __builtin_amdgcn_sched_barrier(0)
#define IC(n) std::integral_constant<int, (n)>{}
#define RLX_AGENT __ATOMIC_RELAXED, __HIP_MEMORY_SCOPE_AGENT

template <class F, int... I> DI void sfor_(F&& f, std::integer_sequence<int, I...>) { (f(std::integral_constant<int, I>{}), ...); }
template <int N, class F> DI void sfor(F&& f) { sfor_(f, std::make_integer_sequence<int, N>{}); }

constexpr int S_ = 2048, D_ = 576, F_ = 4608;
constexpr int SLOT = 24576;
constexpr int TAB_OFF = 147456;
constexpr int LDS_BYTES = 163840;
constexpr int QK_OFF = 80512, QK_RS = 584, V_RS = 592;
constexpr unsigned H_OFF = 0, O_OFF = 20u << 20, P_OFF = 24u << 20, FLAG_OFF = 40u << 20, WS_BYTES = 41u << 20;
constexpr unsigned OOB = 0x40000000u;
constexpr int FL_HA = 0, FL_HB = 12, FL_O = 24, FL_FIN = 28, FL_EPOCH = 40, FL_N = 41;
constexpr int A_H = 56;
constexpr int A_Q = 16;
constexpr int A_V = 92;
constexpr int A_HH = 164;
constexpr int A_HX = 184;

DI int map_role(int bid) { return (bid >> 3) & 3; }
DI int map_kg(int bid) { return bid & 3; }
DI int map_rt(int bid) { return ((bid >> 2) & 1) * 8 + (bid >> 5); }

DI u32x4 make_srd(const void* p, unsigned bytes) {
  uint64_t a = (uint64_t)p;
  u32x4 r; r[0] = (unsigned)a; r[1] = (unsigned)(a >> 32) & 0xffffu; r[2] = bytes; r[3] = 0x00020000u;
  return r;
}
template <unsigned MC, unsigned SC> DI void glds16c(unsigned mb, unsigned voff, u32x4 srd, unsigned sb) {
  unsigned tmp;
  asm volatile("s_add_u32 m0, %1, %5\n\ts_add_u32 %0, %4, %6\n\tbuffer_load_dwordx4 %2, %3, %0 offen lds"
               : "=&s"(tmp) : "s"(mb), "v"(voff), "s"(srd), "s"(sb), "i"(MC), "i"(SC) : "memory", "scc");
}
template <unsigned C> DI void set_m0(unsigned b) { asm volatile("s_add_u32 m0, %0, %1" :: "s"(b), "i"(C) : "scc"); }
template <unsigned C> DI unsigned sadd(unsigned b) { unsigned r; asm volatile("s_add_u32 %0, %1, %2" : "=s"(r) : "s"(b), "i"(C) : "scc"); return r; }
template <int J> DI void glds3(unsigned v, u32x4 srd, unsigned so) { asm volatile("buffer_load_dwordx4 %0, %1, %2 offen offset:%c3 lds" :: "v"(v), "s"(srd), "s"(so), "i"(J * 1024) : "memory"); }
template <unsigned MC> DI void glds16r(unsigned mb, unsigned voff, u32x4 srd, unsigned& so, unsigned step) {
  asm volatile("s_add_u32 m0, %1, %5\n\ts_add_u32 %0, %0, %4\n\tbuffer_load_dwordx4 %2, %3, %0 offen lds"
               : "+s"(so) : "s"(mb), "v"(voff), "s"(srd), "s"(step), "i"(MC) : "memory", "scc");
}
constexpr int x_base(int t) { return (t / 3 == 1 ? 2 : 4) * SLOT + (t % 3 == 2 ? SLOT : 0); }
template <int T> DI void x_line(unsigned m6, unsigned vx0, unsigned vx1, u32x4 srd, unsigned s0, unsigned s1, unsigned s2, unsigned s3) {
  constexpr int d0 = x_base(T) + (T % 3 == 1 ? 4096 : 0) - (T % 3) * 128, d2 = T % 3 == 1 ? SLOT - 1024 : 1024;
  asm volatile("s_add_u32 m0, %0, %8\n\ts_nop 0\n\tbuffer_load_dwordx4 %1, %3, %4 offen offset:%c10 lds\n\t"
               "s_addk_i32 m0, 0x400\n\ts_nop 0\n\tbuffer_load_dwordx4 %2, %3, %5 offen offset:%c10 lds\n\t"
               "s_addk_i32 m0, %c9\n\ts_nop 0\n\tbuffer_load_dwordx4 %1, %3, %6 offen offset:%c10 lds\n\t"
               "s_addk_i32 m0, 0x400\n\ts_nop 0\n\tbuffer_load_dwordx4 %2, %3, %7 offen offset:%c10 lds"
               :: "s"(m6), "v"(vx0), "v"(vx1), "s"(srd), "s"(s0), "s"(s1), "s"(s2), "s"(s3), "i"(d0), "i"(d2), "i"((T % 3) * 128) : "memory", "scc");
}
DI u32x4 bload16(unsigned voff, u32x4 srd, unsigned soff) {
  u32x4 r;
  asm volatile("buffer_load_dwordx4 %0, %1, %2, %3 offen" : "=v"(r) : "v"(voff), "s"(srd), "s"(soff) : "memory");
  return r;
}
DI void warm(unsigned voff, u32x4 srd, unsigned soff) {
  asm volatile("buffer_load_dword a11, %0, %1, %2 offen" :: "v"(voff), "s"(srd), "s"(soff) : "memory");
}
DI u64 bload8_sc1(unsigned voff, u32x4 srd, unsigned soff) {
  u64 r;
  asm volatile("buffer_load_dwordx2 %0, %1, %2, %3 offen sc1" : "=v"(r) : "v"(voff), "s"(srd), "s"(soff) : "memory");
  return r;
}
template <int AR, int OFF> DI void aload16_sc1(unsigned voff, u32x4 srd, unsigned soff) {
  asm volatile("buffer_load_dwordx4 a[%c3:%c4], %0, %1, %2 offen offset:%c5 sc1" :: "v"(voff), "s"(srd), "s"(soff), "i"(AR), "i"(AR + 3), "i"(OFF) : "memory");
}
template <int ROLE, int K> DI void mark() { asm volatile("s_branch 1f\n\t.p2align 12\n.Lm%c0_%c1:\n\ts_setpc_b64 vcc\n1:" :: "i"(ROLE), "i"(K)); }
template <int R, int N0, int N1> DI void visit(unsigned sel) {
  constexpr auto c = [](int k, int n) { return k < n ? k : n - 1; };
  asm volatile("s_getpc_b64 vcc\n\ts_add_u32 s90, vcc_lo, %0\n\ts_addc_u32 s91, vcc_hi, 0\n\ts_add_u32 vcc_lo, vcc_lo, 52\n\ts_addc_u32 vcc_hi, vcc_hi, 0\n\ts_setpc_b64 s[90:91]\n\t"
               "s_branch .Lm0_%c1\n\ts_branch .Lm0_%c2\n\ts_branch .Lm0_%c3\n\ts_branch .Lm0_%c4\n\ts_branch .Lm1_%c5\n\ts_branch .Lm1_%c6\n\ts_branch .Lm1_%c7\n\ts_branch .Lm1_%c8"
               :: "s"(sel + 20), "i"(c(4 * R, N0)), "i"(c(4 * R + 1, N0)), "i"(c(4 * R + 2, N0)), "i"(c(4 * R + 3, N0)),
                  "i"(c(4 * R, N1)), "i"(c(4 * R + 1, N1)), "i"(c(4 * R + 2, N1)), "i"(c(4 * R + 3, N1)) : "vcc", "s90", "s91", "scc");
}
template <int N> DI void waitv() { asm volatile("s_waitcnt vmcnt(%c0)" :: "i"(N > 63 ? 63 : N) : "memory"); }
#define WAITV(n) asm volatile("s_waitcnt vmcnt(" #n ")" ::: "memory")
#define BAR() asm volatile("s_barrier" ::: "memory")
#define WAITL0() asm volatile("s_waitcnt lgkmcnt(0)" ::: "memory")
#define PIN(x) asm volatile("" : "+v"(x))
#define PINS(x) asm volatile("" : "+s"(x))

DI unsigned cvtpk(float lo, float hi) { f32x2 v = {lo, hi}; bf16x2 b = __builtin_convertvector(v, bf16x2); return __builtin_bit_cast(unsigned, b); }
DI float bflo(unsigned u) { return __uint_as_float(u << 16); }
DI float bfhi(unsigned u) { return __uint_as_float(u & 0xffff0000u); }
DI float dot2(unsigned a, unsigned b, float c) { return __builtin_amdgcn_fdot2_f32_bf16(__builtin_bit_cast(bf16x2, a), __builtin_bit_cast(bf16x2, b), c, false); }
DI float xor32(float v) { auto r = __builtin_amdgcn_permlane32_swap(__float_as_uint(v), __float_as_uint(v), false, false); return __uint_as_float(r[0]) + __uint_as_float(r[1]); }
DI float xor16(float v) { auto r = __builtin_amdgcn_permlane16_swap(__float_as_uint(v), __float_as_uint(v), false, false); return __uint_as_float(r[0]) + __uint_as_float(r[1]); }

DI bf16x4 rdhalf(LAS char* p) { return __builtin_amdgcn_ds_read_tr16_b64_v4bf16((LAS bf16x4*)p); }
DI bf16x8 cat(bf16x4 a, bf16x4 b) { return __builtin_shufflevector(a, b, 0, 1, 2, 3, 4, 5, 6, 7); }
template <bool FIRST> DI void mfma_vv(f32x4& acc, bf16x8 a, u32x4 b) {
  if constexpr (FIRST) asm volatile("v_mfma_f32_16x16x32_bf16 %0, %1, %2, 0" : "=&v"(acc) : "v"(a), "v"(b));
  else                 asm volatile("v_mfma_f32_16x16x32_bf16 %0, %1, %2, %0" : "+v"(acc) : "v"(a), "v"(b));
}
DI void mfma_vvc(f32x4& acc, u32x4 a, u32x4 b, const f32x4& c) { asm volatile("v_mfma_f32_16x16x32_bf16 %0, %1, %2, %3" : "=&v"(acc) : "v"(a), "v"(b), "v"(c)); }
template <bool FIRST, int AR> DI void mfma_va(f32x4& acc, bf16x8 a) {
  if constexpr (FIRST) asm volatile("v_mfma_f32_16x16x32_bf16 %0, %1, a[%c2:%c3], 0" : "=&v"(acc) : "v"(a), "i"(AR), "i"(AR + 3));
  else                 asm volatile("v_mfma_f32_16x16x32_bf16 %0, %1, a[%c2:%c3], %0" : "+v"(acc) : "v"(a), "i"(AR), "i"(AR + 3));
}
template <int AR> DI void mfma_k16(f32x4& acc, bf16x4 a) { asm volatile("v_mfma_f32_16x16x16_bf16 %0, %1, a[%c2:%c3], %0" : "+v"(acc) : "v"(a), "i"(AR), "i"(AR + 1)); }
template <bool FIRST, int AR> DI void mfma16_aa(f32x4& acc) {
  if constexpr (FIRST) asm volatile("v_mfma_f32_16x16x32_bf16 %0, a[%c1:%c2], a[%c1:%c2], 0" : "=&v"(acc) : "i"(AR), "i"(AR + 3));
  else                 asm volatile("v_mfma_f32_16x16x32_bf16 %0, a[%c1:%c2], a[%c1:%c2], %0" : "+v"(acc) : "i"(AR), "i"(AR + 3));
}
template <bool FIRST, int AD, int AR> DI void mfma16_ava(bf16x8 a) {
  if constexpr (FIRST) asm volatile("v_mfma_f32_16x16x32_bf16 a[%c1:%c2], %0, a[%c3:%c4], 0" :: "v"(a), "i"(AD), "i"(AD + 3), "i"(AR), "i"(AR + 3));
  else                 asm volatile("v_mfma_f32_16x16x32_bf16 a[%c1:%c2], %0, a[%c3:%c4], a[%c1:%c2]" :: "v"(a), "i"(AD), "i"(AD + 3), "i"(AR), "i"(AR + 3));
}
template <int AR> DI float aread(void) { float r; asm volatile("v_accvgpr_read_b32 %0, a[%c1]" : "=v"(r) : "i"(AR)); return r; }
template <int AR, int OFF> DI void ds_write_a128(unsigned addr) { asm volatile("ds_write_b128 %0, a[%c1:%c2] offset:%c3" :: "v"(addr), "i"(AR), "i"(AR + 3), "i"(OFF) : "memory"); }
DI float ror8(float v) { return __int_as_float(__builtin_amdgcn_update_dpp(0, __float_as_int(v), 0x128, 0xf, 0xf, false)); }
template <int AR> DI void awrite(unsigned v) { asm volatile("v_accvgpr_write_b32 a[%c1], %0" :: "v"(v), "i"(AR)); }
DI float i_fma(float a, float b, float c) { float r; asm volatile("v_fma_f32 %0, %1, %2, %3" : "=v"(r) : "v"(a), "v"(b), "v"(c)); return r; }
DI float i_mul(float a, float b) { float r; asm volatile("v_mul_f32_e32 %0, %1, %2" : "=v"(r) : "v"(a), "v"(b)); return r; }
DI float i_sqs(float a) { float r; asm volatile("v_mul_f32_e64 %0, %1, |%1|" : "=v"(r) : "v"(a)); return r; }
DI unsigned i_cvt(float a, float b) { unsigned r; asm volatile("v_cvt_pk_bf16_f32 %0, %1, %2" : "=v"(r) : "v"(a), "v"(b)); return r; }
DI unsigned i_relu(unsigned a) { unsigned r; asm volatile("v_pk_max_i16 %0, %1, 0" : "=v"(r) : "v"(a)); return r; }
DI void i_swap16(unsigned& a, unsigned& b) { asm volatile("v_permlane16_swap_b32 %0, %1" : "+v"(a), "+v"(b)); }
template <int OFF> DI void st16_sc1(u32x4 v, unsigned voff, u32x4 srd, unsigned soff) {
  asm volatile("buffer_store_dwordx4 %0, %1, %2, %3 offen offset:%c4 sc1" :: "v"(v), "v"(voff), "s"(srd), "s"(soff), "i"(OFF) : "memory");
}
template <int AR, int OFF> DI void ds_read_a64(unsigned addr) { asm volatile("ds_read_b64 a[%c1:%c2], %0 offset:%c3" :: "v"(addr), "i"(AR), "i"(AR + 1), "i"(OFF) : "memory"); }
DI void bstore8_sc1(u64 v, unsigned voff, u32x4 srd, unsigned soff) { asm volatile("buffer_store_dwordx2 %0, %1, %2, %3 offen sc1" :: "v"(v), "v"(voff), "s"(srd), "s"(soff) : "memory"); }
template <int AR, int OFF> DI void ds_write_a64(unsigned addr) { asm volatile("ds_write_b64 %0, a[%c1:%c2] offset:%c3" :: "v"(addr), "i"(AR), "i"(AR + 1), "i"(OFF) : "memory"); }

constexpr int LA = 7;
constexpr int WP1 = 4, WP2 = 2;
constexpr int NB = LA + 1, BMID = (23 - LA) & ~1;
template <bool ATTN> struct Sched {
  static constexpr int P1 = ATTN ? 504 : 432, NSTEP = ATTN ? 504 : 924, NSTAGE = ATTN ? 21 : 39;
  static constexpr int a_stage(int s) { return s < P1 ? 3 * (s / 72) + (s % 36) / 12 : s < 864 ? s / 24 : 36 + (s - 864) / 20; }
  static constexpr int a_tile(int s) { return s < P1 ? (s % 72) / 36 : s < 864 ? (s % 24) / 12 : ((s - 864) % 20) / 10; }
  static constexpr int a_ks(int s) { return s < 864 ? s % 12 : (s - 864) % 10; }
  static constexpr bool skip(int s) { return ATTN && s >= 108 && s < 144 && s % 2 == 1; }
  static constexpr int g_stage(int s) {
    if (s < 0) return (s + 24) / 6;
    if (s >= 864 || s % 4 != 1) return -1;
    const int st = s / 24 + 4;
    return st >= NSTAGE || (st >= 36 && (s % 24) / 4 == 5) ? -1 : st;
  }
  static constexpr int g_piece(int s) { return (s % 24) / 4; }
  static constexpr int g_last(int st) { int r = -25; for (int s = -24; s < NSTEP; ++s) if (g_stage(s) == st) r = s; return r; }
  static constexpr int warm_k(int s) { return s >= 0 && s % 48 == 0 && s / 48 < 5 ? s / 48 : -1; }
  static constexpr int PUB_HA = 288 + BMID, PUB_HB = 504, POLL_HA = 388, CHK_HA = 432, POLL_HB = 580, CHK_HB = 624, POLL_O = 792, CHK_O = 828;
  static constexpr int h_load(int s) {
    if (ATTN || s % 4 != 3) return -1;
    return s >= 432 && s < 480 ? (s - 432) / 4 : s >= 504 && s < 600 ? (s % 8 == 3 ? 12 + (s - 504) / 8 : -1) : s >= 624 && s < 720 ? 24 + (s - 624) / 4 : -1;
  }
  static constexpr int o_load(int s) { return !ATTN && s > CHK_O && s < CHK_O + 19 && s % 4 > 1 ? (s - CHK_O) / 4 * 2 + s % 4 - 2 : -1; }
  static constexpr int x_load(int s) { return !ATTN && s >= 723 && s < 771 && s % 16 == 3 ? (s - 723) / 16 : -1; }
  static constexpr int NMARK = ATTN ? 9 : 11;
  static constexpr int mark_k(int s) {
    constexpr int M[] = {2, 84, 162, 240, 318, 396, 474, 574, 674, 774, 876}, A[] = {16, 98, 190, 268, 348, 430, 492};
    if (ATTN) { for (int k = 0; k < 7; ++k) if (s == A[k]) return k; }
    else { for (int k = 0; k < 11; ++k) if (s == M[k]) return k; }
    return -1;
  }
  static constexpr bool bar(int s) { return s < P1 ? (s % 72 == 0 || s % 72 == BMID || s % 72 == 48) : (s <= 864 && s % 24 == 0); }
  static constexpr bool lwait(int s) { return (s + 1) % (s < 468 && !ATTN ? WP1 : WP2) == 0; }
  static constexpr int bias_rd(int s) { return !ATTN && s < 432 ? (s % 36 == 28 ? 0 : s % 36 == 35 ? 1 : -1) : -1; }
  static constexpr bool g_set(int s) { return g_stage(s + 3) >= 0 && g_piece(s + 3) % 3 == 0; }
  static constexpr bool fixed(int s, int h) {
    if (bar(s) || g_set(s)) return true;
    return h == 0 ? (g_stage(s) >= 0 || h_load(s) >= 0 || o_load(s) >= 0 || x_load(s) >= 0 || bias_rd(s) >= 0) : lwait(s);
  }
  static constexpr int ST0 = 30, ST1 = 57;
  static constexpr int win0(int s) { return s < 864 ? s / 36 * 36 : 864 + (s - 864) / 10 * 10; }
  static constexpr int wlen(int s) { return s < 864 ? 36 : 10; }
  static constexpr int nop(int s) { return ATTN ? (s >= 36 ? 32 : 0) : s >= 36 && s < 468 ? 59 : s >= 874 ? 14 : 0; }
  static constexpr int epi_to(int s, int h) {
    int f = 0, u = 0;
    for (int q = 2 * win0(s) + 2; q < 2 * (win0(s) + wlen(s)); ++q) if (!fixed(q / 2, q % 2)) { ++f; if (q <= 2 * s + h) ++u; }
    return f >= nop(s) ? (u < nop(s) ? u : nop(s)) : u * nop(s) / f;
  }
  static constexpr int epi_from(int s, int h) { return 2 * s + h <= 2 * win0(s) + 2 ? 0 : h ? epi_to(s, 0) : epi_to(s - 1, 1); }
  static constexpr bool has_op(int s, int k) { return epi_from(s, 0) <= k && k < epi_to(s, 1); }
  static constexpr int stores(int s) { return ATTN || s < 0 || !nop(s) ? 0 : s < 864 ? has_op(s, ST0) + has_op(s, ST1) : has_op(s, 10) + has_op(s, 13); }
  static constexpr int vm(int s) {
    int c = (g_stage(s) >= 0) + (warm_k(s) >= 0) + stores(s);
    if (!ATTN && s >= 0) c += (h_load(s) >= 0) + (o_load(s) >= 0) + (s == POLL_HA) + (s == POLL_HB) + (s == POLL_O) + (x_load(s) >= 0);
    return c;
  }
  static constexpr int need(int s) {
    int r = -25;
    if (bar(s) && !(ATTN && s == 480)) r = g_last(s < P1 ? 3 * (s / 72) + (s % 72 == 0 ? 1 : s % 72 == BMID ? 2 : 3) : s < 864 ? s / 24 + 1 : 38);
    if (!ATTN) {
      const int e = s == PUB_HA || s == PUB_HB ? s - 1 : s == CHK_HA ? POLL_HA : s == CHK_HB ? POLL_HB : s == 646 ? 595 : s == CHK_O ? POLL_O : s == 720 ? 671 : s == 768 ? 719 : s == 863 ? 862 : -25;
      if (e > r) r = e;
    }
    return r;
  }
  static constexpr int allow(int s) { int c = 0; for (int k = need(s) + 1; k < s; ++k) c += vm(k); return c; }
  static constexpr bool ring_ok(int st) {
    int first = NSTEP, last = -1, refill = NSTEP;
    for (int s = 0; s < NSTEP; ++s) {
      if (a_stage(s) == st) { if (s - LA < first) first = s - LA; last = s; }
      if (ATTN && s >= 144 && s % 4 == 2 && 3 * (s / 72) + (s % 72) / 24 == st) { if (s - 8 < first) first = s - 8; if (s + 1 > last) last = s + 1; }
      if (g_stage(s) == st + 6 && s < refill) refill = s;
    }
    bool vis = st == 0, fre = refill == NSTEP;
    for (int b = 0; b < NSTEP; b += 2) if (bar(b)) { if (b < first && b + 72 > first && need(b) >= g_last(st)) vis = true; if (b >= last && b < refill) fre = true; }
    return vis && fre;
  }
};
template <bool ATTN, int... I> constexpr bool rings_ok(std::integer_sequence<int, I...>) { return (std::bool_constant<Sched<ATTN>::ring_ok(I)>::value && ...); }
static_assert(rings_ok<false>(std::make_integer_sequence<int, 39>{}) && rings_ok<true>(std::make_integer_sequence<int, 21>{}), "LDS ring protocol");
constexpr int kb_areg(int kb) { return kb == 0 ? A_H : kb == 1 ? A_H + 48 : kb == 2 ? A_H + 96 : kb == 3 ? A_H + 144 : kb == 4 ? A_H : kb == 5 ? A_H + 48 : A_H + 96; }
constexpr int qkv_pair(int kg, int p) {
  constexpr signed char T[4][28] = {
    {0,2,4,6, 16,8,10,-1, 18,20,22,24, 26,28,30,32, 12,14,34,52, 36,38,40,42, 44,46,48,50},
    {6,8,10,12, 0,2,4,-1, 24,26,28,30, 14,16,32,34, 18,20,22,50, 42,44,46,48, 36,38,40,52},
    {4,6,8,10, 12,14,16,-1, 22,24,26,28, 0,2,18,20, 30,32,34,36, 40,42,44,46, 48,50,52,38},
    {2,4,6,8, 0,10,12,-1, 20,22,24,26, 28,30,32,34, 14,16,18,36, 38,40,42,44, 46,48,50,52}};
  return T[kg][p];
}
struct QkvTab { unsigned v[4][16]; };
constexpr QkvTab make_tab() {
  QkvTab t = {};
  for (int kg = 0; kg < 4; ++kg)
    for (int p = 0; p < 28; ++p) {
      const int v = qkv_pair(kg, p), sv = v < 0 ? qkv_pair(kg, p - 1) : v;
      t.v[kg][p / 4] |= (unsigned)((sv / 18) * 72 + sv % 18) << (8 * (p % 4));
      t.v[kg][8 + p / 4] |= (unsigned)(sv >= 36 ? sv - 36 : sv) << (8 * (p % 4));
    }
  return t;
}

DI void check_flag(u64 f, u64 tag, unsigned voff, u32x4 srd, unsigned soff) {
  unsigned n;
  asm volatile("v_cmp_ne_u64_e32 vcc, %1, %2\n\ts_cbranch_vccz 1f\n\ts_mov_b32 %0, 0x40000\n2:\n\ts_sleep 1\n\t"
               "buffer_load_dwordx2 %1, %3, %4, %5 offen sc1\n\ts_sub_u32 %0, %0, 1\n\ts_waitcnt vmcnt(0)\n\ts_cbranch_scc1 1f\n\t"
               "v_cmp_ne_u64_e32 vcc, %1, %2\n\ts_cbranch_vccnz 2b\n1:"
               : "=&s"(n), "+v"(f) : "v"(tag), "v"(voff), "s"(srd), "s"(soff) : "vcc", "scc", "memory");
}
DI void spin_until(gu64* p, u64 tag) {
#pragma unroll 1
  for (unsigned spins = 0; spins < (1u << 18); ++spins) {
    const u64 f = __hip_atomic_load(p, RLX_AGENT);
    if (__all(f == tag)) break;
  }
}

struct Args { const void *x, *wqkv, *wo, *w1, *b1, *w2, *b2; void *out, *ws; QkvTab qt; };
static_assert(sizeof(Args) == 72 + 256, "no padding");
DI char* as_ptr(unsigned lo, unsigned hi) { return (char*)(((u64)hi << 32) | lo); }

__global__ __launch_bounds__(256, 1) void main_k(const Args) {
  extern __shared__ __attribute__((aligned(16))) char smem[];
  LAS char* lds = (LAS char*)smem;
  const int tid = threadIdx.x, lane = tid & 63, bid = blockIdx.x;
  const int kg = map_kg(bid), cg = map_role(bid), rt = map_rt(bid);
  u32x16 ka, kt; u32x2 kw;
  asm volatile("s_load_dwordx16 %0, %3, 0x0\n\ts_load_dwordx2 %1, %3, 0x40\n\ts_load_dwordx16 %2, %3, %4 offset:0x48"
               : "=&s"(ka), "=&s"(kw), "=&s"(kt) : "s"(__builtin_amdgcn_kernarg_segment_ptr()), "s"(kg * 64));
  const void* code;
  unsigned code_bytes;
  asm volatile("s_getpc_b64 %0\n.Lk_pc:\n\ts_mov_b32 %1, .Lk_end0-.Lk_pc\n\ts_max_i32 %1, %1, .Lk_end1-.Lk_pc" : "=s"(code), "=s"(code_bytes) :: "scc", "a255");
  const int w = __builtin_amdgcn_readfirstlane(tid >> 6);
  const bool attn = cg == 3;
  const int tokbase = 256 * (rt >> 1) + (rt & 1);
  const int n16 = lane & 15, r8 = lane >> 3, pos = lane & 7;
  const int G = lane >> 4, q4 = (lane >> 2) & 3, p4 = lane & 3;

  const unsigned rot = (0x24924924u >> (2 * ((rt & 7) + cg))) & 3;
  unsigned kro[3] = {rot * 192, rot == 2 ? 0 : rot * 192 + 192, rot == 0 ? 384 : rot * 192 - 192};
  unsigned m6 = 6 * w * 1024, sx = tokbase * 1152 + w * 32 * 2304;
  unsigned vx0 = r8 * 2304 + ((pos ^ G) << 4), vx1 = vx0 ^ 64;
  PINS(m6); PINS(sx); PIN(vx0); PIN(vx1); PINS(kro[0]); PINS(kro[1]); PINS(kro[2]);
  const unsigned swz = pos ^ (((r8 >> 1) & 3) << 1);
  const int fs = 3 * kg + cg, f0 = fs * 384;
  unsigned strideA = attn ? 3456u : 9216u;
  PINS(strideA);
  unsigned soA[3];
#pragma unroll
  for (int j = 0; j < 3; ++j) { soA[j] = (kro[j] + 48 * w) * strideA + (attn ? 288 * kg : f0 * 2); PINS(soA[j]); }
  unsigned vab = r8 * strideA + (swz & 1) * 16, vsh = 8 * (swz >> 1), vcode = tid < 64 ? tid * 128 : OOB;
  PIN(vab); PIN(vsh); PIN(vcode);
  u32x4 id0, id1;
  {
    const unsigned one = 0x3f80u << (16 * (n16 & 1));
    const int e = (n16 >> 1) - 4 * G;
#pragma unroll
    for (int j = 0; j < 4; ++j) { id0[j] = e == j ? one : 0u; id1[j] = e + 8 == j ? one : 0u; }
  }
  PIN(id0); PIN(id1);
  unsigned po0 = (4 * G + q4) * 128 + (((2 * G + (q4 >> 1)) & 3) << 5) + p4 * 8;
  LAS char* rb[4][3];
#pragma unroll
  for (int i = 0; i < 3; ++i)
#pragma unroll
    for (int c = 0; c < 4; ++c) { rb[c][i] = lds + (po0 ^ (c << 5)) + 2 * i * SLOT; PIN(rb[c][i]); }
  LAS char* xp[2][2];
#pragma unroll
  for (int u = 0; u < 2; ++u) {
    const unsigned xr0 = w * 6144 + (n16 >> 3) * 1024 + (n16 & 7) * 128 + (((G ^ (n16 >> 1)) << 4) ^ (u << 6));
    xp[0][u] = lds + xr0 + 4 * SLOT; xp[1][u] = lds + xr0 + 2 * SLOT; PIN(xp[0][u]); PIN(xp[1][u]);
  }
  unsigned ones = 0x3f803f80u; PIN(ones);
  const int c1 = cg == 2 ? 0 : cg + 1, c2 = cg == 0 ? 2 : cg - 1;
  unsigned soB[3];
  soB[0] = (f0 + 48 * w) * 1152 + 384 * cg; soB[1] = ((3 * kg + c1) * 384 + 48 * w) * 1152 + 384 * cg; soB[2] = ((3 * kg + c2) * 384 + 48 * w) * 1152 + 384 * cg;
  const int p5 = w == 3 ? 13 : 5 * w;
  unsigned soO = (144 * kg + 8 * p5) * 1152 + 384 * cg, m5 = p5 * 1024;
  unsigned hso[3];
  hso[0] = H_OFF + ((rt * 12 + fs) * 4 + w) * 24576; hso[1] = H_OFF + ((rt * 12 + 3 * kg + c1) * 4 + w) * 24576; hso[2] = H_OFF + ((rt * 12 + 3 * kg + c2) * 4 + w) * 24576;
  unsigned oso = O_OFF + ((rt * 4 + kg) * 4 + w) * 9216;
  unsigned pso = P_OFF + (((rt * 3 + cg) * 4 + kg) * 4 + w) * 12288;
  unsigned os[3];
#pragma unroll
  for (int k = 0; k < 3; ++k) { const int un = 3 * kg + k; os[k] = (tokbase + 64 * w + 32 * (un & 1)) * 1152 + (192 * cg + 32 * (un >> 1)) * 2; }
#pragma unroll
  for (int k = 0; k < 3; ++k) { PINS(soB[k]); PINS(hso[k]); PINS(os[k]); }
  PINS(pso); PINS(oso); PINS(soO); PINS(m5);
  asm volatile("s_waitcnt lgkmcnt(0)" : "+s"(ka), "+s"(kw), "+s"(kt));
  const char *x = as_ptr(ka[0], ka[1]), *wo = as_ptr(ka[4], ka[5]), *b1 = as_ptr(ka[8], ka[9]), *w2 = as_ptr(ka[10], ka[11]), *b2 = as_ptr(ka[12], ka[13]);
  char *out = as_ptr(ka[14], ka[15]), *ws = as_ptr(kw[0], kw[1]);
  const u32x4 srdx = make_srd(x, S_ * D_ * 2);
  auto x_more = [&](auto T) { const unsigned b = sx + kro[T / 3] * 2; x_line<T>(m6, vx0, vx1, srdx, b, b + 8 * 2304, b + 16 * 2304, b + 24 * 2304); };
  sfor<3>(x_more);
  const unsigned vsel = 4 * w + (attn ? 16 : 0);
  visit<0, Sched<false>::NMARK, Sched<true>::NMARK>(vsel);
  u32x4 srdA = make_srd(attn ? as_ptr(ka[2], ka[3]) : as_ptr(ka[6], ka[7]), attn ? D_ * 3 * D_ * 2 : D_ * F_ * 2);
  PINS(srdA);
  unsigned qs[7], qd[7];
#pragma unroll
  for (int i = 0; i < 7; ++i) { qs[i] = kt[i]; qd[i] = kt[8 + i]; }
  auto va_of = [&](unsigned word) { return vab + ((word >> vsh) & 255) * 16; };
  unsigned va = va_of(attn ? qs[0] : 0x06040200u), so = soA[0] - 8 * strideA;
  auto w_piece = [&](auto K) {
    constexpr int n = K / 6, p = K % 6;
    if constexpr (p == 0 && n > 0) so = soA[n % 3] - 8 * strideA;
    if constexpr (K == 18) va = va_of(attn ? qs[1] : 0x0e0c0a08u);
    glds16r<n * SLOT + p * 1024>(m6, va, srdA, so, 8 * strideA);
  };
  sfor<6>(w_piece);
  warm(vcode, make_srd(code, code_bytes), ((bid >> 3) & 15) * 8192);
  visit<1, Sched<false>::NMARK, Sched<true>::NMARK>(vsel);
  const u32x4 srdws = make_srd(ws, WS_BYTES);
  const unsigned flo = FLAG_OFF + rt * FL_N * 128;
  auto misc_loads = [&]() {
    asm volatile("buffer_load_dwordx2 a[12:13], %0, %1, %2 offen sc1" :: "v"(0), "s"(srdws), "s"(flo + FL_EPOCH * 128) : "memory");
    if (attn) {
      int ht = tokbase + 2 * (r8 - 8); ht = ht < 0 ? 0 : ht;
      const unsigned vh = ht * 1152 + pos * 16;
      const unsigned kb = ((0x24924924u >> (2 * (rot + w))) & 3) * 384 | (w == 3 ? OOB : 0);
      glds16c<TAB_OFF, 0>(w * 3072, vh, srdx, kb); glds16c<TAB_OFF + 1024, 128>(w * 3072, vh, srdx, kb); glds16c<TAB_OFF + 2048, 256>(w * 3072, vh, srdx, kb);
    } else {
      asm volatile("buffer_load_dword a14, %0, %1, 0 offen" :: "v"(tid * 4), "s"(make_srd(b1 + f0 * 2, 768)) : "memory");
      asm volatile("buffer_load_dword a15, %0, %1, 0 offen" :: "v"(tid * 4), "s"(make_srd(b2 + 192 * cg * 2, 384)) : "memory");
    }
  };
  u32x4 xf[36];
  f32x4 nm[2];
  float rstd[2];
  {
    float sm[2] = {0.f, 0.f}, sq[2] = {0.f, 0.f};
    SB();
    sfor<10>([&](auto T) {
      constexpr int t = T;
      if constexpr (t == 0) x_more(IC(3));
      if constexpr (t + 4 < 9) x_more(IC(t + 4));
      if constexpr (t == 4) misc_loads();
      if constexpr (t == 0) visit<2, Sched<false>::NMARK, Sched<true>::NMARK>(vsel);
      if constexpr (t < 9) {
        waitv<4 * ((t + 4 > 8 ? 8 : t + 4) - t) + (t < 3 ? 6 : 0)>();
#pragma unroll
        for (int u = 0; u < 4; ++u) xf[4 * t + u] = *(LAS u32x4*)(xp[t / 3 == 1][u >> 1] + (t % 3 == 1 ? 4096 + (u & 1) * SLOT : (t % 3 == 2 ? SLOT : 0) + (u & 1) * 2048));
      }
      SB();
      if constexpr (t > 0) {
#pragma unroll
        for (int u = 0; u < 4; u += 2)
#pragma unroll
          for (int j = 0; j < 4; ++j) {
            const unsigned v0 = xf[4 * t - 4 + u][j], v1 = xf[4 * t - 3 + u][j];
            sm[0] = dot2(v0, ones, sm[0]); sm[1] = dot2(v1, ones, sm[1]); sq[0] = dot2(v0, v0, sq[0]); sq[1] = dot2(v1, v1, sq[1]);
          }
        PIN(sm[0]); PIN(sm[1]); PIN(sq[0]); PIN(sq[1]);
      }
      if constexpr (t == 8) sfor<6>([&](auto P) { w_piece(IC(6 + P)); });
      SB();
    });
#pragma unroll
    for (int tb = 0; tb < 2; ++tb) {
      const float negmu = xor32(xor16(sm[tb])) * (-1.0f / D_);
      nm[tb] = f32x4{negmu, negmu, negmu, negmu};
      rstd[tb] = __builtin_amdgcn_rsqf(fmaxf(xor32(xor16(sq[tb])) * (1.0f / D_) - negmu * negmu, 0.f) + 1e-5f);
    }
  }
  bf16x4 wl[NB], wu[NB];
  {
    f32x4 dl[3], dh[3];
    asm volatile("s_nop 1" : "+v"(nm[0]), "+v"(nm[1]));
    sfor<38>([&](auto B) {
      constexpr int b = B;
      if constexpr (b < 36) mfma_vvc(dl[b % 3], id0, xf[b], nm[b & 1]);
      if constexpr (b == 36) {
        asm volatile("s_waitcnt vmcnt(18)\n\ts_barrier" : "+v"(dl[1]), "+v"(dh[1]), "+v"(dl[2]), "+v"(dh[2]) : "v"(nm[0]), "v"(nm[1]) : "memory");
#pragma unroll
        for (int t = 0; t < LA; ++t) { wl[t] = rdhalf(rb[t & 1][0] + (t >> 1) * 4096); wu[t] = rdhalf(rb[t & 1][0] + (t >> 1) * 4096 + 2048); }
      }
      SB();
      if constexpr (b >= 2) { xf[b - 2][0] = cvtpk(dl[(b - 2) % 3][0], dl[(b - 2) % 3][1]); xf[b - 2][1] = cvtpk(dl[(b - 2) % 3][2], dl[(b - 2) % 3][3]); }
      SB();
      if constexpr (b < 36) mfma_vvc(dh[b % 3], id1, xf[b], nm[b & 1]);
      SB();
      if constexpr (b >= 2) { xf[b - 2][2] = cvtpk(dh[(b - 2) % 3][0], dh[(b - 2) % 3][1]); xf[b - 2][3] = cvtpk(dh[(b - 2) % 3][2], dh[(b - 2) % 3][3]); PIN(xf[b - 2]); }
      if constexpr (b % 2 == 0 && b < 24) w_piece(IC(12 + b / 2));
      SB();
    });
  }

  const u32x4 srdW2 = make_srd(w2, F_ * D_ * 2), srdWo = make_srd(wo, D_ * D_ * 2);
  const __amdgpu_buffer_rsrc_t rws = __builtin_amdgcn_make_buffer_rsrc(ws, 0, WS_BYTES, 0x00020000);
  gu64* flags = (gu64*)((char*)ws + flo);
  auto get_tag = [&]() __attribute__((always_inline)) { return (((u64)__float_as_uint(aread<13>()) << 32) | __float_as_uint(aread<12>())) * 0x9E3779B97F4A7C15ull + 0x632BE59BD9B4E019ull; };
  float t0, t1;
  f32x4 acc[2][2][2];
#define LANE_VALUES int tid_ = threadIdx.x; PIN(tid_); \
  const int tid = tid_, lane = tid & 63, n16 = lane & 15, r8 = lane >> 3, pos = lane & 7, G = lane >> 4, q4 = (lane >> 2) & 3, p4 = lane & 3; \
  const unsigned swz = pos ^ (((r8 >> 1) & 3) << 1), l16 = lane * 16, vflag = tid == 0 ? 0u : OOB, wa = rt * 72 + w * 18 + lane, wm = lane < 18 ? 0 : OOB; \
  (void)q4; (void)p4;

  if (!attn) {
    using SC = Sched<false>;
    LANE_VALUES
    constexpr bool attn_role = false;
    auto rd_a = [&](auto Sc, auto U) {
      constexpr int s = Sc, u = U;
      if constexpr (s < SC::NSTEP && !SC::skip(s) && !(u == 1 && s >= 864 && SC::a_ks(s) >= 8)) {
        constexpr int sl = SC::a_stage(s) % 6, ks = SC::a_ks(s);
        (u ? wu : wl)[s % NB] = rdhalf(rb[2 * SC::a_tile(s) + (ks & 1)][sl >> 1] + (sl & 1) * SLOT + (ks >> 1) * 4096 + u * 2048);
      }
    };
    auto mma1 = [&](auto Sc, auto H) {
      constexpr int s = Sc, k = s % 36, o = k & 1, tb = o ^ H;
      if constexpr (!SC::skip(s)) mfma_vv<k < 2>(acc[SC::a_tile(s)][o][tb], cat(wl[s % NB], wu[s % NB]), xf[(k & ~1) + tb]);
    };
    auto sync = [&](auto Sc, auto H) {
      constexpr int s = Sc, h = H;
      if constexpr (h == 0 && SC::need(s) > -25) waitv<SC::allow(s)>();
      if constexpr (h == 1 && (s == BMID || (attn_role && s == 480))) WAITL0();
      if constexpr (h == 1 && SC::bar(s)) BAR();
      if constexpr (h == 1 && SC::lwait(s)) __builtin_amdgcn_s_waitcnt(0xC07F | ((2 * (LA - (s < 468 && !attn_role ? WP1 : WP2))) << 8));
    };
    unsigned vA[3] = {va - 128, va - 128 + 8 * 9216 - 1024, va - 128 + 2 * (8 * 9216 - 1024)}, vB[3], lk[3], gso = sadd<128>(soA[1]), hst = hso[0];
    set_m0<4 * SLOT>(m6);
    u64 tag;
    unsigned vpoll, pso_;
    LAS char* tab = lds + TAB_OFF + G * 16;
    f32x4 bs[2], bn;
    f32x4 ac[6][2][2];
    unsigned e[8];
    u64 pollv = 0;
    const unsigned ov = n16 * 2304 + (G & 1) * 32 + (G >> 1) * 16;
    u32x4 xr[3];
    float u0[2], u1[2];
    auto op1 = [&](auto Tc, auto K) {
      constexpr int T = Tc, k = K;
      if constexpr (k == SC::ST0 || k == SC::ST1) { constexpr int q = k == SC::ST1; st16_sc1<(T & 1) * 2048 + q * 1024>(u32x4{e[4 * q], e[4 * q + 1], e[4 * q + 2], e[4 * q + 3]}, l16, srdws, hst); }
      else if constexpr (k == 58) { if constexpr (T & 1) hst = sadd<4096>(hst); }
      else {
        constexpr int kk = k > SC::ST0 ? k - 1 : k, r = (kk - 4) % 7;
        constexpr int j = kk < 4 ? kk : kk >= 53 ? kk - 49 : r == 0 ? 4 : r == 1 ? 0 : r == 2 ? 5 : r == 3 ? 1 : r == 4 ? 2 : r == 5 ? 3 : 6;
        constexpr int d = kk < 4 ? 0 : kk >= 53 ? 7 : (kk - 4) / 7 + (j < 4), hf = d & 1;
        const f32x4& a = acc[T & 1][(d >> 1) & 1][d >> 2];
        if constexpr (j == 0) u0[hf] = i_fma(a[2 * hf], rstd[d >> 2], bs[(d >> 1) & 1][2 * hf]);
        if constexpr (j == 1) u1[hf] = i_fma(a[2 * hf + 1], rstd[d >> 2], bs[(d >> 1) & 1][2 * hf + 1]);
        if constexpr (j == 2) u0[hf] = i_sqs(u0[hf]);
        if constexpr (j == 3) u1[hf] = i_sqs(u1[hf]);
        if constexpr (j == 4) e[d] = i_cvt(u0[hf], u1[hf]);
        if constexpr (j == 5) e[d] = i_relu(e[d]);
        if constexpr (j == 6) awrite<A_H + 8 * T + d>(e[d]);
      }
    };
    auto op2 = [&](auto Tc, auto K) {
      constexpr int T = Tc, k = K;
      if constexpr (k < 8) e[k] = i_cvt(ac[T][(k >> 1) & 1][k >> 2][2 * (k & 1)], ac[T][(k >> 1) & 1][k >> 2][2 * (k & 1) + 1]);
      else {
        constexpr int q = (k - 8) / 3, j = (k - 8) % 3;
        if constexpr (j < 2) i_swap16(e[4 * q + j], e[4 * q + 2 + j]);
        else st16_sc1<(T & 1) * 2048 + q * 1024>(u32x4{e[4 * q], e[4 * q + 1], e[4 * q + 2], e[4 * q + 3]}, lk[T >> 1], srdws, pso);
      }
    };
    auto slot = [&](auto Sc, auto H) {
      constexpr int s = Sc, h = H;
      sync(Sc, H);
      if constexpr (SC::g_set(s)) {
        constexpr int np = SC::g_stage(s + 3), p = SC::g_piece(s + 3), kb = (np - 18) / 3, src = kb < 2 ? 0 : 1 + (kb & 1), half = kb < 2 ? kb : (kb >= 4);
        if constexpr (h == 0) set_m0<(np % 6) * SLOT + p * 1024>(np < 36 ? m6 : m5);
        else if constexpr (np < 18) gso = sadd<8 * p * 9216 + 128 * (np / 3)>(soA[np % 3]);
        else if constexpr (np < 36) gso = sadd<(192 * half + 8 * p) * 1152 + 128 * ((np - 18) % 3)>(soB[src]);
        else gso = sadd<8 * p * 1152 + 128 * (np - 36)>(soO);
      }
      if constexpr (h == 0) {
        if constexpr (SC::g_stage(s) >= 0) { constexpr int np = SC::g_stage(s), j = SC::g_piece(s) % 3; glds3<j>(np < 18 ? vA[j] : vB[j], np < 18 ? srdA : np < 36 ? srdW2 : srdWo, gso); }
        if constexpr (SC::bias_rd(s) == 0) bn = *(LAS f32x4*)(tab + (s / 36) * 128);
        if constexpr (SC::bias_rd(s) == 1) { bs[0] = bn; bs[1] = *(LAS f32x4*)(tab + (s / 36) * 128 + 64); }
        if constexpr (SC::x_load(s) >= 0) { constexpr int k = SC::x_load(s); xr[k] = bload16(ov, srdx, os[k]); }
        if constexpr (SC::o_load(s) >= 0) { constexpr int f = SC::o_load(s); aload16_sc1<kb_areg(6) + 4 * f, (f & 3) * 1024>(lk[f >> 2], srdws, oso); }
        if constexpr (SC::h_load(s) >= 0) { constexpr int b = SC::h_load(s) / 12, f = SC::h_load(s) % 12; aload16_sc1<kb_areg(2 + b) + 4 * f, (f & 3) * 1024>(lk[f >> 2], srdws, hso[1 + (b & 1)]); }
      }
      if constexpr (SC::nop(s) > 0) {
        static_assert(SC::bias_rd(s) != 1 || s < 36 || SC::epi_from(s, 0) > 52, "the bias registers are free");
        constexpr int k0 = SC::epi_from(s, h), k1 = SC::epi_to(s, h);
        sfor<k1 - k0>([&](auto I) { if constexpr (s < 864) op1(IC(s / 36 - 1), IC(k0 + I)); else op2(IC((s - 864) / 10 - 1), IC(k0 + I)); if constexpr (s < 864 && k0 + I == SC::ST0 && k0 + I + 1 < k1) asm volatile("s_nop 1"); });
      }
    };
    sfor<77>([&](auto So) { sfor<12>([&](auto Si) {
      constexpr int s = 12 * So + Si;
      sfor<2>([&](auto H) {
        if constexpr (s < 432) mma1(IC(s), H);
        else {
          constexpr int n = SC::a_stage(s) - 18, kb = n / 3, T = 2 * (n % 3) + SC::a_tile(s), ks = SC::a_ks(s), o = ks & 1, tb = o ^ H, ar = kb_areg(kb) + 8 * (ks >> 1);
          if constexpr (kb == 6 && ks >= 8) mfma_k16<ar + 2 * tb>(ac[T][o][tb], wl[s % NB]);
          else mfma_va<kb == 0 && ks < 2, ar + 4 * tb>(ac[T][o][tb], cat(wl[s % NB], wu[s % NB]));
        }
        SB(); rd_a(IC(s + LA), H); SB();
        slot(IC(s), H); SB();
      });
      constexpr int k = SC::warm_k(s);
      if constexpr (SC::mark_k(s) >= 0) mark<0, SC::mark_k(s)>();
      if constexpr (k >= 0 && k < 2) warm(((wa >> 1) * 9216 + (wa & 1) * 128) | wm, srdA, f0 * 2 + 256 + 256 * k);
      if constexpr (k >= 2) warm((wa * 128) | wm, srdW2, f0 * 1152 + (k - 2) * 147456);
      if constexpr (s == SC::PUB_HA || s == SC::PUB_HB) bstore8_sc1(tag, vflag, srdws, flo + ((s == SC::PUB_HA ? FL_HA : FL_HB) + fs) * 128);
      if constexpr (s == SC::CHK_HA || s == SC::CHK_HB || s == SC::CHK_O) check_flag(pollv, tag, vpoll, srdws, pso_);
      if constexpr (s == 4) { const unsigned r = __float_as_uint(aread<14>()); *(LAS f32x2*)(lds + TAB_OFF + tid * 8) = f32x2{bflo(r), bfhi(r)}; }
      if constexpr (s == 8) { const unsigned r = __float_as_uint(aread<15>()); *(LAS f32x2*)(lds + TAB_OFF + 2048 + tid * 8) = f32x2{bflo(r), bfhi(r)}; }
      if constexpr (s == 12) { tag = get_tag(); PIN(tag); }
      if constexpr (s == 334) { vB[0] = r8 * 1152 + swz * 16; vB[1] = vB[0] + 8192; vB[2] = vB[0] + 16384; PIN(vB[0]); PIN(vB[1]); PIN(vB[2]); }
      if constexpr (s == 386) { vpoll = (3 * kg + ((lane & 1) ? c1 : c2)) * 128; PIN(vpoll); }
      if constexpr (s == 432) { lk[0] = l16; lk[1] = l16 + 4096; lk[2] = l16 + 8192; PIN(lk[1]); PIN(lk[2]); }
      if constexpr (s == 600) { hso[1] += 12288; hso[2] += 12288; PINS(hso[1]); PINS(hso[2]); }
      if constexpr (s == SC::POLL_HA || s == SC::POLL_HB || s == SC::POLL_O) {
        if constexpr (s == SC::POLL_O) vpoll = kg * 128;
        pso_ = flo + (s == SC::POLL_HA ? FL_HA : s == SC::POLL_HB ? FL_HB : FL_O) * 128;
        pollv = bload8_sc1(vpoll, srdws, pso_);
      }
      SB();
    }); });
    asm volatile("s_nop 7" : "+v"(ac[5][0][0]), "+v"(ac[5][0][1]), "+v"(ac[5][1][0]), "+v"(ac[5][1][1]));
    sfor<14>([&](auto K) { op2(IC(5), K); SB(); });
    asm volatile("s_nop 1" : "+v"(xr[0]), "+v"(xr[1]), "+v"(xr[2]));
    SB();
    float a[3][8];
    f32x4 cb[3][2];
#pragma unroll
    for (int k = 0; k < 3; ++k) {
      LAS char* bp = lds + TAB_OFF + 2048 + 128 * ((3 * kg + k) >> 1) + (G & 1) * 64 + (G >> 1) * 32;
      cb[k][0] = *(LAS f32x4*)bp; cb[k][1] = *(LAS f32x4*)(bp + 16);
    }
    gu64* fp = flags + (FL_FIN + 3 * (lane & 3) + cg) * 16;
    PIN(fp);
    SB();
#pragma unroll
    for (int k = 0; k < 3; ++k) {
      const f32x4 c0 = cb[k][0], c1 = cb[k][1];
#pragma unroll
      for (int j = 0; j < 4; ++j) { a[k][2 * j] = bflo(xr[k][j]) + (j < 2 ? c0 : c1)[(2 * j) & 3]; a[k][2 * j + 1] = bfhi(xr[k][j]) + (j < 2 ? c0 : c1)[(2 * j + 1) & 3]; PIN(a[k][2 * j]); PIN(a[k][2 * j + 1]); }
    }
    unsigned sel0 = 0x00003f80u, sel1 = 0x3f800000u, pb = P_OFF + ((rt * 3 + cg) * 16 + w) * 12288 + 3 * kg * 1024;
    PIN(sel0); PIN(sel1); PINS(pb);
    WAITV(0); BAR();
    bstore8_sc1(tag, vflag, srdws, flo + (FL_FIN + fs) * 128);
    spin_until(fp, tag);
    const __amdgpu_buffer_rsrc_t rout = __builtin_amdgcn_make_buffer_rsrc(out, 0, S_ * D_ * 2, 0x00020000);
    u32x4 v[3][4];
#pragma unroll
    for (int k = 0; k < 3; ++k)
#pragma unroll
      for (int s = 0; s < 4; ++s) v[k][s] = __builtin_amdgcn_raw_buffer_load_b128(rws, l16 + k * 1024, pb + s * 4 * 12288, 16);
#pragma unroll
    for (int k = 0; k < 3; ++k) {
#pragma unroll
      for (int s = 0; s < 4; ++s)
#pragma unroll
        for (int j = 0; j < 4; ++j) { a[k][2 * j] = dot2(v[k][s][j], sel0, a[k][2 * j]); a[k][2 * j + 1] = dot2(v[k][s][j], sel1, a[k][2 * j + 1]); }
      __builtin_amdgcn_raw_buffer_store_b128(u32x4{cvtpk(a[k][0], a[k][1]), cvtpk(a[k][2], a[k][3]), cvtpk(a[k][4], a[k][5]), cvtpk(a[k][6], a[k][7])}, rout, ov, os[k], 0);
    }
    if (fs == 0) bstore8_sc1(tag, vflag, srdws, flo + FL_EPOCH * 128);
    asm volatile(".Lk_end0:");
    __builtin_amdgcn_endpgm();
  }
  {
    using SC = Sched<true>;
    LANE_VALUES
    auto va_of = [&](unsigned word) { return r8 * 3456 + (swz & 1) * 16 + ((word >> (8 * (swz >> 1))) & 255) * 16; };
    constexpr bool attn_role = true;
    auto rd_a = [&](auto Sc, auto U) {
      constexpr int s = Sc, u = U;
      if constexpr (s < SC::NSTEP && !SC::skip(s) && !(u == 1 && s >= 864 && SC::a_ks(s) >= 8)) {
        constexpr int sl = SC::a_stage(s) % 6, ks = SC::a_ks(s);
        (u ? wu : wl)[s % NB] = rdhalf(rb[2 * SC::a_tile(s) + (ks & 1)][sl >> 1] + (sl & 1) * SLOT + (ks >> 1) * 4096 + u * 2048);
      }
    };
    auto mma1 = [&](auto Sc, auto H) {
      constexpr int s = Sc, k = s % 36, o = k & 1, tb = o ^ H;
      if constexpr (!SC::skip(s)) mfma_vv<k < 2>(acc[SC::a_tile(s)][o][tb], cat(wl[s % NB], wu[s % NB]), xf[(k & ~1) + tb]);
    };
    auto sync = [&](auto Sc, auto H) {
      constexpr int s = Sc, h = H;
      if constexpr (h == 0 && SC::need(s) > -25) waitv<SC::allow(s)>();
      if constexpr (h == 1 && (s == BMID || (attn_role && s == 480))) WAITL0();
      if constexpr (h == 1 && SC::bar(s)) BAR();
      if constexpr (h == 1 && SC::lwait(s)) __builtin_amdgcn_s_waitcnt(0xC07F | ((2 * (LA - (s < 468 && !attn_role ? WP1 : WP2))) << 8));
    };
    unsigned vA[3] = {va, va + 8 * 3456 - 1024, va + 2 * (8 * 3456 - 1024)}, gso = soA[1];
    set_m0<4 * SLOT>(m6);
    f32x4 gram;
    float rstdh, negmuh;
    const unsigned hrow = n16 < 8 ? n16 : 0;
    auto halo_val = [&](auto I) {
      f32x4 r;
      sfor<4>([&](auto K) { const float a = aread<A_HH + 4 * (I - 2) + K>(); r[(int)K] = fmaf(ror8(a), negmuh, a) * rstdh; });
      return r;
    };
    auto halo_col = [&](auto I) { return ((qd[I] >> (8 * w)) & 255); };
    LAS char* hb[3];
#pragma unroll
    for (int i = 0; i < 3; ++i) { hb[i] = (LAS char*)(uintptr_t)((unsigned)(uintptr_t)rb[0][i] ^ (w << 5)); PIN(hb[i]); }
    const unsigned ha = (unsigned)(uintptr_t)lds + TAB_OFF + hrow * 128 + G * 8;
    const unsigned qrow = (unsigned)(uintptr_t)lds + QK_OFF + (32 * w + n16 + 8) * QK_RS + G * 8, vrow = (unsigned)(uintptr_t)lds + (32 * w + n16 + 8) * V_RS + G * 16;
    bf16x4 hl[2], hu[2];
    auto opa = [&](auto Tc, auto K) {
      constexpr int T = Tc, d = K / 4, j = K % 4, p = 2 * T + (d >> 2), tb = (d >> 1) & 1, hf = d & 1;
      if constexpr (p != 7) {
        const f32x4& a = acc[T & 1][d >> 2][tb];
        if constexpr (j == 0) t0 = i_mul(a[2 * hf], rstd[tb]);
        if constexpr (j == 1) t1 = i_mul(a[2 * hf + 1], rstd[tb]);
        if constexpr (p < 19) {
          if constexpr (j == 2) t0 = __uint_as_float(i_cvt(t0, t1));
          if constexpr (j == 3) awrite<A_Q + 4 * p + 2 * tb + hf>(__float_as_uint(t0));
        } else if constexpr (j >= 2) awrite<A_V + 8 * (p - 19) + 4 * tb + 2 * hf + j - 2>(__float_as_uint(j == 2 ? t0 : t1));
      }
    };
    sfor<28>([&](auto So) { sfor<18>([&](auto Si) {
      constexpr int s = 18 * So + Si, i = s / 72, u = s % 72;
      sfor<2>([&](auto H) {
        constexpr int h = H;
        mma1(IC(s), H);
        SB(); rd_a(IC(s + LA), H); SB();
        sync(IC(s), H);
        if constexpr (SC::g_set(s)) {
          constexpr int np = SC::g_stage(s + 3), p = SC::g_piece(s + 3);
          if constexpr (h == 0) set_m0<(np % 6) * SLOT + p * 1024>(m6); else gso = sadd<8 * p * 3456>(soA[np % 3]);
        }
        if constexpr (h == 0 && SC::g_stage(s) >= 0) glds3<SC::g_piece(s) % 3>(vA[SC::g_piece(s) % 3], srdA, gso);
        if constexpr (SC::nop(s) > 0) { constexpr int k0 = SC::epi_from(s, h), k1 = SC::epi_to(s, h); sfor<k1 - k0>([&](auto I) { opa(IC(s / 36 - 1), IC(k0 + I)); }); }
        SB();
      });
      if constexpr (u % 4 == 2) {
        constexpr int sg = u / 4;
        if constexpr (s >= 46 && s <= 114) mfma16_aa<s == 46, A_HX + s - 46>(gram);
        if constexpr (i >= 2) mfma16_ava<sg == 0, A_HH + 4 * (i - 2), A_HX + 4 * sg>(cat(hl[sg & 1], hu[sg & 1]));
        SB();
        if constexpr (s >= 22 && s < 94) { constexpr int f = (s - 22) / 4; ds_read_a64<A_HX + 4 * f, (f >> 1) * 1024 + (f & 1) * 64>(ha); ds_read_a64<A_HX + 4 * f + 2, (f >> 1) * 1024 + (f & 1) * 64 + 32>(ha); }
        constexpr int s2 = s + 8, sl = (3 * (s2 / 72) + (s2 % 72) / 24) % 6;
        if constexpr (s2 >= 144 && s2 < 504) { LAS char* p = hb[sl >> 1] + (sl & 1) * SLOT + (((s2 % 72) / 4) % 6) * 4096; hl[sg & 1] = rdhalf(p); hu[sg & 1] = rdhalf(p + 2048); }
        if constexpr (s == 122) {
          const float dg = (p4 == 0) ? gram[0] : (p4 == 1) ? gram[1] : (p4 == 2) ? gram[2] : gram[3];
          rstdh = G == q4 ? dg : 0.f; negmuh = G == 2 ? gram[0] : 0.f;
          PIN(rstdh); PIN(negmuh);
        }
        if constexpr (s == 126) { rstdh = xor16(rstdh); negmuh = xor16(negmuh); PIN(rstdh); PIN(negmuh); }
        if constexpr (s == 130) { rstdh = xor32(rstdh); negmuh = xor32(negmuh); PIN(rstdh); PIN(negmuh); }
        if constexpr (s == 134) {
          negmuh *= -1.0f / D_;
          rstdh = __builtin_amdgcn_rsqf(fmaxf(rstdh * (1.0f / D_) - negmuh * negmuh, 0.f) + 1e-5f);
          PIN(rstdh); PIN(negmuh);
        }
      }
      constexpr int k = SC::warm_k(s);
      if constexpr (SC::mark_k(s) >= 0) mark<1, SC::mark_k(s)>();
      if constexpr (k >= 0 && k < 4) {
        const unsigned la = (288 * kg + ((qs[k < 3 ? 2 + k : 5] >> (k == 2 ? 16 : 0)) & 255) * 16) & ~127u, lb = (288 * kg + (k < 3 ? qs[2 + k] >> 24 : qs[6] & 255) * 16) & ~127u;
        warm(((wa >> 1) * 3456 + ((wa & 1) ? lb : la)) | wm, srdA, 0);
      }
      if constexpr (k == 4) warm(((rt * 81 + w * 21 + lane) * 128) | (lane < 21 ? 0 : OOB), srdWo, 144 * kg * 1152);
      if constexpr (s == 2) *(LAS u32x4*)(lds + TAB_OFF + w * 3072 + (r8 < 3 ? r8 : 0) * 1024 + pos * 16) = u32x4{0x3f803f80u, 0x3f803f80u, 0x3f803f80u, 0x3f803f80u};
      if constexpr (u == 47 && i < 5) { vA[0] = va_of(qs[i + 2]); vA[1] = vA[0] + 8 * 3456 - 1024; vA[2] = vA[0] + 2 * (8 * 3456 - 1024); PIN(vA[0]); PIN(vA[1]); PIN(vA[2]); }
      if constexpr (s > 432 && s < 471 && s % 2 == 1 && s != 447) {
        constexpr int p = (s - 433) / 2;
        const unsigned ad = qrow + ((qd[p / 4] >> (8 * (p % 4))) & 255) * 16;
        ds_write_a64<A_Q + 4 * p, 0>(ad); ds_write_a64<A_Q + 4 * p + 2, 16 * QK_RS>(ad);
      }
      if constexpr (s == 471 || s == 473 || s == 477) {
        constexpr int hi = s == 471 ? 2 : s == 473 ? 3 : 4;
        const f32x4 r = halo_val(IC(hi));
        const unsigned row = (hi == 4 && w == 3) ? 0 : hrow;
        *(LAS u32x2*)(lds + QK_OFF + row * QK_RS + halo_col(IC(hi)) * 16 + G * 8) = u32x2{cvtpk(r[0], r[1]), cvtpk(r[2], r[3])};
      }
      SB();
    }); });
    asm volatile("s_nop 7" : "+v"(acc[1][0][0]), "+v"(acc[1][0][1]), "+v"(acc[1][1][0]), "+v"(acc[1][1][1]));
    WAITL0(); BAR();
    {
      const int hd = lane & 7, i0 = 32 * w + 4 * r8;
      LAS char* rp[11];
#pragma unroll
      for (int jj = 0; jj < 11; ++jj) { rp[jj] = lds + QK_OFF + (i0 + 1 + jj) * QK_RS + hd * 36; PIN(rp[jj]); }
      unsigned qv[4][9], kv[11][9];
#pragma unroll
      for (int a = 0; a < 4; ++a)
#pragma unroll
        for (int c = 0; c < 9; ++c) qv[a][c] = *(LAS unsigned*)(rp[7 + a] + c * 4);
#pragma unroll
      for (int jj = 0; jj < 4; ++jj)
#pragma unroll
        for (int c = 0; c < 9; ++c) kv[jj][c] = *(LAS unsigned*)(rp[jj] + 288 + c * 4);
      {
#pragma unroll
        for (int k = 0; k < 4; ++k)
          *(LAS f32x4*)(uintptr_t)(vrow + ((qd[6] >> (16 + 8 * (k >> 1))) & 255) * 32 + (k & 1) * 16 * V_RS) = acc[1][k >> 1][k & 1] * rstd[k & 1];
        sfor<3>([&](auto I) {
          constexpr int hi = I + 4;
          const unsigned row = (hi == 4 && w != 3) ? 0 : hrow, col = (hi == 4 && w != 3) ? 0 : halo_col(IC(hi));
          *(LAS f32x4*)(lds + row * V_RS + col * 32 + G * 16) = halo_val(IC(hi));
        });
      }
      SB();
      float lg[4][8];
      sfor<8>([&](auto U) {
        constexpr int u = U;
        if constexpr (u + 4 < 11) {
#pragma unroll
          for (int c = 0; c < 9; ++c) kv[u + 4][c] = *(LAS unsigned*)(rp[u + 4] + 288 + c * 4);
        }
        if constexpr (u < 7) {
          constexpr int p = 19 + u;
          const unsigned ad = vrow + ((qd[p / 4] >> (8 * (p % 4))) & 255) * 32;
          ds_write_a128<A_V + 8 * u, 0>(ad); ds_write_a128<A_V + 8 * u + 4, 16 * V_RS>(ad);
        }
#pragma unroll
        for (int a = 0; a < 4; ++a) lg[a][u] = 0.f;
#pragma unroll
        for (int c = 0; c < 9; ++c)
#pragma unroll
          for (int a = 0; a < 4; ++a) lg[a][u] = dot2(qv[a][c], kv[a + u][c], lg[a][u]);
        SB();
      });
      WAITL0(); BAR();
      mark<1, 7>();
      LAS char* vp = lds + (i0 + 1) * V_RS + hd * 72;
      f32x2 vv[11][9];
#pragma unroll
      for (int jj = 0; jj < 3; ++jj)
#pragma unroll
        for (int c = 0; c < 9; ++c) vv[jj][c] = *(volatile LAS f32x2*)(vp + jj * V_RS + c * 8);
      if (rt < 2) {
#pragma unroll
        for (int a = 0; a < 4; ++a)
#pragma unroll
          for (int u = 0; u < 7 - a; ++u) lg[a][u] = (i0 + a + u < 7) ? -1e9f : lg[a][u];
      }
      constexpr float CS = 0.23570226039551584f * 1.4426950408889634f;
#pragma unroll
      for (int a = 0; a < 4; ++a) {
        float mx = lg[a][0];
#pragma unroll
        for (int u = 1; u < 8; ++u) mx = fmaxf(mx, lg[a][u]);
        const float nb = -mx * CS;
        float sum = 0.f;
#pragma unroll
        for (int u = 0; u < 8; ++u) { lg[a][u] = __builtin_amdgcn_exp2f(fmaf(lg[a][u], CS, nb)); sum += lg[a][u]; }
        const float inv = __builtin_amdgcn_rcpf(sum);
#pragma unroll
        for (int u = 0; u < 8; ++u) lg[a][u] *= inv;
      }
      SB();
      f32x2 o[4][9];
      sfor<11>([&](auto J) {
        constexpr int jj = J;
        if constexpr (jj + 3 < 11) {
#pragma unroll
          for (int c = 0; c < 9; ++c) vv[jj + 3][c] = *(volatile LAS f32x2*)(vp + (jj + 3) * V_RS + c * 8);
        }
#pragma unroll
        for (int c = 0; c < 9; ++c) {
#pragma unroll
          for (int a = 0; a < 4; ++a) {
            if (jj == a) o[a][c] = vv[jj][c] * lg[a][0];
            else if (jj > a && jj <= a + 7) o[a][c] = __builtin_elementwise_fma(vv[jj][c], f32x2{lg[a][jj - a], lg[a][jj - a]}, o[a][c]);
          }
        }
        if constexpr (jj == 7) mark<1, 8>();
        if constexpr (jj >= 7) {
#pragma unroll
          for (int c = 0; c < 9; ++c) *(LAS unsigned*)(rp[jj] + c * 4) = cvtpk(o[jj - 7][c][0], o[jj - 7][c][1]);
        }
        SB();
      });
    }
    {
      LAS char* rowp = (LAS char*)(uintptr_t)qrow;
#pragma unroll
      for (int f = 0; f < 9; ++f) {
        const u32x2 a = *(LAS u32x2*)(rowp + (f >> 1) * 64 + (f & 1) * 16 * QK_RS), b = *(LAS u32x2*)(rowp + (f < 8 ? (f >> 1) * 64 + (f & 1) * 16 * QK_RS + 32 : 256 + 16 * QK_RS));
        __builtin_amdgcn_raw_buffer_store_b128(u32x4{a[0], a[1], b[0], b[1]}, rws, l16, oso + f * 1024, 16);
      }
    }
    WAITV(0); BAR();
    bstore8_sc1(get_tag(), vflag, srdws, flo + (FL_O + kg) * 128);
  }
  asm volatile(".Lk_end1:");
}

extern "C" void kernel_launch(void* const* d_in, const int* in_sizes, int n_in, void* d_out, int out_size, void* d_ws, size_t ws_size, hipStream_t stream) {
  (void)hipFuncSetAttribute((const void*)main_k, hipFuncAttributeMaxDynamicSharedMemorySize, LDS_BYTES);
  const Args args = {d_in[0], d_in[1], d_in[2], d_in[4], d_in[5], d_in[6], d_in[7], d_out, d_ws, make_tab()};
  main_k<<<256, 256, LDS_BYTES, stream>>>(args);
}
```

```cpp
#define USEMIN 0
#define KS 0
#include <hip/hip_runtime.h>
#include <stdint.h>
#include <utility>

typedef __bf16 bf16x8 __attribute__((ext_vector_type(8)));
typedef __bf16 bf16x4 __attribute__((ext_vector_type(4)));
typedef __bf16 bf16x2 __attribute__((ext_vector_type(2)));
typedef float f32x4 __attribute__((ext_vector_type(4)));
typedef float f32x2 __attribute__((ext_vector_type(2)));
typedef unsigned u32x4 __attribute__((ext_vector_type(4)));
typedef unsigned u32x2 __attribute__((ext_vector_type(2)));
typedef unsigned u32x16 __attribute__((ext_vector_type(16)));
typedef unsigned long long u64;
#define LAS __attribute__((address_space(3)))
#define GAS __attribute__((address_space(1)))
typedef GAS u64 gu64;
#define DI __device__ __forceinline__
#define SB() __builtin_amdgcn_sched_barrier(0)
#define IC(n) std::integral_constant<int, (n)>{}
#define RLX_AGENT __ATOMIC_RELAXED, __HIP_MEMORY_SCOPE_AGENT

template <class F, int... I> DI void sfor_(F&& f, std::integer_sequence<int, I...>) { (f(std::integral_constant<int, I>{}), ...); }
template <int N, class F> DI void sfor(F&& f) { sfor_(f, std::make_integer_sequence<int, N>{}); }

constexpr int S_ = 2048, D_ = 576, F_ = 4608;
constexpr int SLOT = 24576;
constexpr int TAB_OFF = 147456;
constexpr int LDS_BYTES = 163840;
constexpr int QK_OFF = 80512, QK_RS = 584, V_RS = 592;
constexpr unsigned H_OFF = 0, O_OFF = 20u << 20, P_OFF = 24u << 20, FLAG_OFF = 40u << 20, WS_BYTES = 41u << 20;
constexpr unsigned OOB = 0x40000000u;
constexpr int FL_HA = 0, FL_HB = 12, FL_O = 24, FL_FIN = 28, FL_EPOCH = 40, FL_N = 41;
constexpr int A_H = 56;
constexpr int A_Q = 16;
constexpr int A_V = 92;
constexpr int A_HH = 164;
constexpr int A_HX = 184;

DI int map_role(int bid) { return (bid >> 3) & 3; }
DI int map_kg(int bid) { return bid & 3; }
DI int map_rt(int bid) { return ((bid >> 2) & 1) * 8 + (bid >> 5); }

DI u32x4 make_srd(const void* p, unsigned bytes) {
  uint64_t a = (uint64_t)p;
  u32x4 r; r[0] = (unsigned)a; r[1] = (unsigned)(a >> 32) & 0xffffu; r[2] = bytes; r[3] = 0x00020000u;
  return r;
}
template <unsigned MC, unsigned SC> DI void glds16c(unsigned mb, unsigned voff, u32x4 srd, unsigned sb) {
  unsigned tmp;
  asm volatile("s_add_u32 m0, %1, %5\n\ts_add_u32 %0, %4, %6\n\tbuffer_load_dwordx4 %2, %3, %0 offen lds"
               : "=&s"(tmp) : "s"(mb), "v"(voff), "s"(srd), "s"(sb), "i"(MC), "i"(SC) : "memory", "scc");
}
template <unsigned C> DI void set_m0(unsigned b) { asm volatile("s_add_u32 m0, %0, %1" :: "s"(b), "i"(C) : "scc"); }
template <unsigned C> DI unsigned sadd(unsigned b) { unsigned r; asm volatile("s_add_u32 %0, %1, %2" : "=s"(r) : "s"(b), "i"(C) : "scc"); return r; }
template <int J> DI void glds3(unsigned v, u32x4 srd, unsigned so) { asm volatile("buffer_load_dwordx4 %0, %1, %2 offen offset:%c3 lds" :: "v"(v), "s"(srd), "s"(so), "i"(J * 1024) : "memory"); }
template <unsigned MC> DI void glds16r(unsigned mb, unsigned voff, u32x4 srd, unsigned& so, unsigned step) {
  asm volatile("s_add_u32 m0, %1, %5\n\ts_add_u32 %0, %0, %4\n\tbuffer_load_dwordx4 %2, %3, %0 offen lds"
               : "+s"(so) : "s"(mb), "v"(voff), "s"(srd), "s"(step), "i"(MC) : "memory", "scc");
}
constexpr int x_base(int t) { return (t / 3 == 1 ? 2 : 4) * SLOT + (t % 3 == 2 ? SLOT : 0); }
template <int T> DI void x_line(unsigned m6, unsigned vx0, unsigned vx1, u32x4 srd, unsigned s0, unsigned s1, unsigned s2, unsigned s3) {
  constexpr int d0 = x_base(T) + (T % 3 == 1 ? 4096 : 0) - (T % 3) * 128, d2 = T % 3 == 1 ? SLOT - 1024 : 1024;
  asm volatile("s_add_u32 m0, %0, %8\n\ts_nop 0\n\tbuffer_load_dwordx4 %1, %3, %4 offen offset:%c10 lds\n\t"
               "s_addk_i32 m0, 0x400\n\ts_nop 0\n\tbuffer_load_dwordx4 %2, %3, %5 offen offset:%c10 lds\n\t"
               "s_addk_i32 m0, %c9\n\ts_nop 0\n\tbuffer_load_dwordx4 %1, %3, %6 offen offset:%c10 lds\n\t"
               "s_addk_i32 m0, 0x400\n\ts_nop 0\n\tbuffer_load_dwordx4 %2, %3, %7 offen offset:%c10 lds"
               :: "s"(m6), "v"(vx0), "v"(vx1), "s"(srd), "s"(s0), "s"(s1), "s"(s2), "s"(s3), "i"(d0), "i"(d2), "i"((T % 3) * 128) : "memory", "scc");
}
DI u32x4 bload16(unsigned voff, u32x4 srd, unsigned soff) {
  u32x4 r;
  asm volatile("buffer_load_dwordx4 %0, %1, %2, %3 offen" : "=v"(r) : "v"(voff), "s"(srd), "s"(soff) : "memory");
  return r;
}
DI void warm(unsigned voff, u32x4 srd, unsigned soff) {
  asm volatile("buffer_load_dword a11, %0, %1, %2 offen" :: "v"(voff), "s"(srd), "s"(soff) : "memory");
}
DI u64 bload8_sc1(unsigned voff, u32x4 srd, unsigned soff) {
  u64 r;
  asm volatile("buffer_load_dwordx2 %0, %1, %2, %3 offen sc1" : "=v"(r) : "v"(voff), "s"(srd), "s"(soff) : "memory");
  return r;
}
template <int AR, int OFF> DI void aload16_sc1(unsigned voff, u32x4 srd, unsigned soff) {
  asm volatile("buffer_load_dwordx4 a[%c3:%c4], %0, %1, %2 offen offset:%c5 sc1" :: "v"(voff), "s"(srd), "s"(soff), "i"(AR), "i"(AR + 3), "i"(OFF) : "memory");
}
template <int ROLE, int K> DI void mark() { asm volatile("s_branch 1f\n\t.p2align 12\n.Lm%c0_%c1:\n\ts_setpc_b64 vcc\n1:" :: "i"(ROLE), "i"(K)); }
template <int R, int N0, int N1> DI void visit(unsigned sel) {
  constexpr auto c = [](int k, int n) { return k < n ? k : n - 1; };
  asm volatile("s_getpc_b64 vcc\n\ts_add_u32 s90, vcc_lo, %0\n\ts_addc_u32 s91, vcc_hi, 0\n\ts_add_u32 vcc_lo, vcc_lo, 52\n\ts_addc_u32 vcc_hi, vcc_hi, 0\n\ts_setpc_b64 s[90:91]\n\t"
               "s_branch .Lm0_%c1\n\ts_branch .Lm0_%c2\n\ts_branch .Lm0_%c3\n\ts_branch .Lm0_%c4\n\ts_branch .Lm1_%c5\n\ts_branch .Lm1_%c6\n\ts_branch .Lm1_%c7\n\ts_branch .Lm1_%c8"
               :: "s"(sel + 20), "i"(c(4 * R, N0)), "i"(c(4 * R + 1, N0)), "i"(c(4 * R + 2, N0)), "i"(c(4 * R + 3, N0)),
                  "i"(c(4 * R, N1)), "i"(c(4 * R + 1, N1)), "i"(c(4 * R + 2, N1)), "i"(c(4 * R + 3, N1)) : "vcc", "s90", "s91", "scc");
}
template <int N> DI void waitv() { asm volatile("s_waitcnt vmcnt(%c0)" :: "i"(N > 63 ? 63 : N) : "memory"); }
#define WAITV(n) asm volatile("s_waitcnt vmcnt(" #n ")" ::: "memory")
#define BAR() asm volatile("s_barrier" ::: "memory")
#define WAITL0() asm volatile("s_waitcnt lgkmcnt(0)" ::: "memory")
#define PIN(x) asm volatile("" : "+v"(x))
#define PINS(x) asm volatile("" : "+s"(x))

DI unsigned cvtpk(float lo, float hi) { f32x2 v = {lo, hi}; bf16x2 b = __builtin_convertvector(v, bf16x2); return __builtin_bit_cast(unsigned, b); }
DI float bflo(unsigned u) { return __uint_as_float(u << 16); }
DI float bfhi(unsigned u) { return __uint_as_float(u & 0xffff0000u); }
DI float dot2(unsigned a, unsigned b, float c) { return __builtin_amdgcn_fdot2_f32_bf16(__builtin_bit_cast(bf16x2, a), __builtin_bit_cast(bf16x2, b), c, false); }
DI float xor32(float v) { auto r = __builtin_amdgcn_permlane32_swap(__float_as_uint(v), __float_as_uint(v), false, false); return __uint_as_float(r[0]) + __uint_as_float(r[1]); }
DI float xor16(float v) { auto r = __builtin_amdgcn_permlane16_swap(__float_as_uint(v), __float_as_uint(v), false, false); return __uint_as_float(r[0]) + __uint_as_float(r[1]); }

DI bf16x4 rdhalf(LAS char* p) { return __builtin_amdgcn_ds_read_tr16_b64_v4bf16((LAS bf16x4*)p); }
DI bf16x8 cat(bf16x4 a, bf16x4 b) { return __builtin_shufflevector(a, b, 0, 1, 2, 3, 4, 5, 6, 7); }
template <bool FIRST> DI void mfma_vv(f32x4& acc, bf16x8 a, u32x4 b) {
  if constexpr (FIRST) asm volatile("v_mfma_f32_16x16x32_bf16 %0, %1, %2, 0" : "=&v"(acc) : "v"(a), "v"(b));
  else                 asm volatile("v_mfma_f32_16x16x32_bf16 %0, %1, %2, %0" : "+v"(acc) : "v"(a), "v"(b));
}
DI void mfma_vvc(f32x4& acc, u32x4 a, u32x4 b, const f32x4& c) { asm volatile("v_mfma_f32_16x16x32_bf16 %0, %1, %2, %3" : "=&v"(acc) : "v"(a), "v"(b), "v"(c)); }
template <bool FIRST, int AR> DI void mfma_va(f32x4& acc, bf16x8 a) {
  if constexpr (FIRST) asm volatile("v_mfma_f32_16x16x32_bf16 %0, %1, a[%c2:%c3], 0" : "=&v"(acc) : "v"(a), "i"(AR), "i"(AR + 3));
  else                 asm volatile("v_mfma_f32_16x16x32_bf16 %0, %1, a[%c2:%c3], %0" : "+v"(acc) : "v"(a), "i"(AR), "i"(AR + 3));
}
template <int AR> DI void mfma_k16(f32x4& acc, bf16x4 a) { asm volatile("v_mfma_f32_16x16x16_bf16 %0, %1, a[%c2:%c3], %0" : "+v"(acc) : "v"(a), "i"(AR), "i"(AR + 1)); }
template <bool FIRST, int AR> DI void mfma16_aa(f32x4& acc) {
  if constexpr (FIRST) asm volatile("v_mfma_f32_16x16x32_bf16 %0, a[%c1:%c2], a[%c1:%c2], 0" : "=&v"(acc) : "i"(AR), "i"(AR + 3));
  else                 asm volatile("v_mfma_f32_16x16x32_bf16 %0, a[%c1:%c2], a[%c1:%c2], %0" : "+v"(acc) : "i"(AR), "i"(AR + 3));
}
template <bool FIRST, int AD, int AR> DI void mfma16_ava(bf16x8 a) {
  if constexpr (FIRST) asm volatile("v_mfma_f32_16x16x32_bf16 a[%c1:%c2], %0, a[%c3:%c4], 0" :: "v"(a), "i"(AD), "i"(AD + 3), "i"(AR), "i"(AR + 3));
  else                 asm volatile("v_mfma_f32_16x16x32_bf16 a[%c1:%c2], %0, a[%c3:%c4], a[%c1:%c2]" :: "v"(a), "i"(AD), "i"(AD + 3), "i"(AR), "i"(AR + 3));
}
template <int AR> DI float aread(void) { float r; asm volatile("v_accvgpr_read_b32 %0, a[%c1]" : "=v"(r) : "i"(AR)); return r; }
template <int AR, int OFF> DI void ds_write_a128(unsigned addr) { asm volatile("ds_write_b128 %0, a[%c1:%c2] offset:%c3" :: "v"(addr), "i"(AR), "i"(AR + 3), "i"(OFF) : "memory"); }
DI float ror8(float v) { return __int_as_float(__builtin_amdgcn_update_dpp(0, __float_as_int(v), 0x128, 0xf, 0xf, false)); }
template <int AR> DI void awrite(unsigned v) { asm volatile("v_accvgpr_write_b32 a[%c1], %0" :: "v"(v), "i"(AR)); }
DI float i_fma(float a, float b, float c) { float r; asm volatile("v_fma_f32 %0, %1, %2, %3" : "=v"(r) : "v"(a), "v"(b), "v"(c)); return r; }
DI float i_mul(float a, float b) { float r; asm volatile("v_mul_f32_e32 %0, %1, %2" : "=v"(r) : "v"(a), "v"(b)); return r; }
DI float i_sqs(float a) { float r; asm volatile("v_mul_f32_e64 %0, %1, |%1|" : "=v"(r) : "v"(a)); return r; }
DI unsigned i_cvt(float a, float b) { unsigned r; asm volatile("v_cvt_pk_bf16_f32 %0, %1, %2" : "=v"(r) : "v"(a), "v"(b)); return r; }
DI unsigned i_relu(unsigned a) { unsigned r; asm volatile("v_pk_max_i16 %0, %1, 0" : "=v"(r) : "v"(a)); return r; }
DI void i_swap16(unsigned& a, unsigned& b) { asm volatile("v_permlane16_swap_b32 %0, %1" : "+v"(a), "+v"(b)); }
template <int OFF> DI void st16_sc1(u32x4 v, unsigned voff, u32x4 srd, unsigned soff) {
  asm volatile("buffer_store_dwordx4 %0, %1, %2, %3 offen offset:%c4 sc1" :: "v"(v), "v"(voff), "s"(srd), "s"(soff), "i"(OFF) : "memory");
}
template <int AR, int OFF> DI void ds_read_a64(unsigned addr) { asm volatile("ds_read_b64 a[%c1:%c2], %0 offset:%c3" :: "v"(addr), "i"(AR), "i"(AR + 1), "i"(OFF) : "memory"); }
DI void bstore8_sc1(u64 v, unsigned voff, u32x4 srd, unsigned soff) { asm volatile("buffer_store_dwordx2 %0, %1, %2, %3 offen sc1" :: "v"(v), "v"(voff), "s"(srd), "s"(soff) : "memory"); }
template <int AR, int OFF> DI void ds_write_a64(unsigned addr) { asm volatile("ds_write_b64 %0, a[%c1:%c2] offset:%c3" :: "v"(addr), "i"(AR), "i"(AR + 1), "i"(OFF) : "memory"); }

constexpr int LA = 7;
constexpr int WP1 = 4, WP2 = 2;
constexpr int NB = LA + 1, BMID = (23 - LA) & ~1;
template <bool ATTN> struct Sched {
  static constexpr int P1 = ATTN ? 504 : 432, NSTEP = ATTN ? 504 : 924, NSTAGE = ATTN ? 21 : 39;
  static constexpr int a_stage(int s) { return s < P1 ? 3 * (s / 72) + (s % 36) / 12 : s < 864 ? s / 24 : 36 + (s - 864) / 20; }
  static constexpr int a_tile(int s) { return s < P1 ? (s % 72) / 36 : s < 864 ? (s % 24) / 12 : ((s - 864) % 20) / 10; }
  static constexpr int a_ks(int s) { return s < 864 ? s % 12 : (s - 864) % 10; }
  static constexpr bool skip(int s) { return ATTN && s >= 108 && s < 144 && s % 2 == 1; }
  static constexpr int g_stage(int s) {
    if (s < 0) return (s + 24) / 6;
    if (s >= 864 || s % 4 != 1) return -1;
    const int st = s / 24 + 4;
    return st >= NSTAGE || (st >= 36 && (s % 24) / 4 == 5) ? -1 : st;
  }
  static constexpr int g_piece(int s) { return (s % 24) / 4; }
  static constexpr int g_last(int st) { int r = -25; for (int s = -24; s < NSTEP; ++s) if (g_stage(s) == st) r = s; return r; }
  static constexpr int warm_k(int s) { return s >= 0 && s % 48 == 0 && s / 48 < 5 ? s / 48 : -1; }
  static constexpr int PUB_HA = 288 + BMID, PUB_HB = 504, POLL_HA = 388, CHK_HA = 432, POLL_HB = 580, CHK_HB = 624, POLL_O = 792, CHK_O = 828;
  static constexpr int h_load(int s) {
    if (ATTN || s % 4 != 3) return -1;
    return s >= 432 && s < 480 ? (s - 432) / 4 : s >= 504 && s < 600 ? (s % 8 == 3 ? 12 + (s - 504) / 8 : -1) : s >= 624 && s < 720 ? 24 + (s - 624) / 4 : -1;
  }
  static constexpr int o_load(int s) { return !ATTN && s > CHK_O && s < CHK_O + 19 && s % 4 > 1 ? (s - CHK_O) / 4 * 2 + s % 4 - 2 : -1; }
  static constexpr int x_load(int s) { return !ATTN && s >= 723 && s < 771 && s % 16 == 3 ? (s - 723) / 16 : -1; }
  static constexpr int NMARK = ATTN ? 9 : 11;
  static constexpr int mark_k(int s) {
    constexpr int M[] = {2, 84, 162, 240, 318, 396, 474, 574, 674, 774, 876}, A[] = {16, 98, 190, 268, 348, 430, 492};
    if (ATTN) { for (int k = 0; k < 7; ++k) if (s == A[k]) return k; }
    else { for (int k = 0; k < 11; ++k) if (s == M[k]) return k; }
    return -1;
  }
  static constexpr bool bar(int s) { return s < P1 ? (s % 72 == 0 || s % 72 == BMID || s % 72 == 48) : (s <= 864 && s % 24 == 0); }
  static constexpr bool lwait(int s) { return (s + 1) % (s < 468 && !ATTN ? WP1 : WP2) == 0; }
  static constexpr int bias_rd(int s) { return !ATTN && s < 432 ? (s % 36 == 28 ? 0 : s % 36 == 35 ? 1 : -1) : -1; }
  static constexpr bool g_set(int s) { return g_stage(s + 3) >= 0 && g_piece(s + 3) % 3 == 0; }
  static constexpr bool fixed(int s, int h) {
    if (bar(s) || g_set(s)) return true;
    return h == 0 ? (g_stage(s) >= 0 || h_load(s) >= 0 || o_load(s) >= 0 || x_load(s) >= 0 || bias_rd(s) >= 0) : lwait(s);
  }
  static constexpr int ST0 = 30, ST1 = 57;
  static constexpr int win0(int s) { return s < 864 ? s / 36 * 36 : 864 + (s - 864) / 10 * 10; }
  static constexpr int wlen(int s) { return s < 864 ? 36 : 10; }
  static constexpr int nop(int s) { return ATTN ? (s >= 36 ? 32 : 0) : s >= 36 && s < 468 ? 59 : s >= 874 ? 14 : 0; }
  static constexpr int epi_to(int s, int h) {
    int f = 0, u = 0;
    for (int q = 2 * win0(s) + 2; q < 2 * (win0(s) + wlen(s)); ++q) if (!fixed(q / 2, q % 2)) { ++f; if (q <= 2 * s + h) ++u; }
    return f >= nop(s) ? (u < nop(s) ? u : nop(s)) : u * nop(s) / f;
  }
  static constexpr int epi_from(int s, int h) { return 2 * s + h <= 2 * win0(s) + 2 ? 0 : h ? epi_to(s, 0) : epi_to(s - 1, 1); }
  static constexpr bool has_op(int s, int k) { return epi_from(s, 0) <= k && k < epi_to(s, 1); }
  static constexpr int stores(int s) { return ATTN || s < 0 || !nop(s) ? 0 : s < 864 ? has_op(s, ST0) + has_op(s, ST1) : has_op(s, 10) + has_op(s, 13); }
  static constexpr int vm(int s) {
    int c = (g_stage(s) >= 0) + (warm_k(s) >= 0) + stores(s);
    if (!ATTN && s >= 0) c += (h_load(s) >= 0) + (o_load(s) >= 0) + (s == POLL_HA) + (s == POLL_HB) + (s == POLL_O) + (x_load(s) >= 0);
    return c;
  }
  static constexpr int need(int s) {
    int r = -25;
    if (bar(s) && !(ATTN && s == 480)) r = g_last(s < P1 ? 3 * (s / 72) + (s % 72 == 0 ? 1 : s % 72 == BMID ? 2 : 3) : s < 864 ? s / 24 + 1 : 38);
    if (!ATTN) {
      const int e = s == PUB_HA || s == PUB_HB ? s - 1 : s == CHK_HA ? POLL_HA : s == CHK_HB ? POLL_HB : s == 646 ? 595 : s == CHK_O ? POLL_O : s == 720 ? 671 : s == 768 ? 719 : s == 863 ? 862 : -25;
      if (e > r) r = e;
    }
    return r;
  }
  static constexpr int allow(int s) { int c = 0; for (int k = need(s) + 1; k < s; ++k) c += vm(k); return c; }
  static constexpr bool ring_ok(int st) {
    int first = NSTEP, last = -1, refill = NSTEP;
    for (int s = 0; s < NSTEP; ++s) {
      if (a_stage(s) == st) { if (s - LA < first) first = s - LA; last = s; }
      if (ATTN && s >= 144 && s % 4 == 2 && 3 * (s / 72) + (s % 72) / 24 == st) { if (s - 8 < first) first = s - 8; if (s + 1 > last) last = s + 1; }
      if (g_stage(s) == st + 6 && s < refill) refill = s;
    }
    bool vis = st == 0, fre = refill == NSTEP;
    for (int b = 0; b < NSTEP; b += 2) if (bar(b)) { if (b < first && b + 72 > first && need(b) >= g_last(st)) vis = true; if (b >= last && b < refill) fre = true; }
    return vis && fre;
  }
};
template <bool ATTN, int... I> constexpr bool rings_ok(std::integer_sequence<int, I...>) { return (std::bool_constant<Sched<ATTN>::ring_ok(I)>::value && ...); }
static_assert(rings_ok<false>(std::make_integer_sequence<int, 39>{}) && rings_ok<true>(std::make_integer_sequence<int, 21>{}), "LDS ring protocol");
constexpr int kb_areg(int kb) { return kb == 0 ? A_H : kb == 1 ? A_H + 48 : kb == 2 ? A_H + 96 : kb == 3 ? A_H + 144 : kb == 4 ? A_H : kb == 5 ? A_H + 48 : A_H + 96; }
constexpr int qkv_pair(int kg, int p) {
  constexpr signed char T[4][28] = {
    {0,2,4,6, 16,8,10,-1, 18,20,22,24, 26,28,30,32, 12,14,34,52, 36,38,40,42, 44,46,48,50},
    {6,8,10,12, 0,2,4,-1, 24,26,28,30, 14,16,32,34, 18,20,22,50, 42,44,46,48, 36,38,40,52},
    {4,6,8,10, 12,14,16,-1, 22,24,26,28, 0,2,18,20, 30,32,34,36, 40,42,44,46, 48,50,52,38},
    {2,4,6,8, 0,10,12,-1, 20,22,24,26, 28,30,32,34, 14,16,18,36, 38,40,42,44, 46,48,50,52}};
  return T[kg][p];
}
struct QkvTab { unsigned v[4][16]; };
constexpr QkvTab make_tab() {
  QkvTab t = {};
  for (int kg = 0; kg < 4; ++kg)
    for (int p = 0; p < 28; ++p) {
      const int v = qkv_pair(kg, p), sv = v < 0 ? qkv_pair(kg, p - 1) : v;
      t.v[kg][p / 4] |= (unsigned)((sv / 18) * 72 + sv % 18) << (8 * (p % 4));
      t.v[kg][8 + p / 4] |= (unsigned)(sv >= 36 ? sv - 36 : sv) << (8 * (p % 4));
    }
  return t;
}

DI void check_flag(u64 f, u64 tag, unsigned voff, u32x4 srd, unsigned soff) {
  unsigned n;
  asm volatile("v_cmp_ne_u64_e32 vcc, %1, %2\n\ts_cbranch_vccz 1f\n\ts_mov_b32 %0, 0x40000\n2:\n\ts_sleep 1\n\t"
               "buffer_load_dwordx2 %1, %3, %4, %5 offen sc1\n\ts_sub_u32 %0, %0, 1\n\ts_waitcnt vmcnt(0)\n\ts_cbranch_scc1 1f\n\t"
               "v_cmp_ne_u64_e32 vcc, %1, %2\n\ts_cbranch_vccnz 2b\n1:"
               : "=&s"(n), "+v"(f) : "v"(tag), "v"(voff), "s"(srd), "s"(soff) : "vcc", "scc", "memory");
}
DI void spin_until(gu64* p, u64 tag) {
#pragma unroll 1
  for (unsigned spins = 0; spins < (1u << 18); ++spins) {
    const u64 f = __hip_atomic_load(p, RLX_AGENT);
    if (__all(f == tag)) break;
  }
}

struct Args { const void *x, *wqkv, *wo, *w1, *b1, *w2, *b2; void *out, *ws; QkvTab qt; };
static_assert(sizeof(Args) == 72 + 256, "no padding");
DI char* as_ptr(unsigned lo, unsigned hi) { return (char*)(((u64)hi << 32) | lo); }

__global__ __launch_bounds__(256, 1) void main_k(const Args) {
  extern __shared__ __attribute__((aligned(16))) char smem[];
  LAS char* lds = (LAS char*)smem;
  const int tid = threadIdx.x, lane = tid & 63, bid = blockIdx.x;
  const int kg = map_kg(bid), cg = map_role(bid), rt = map_rt(bid);
  u64 tent; asm volatile("s_memrealtime %0" : "=s"(tent));
  u32x16 ka, kt; u32x2 kw;
  asm volatile("s_load_dwordx16 %0, %3, 0x0\n\ts_load_dwordx2 %1, %3, 0x40\n\ts_load_dwordx16 %2, %3, %4 offset:0x48"
               : "=&s"(ka), "=&s"(kw), "=&s"(kt) : "s"(__builtin_amdgcn_kernarg_segment_ptr()), "s"(kg * 64));
  const void* code;
  unsigned code_bytes;
  asm volatile("s_getpc_b64 %0\n.Lk_pc:\n\ts_mov_b32 %1, .Lk_end0-.Lk_pc\n\ts_max_i32 %1, %1, .Lk_end1-.Lk_pc" : "=s"(code), "=s"(code_bytes) :: "scc", "a255");
  const int w = __builtin_amdgcn_readfirstlane(tid >> 6);
  const bool attn = cg == 3;
  const int tokbase = 256 * (rt >> 1) + (rt & 1);
  const int n16 = lane & 15, r8 = lane >> 3, pos = lane & 7;
  const int G = lane >> 4, q4 = (lane >> 2) & 3, p4 = lane & 3;

  const unsigned rot = (0x24924924u >> (2 * ((rt & 7) + cg))) & 3;
  unsigned kro[3] = {rot * 192, rot == 2 ? 0 : rot * 192 + 192, rot == 0 ? 384 : rot * 192 - 192};
  unsigned m6 = 6 * w * 1024, sx = tokbase * 1152 + w * 32 * 2304;
  unsigned vx0 = r8 * 2304 + ((pos ^ G) << 4), vx1 = vx0 ^ 64;
  PINS(m6); PINS(sx); PIN(vx0); PIN(vx1); PINS(kro[0]); PINS(kro[1]); PINS(kro[2]);
  const unsigned swz = pos ^ (((r8 >> 1) & 3) << 1);
  const int fs = 3 * kg + cg, f0 = fs * 384;
  unsigned strideA = attn ? 3456u : 9216u;
  PINS(strideA);
  unsigned soA[3];
#pragma unroll
  for (int j = 0; j < 3; ++j) { soA[j] = (kro[j] + 48 * w) * strideA + (attn ? 288 * kg : f0 * 2); PINS(soA[j]); }
  unsigned vab = r8 * strideA + (swz & 1) * 16, vsh = 8 * (swz >> 1), vcode = tid < 64 ? tid * 128 : OOB;
  PIN(vab); PIN(vsh); PIN(vcode);
  u32x4 id0, id1;
  {
    const unsigned one = 0x3f80u << (16 * (n16 & 1));
    const int e = (n16 >> 1) - 4 * G;
#pragma unroll
    for (int j = 0; j < 4; ++j) { id0[j] = e == j ? one : 0u; id1[j] = e + 8 == j ? one : 0u; }
  }
  PIN(id0); PIN(id1);
  unsigned po0 = (4 * G + q4) * 128 + (((2 * G + (q4 >> 1)) & 3) << 5) + p4 * 8;
  LAS char* rb[4][3];
#pragma unroll
  for (int i = 0; i < 3; ++i)
#pragma unroll
    for (int c = 0; c < 4; ++c) { rb[c][i] = lds + (po0 ^ (c << 5)) + 2 * i * SLOT; PIN(rb[c][i]); }
  LAS char* xp[2][2];
#pragma unroll
  for (int u = 0; u < 2; ++u) {
    const unsigned xr0 = w * 6144 + (n16 >> 3) * 1024 + (n16 & 7) * 128 + (((G ^ (n16 >> 1)) << 4) ^ (u << 6));
    xp[0][u] = lds + xr0 + 4 * SLOT; xp[1][u] = lds + xr0 + 2 * SLOT; PIN(xp[0][u]); PIN(xp[1][u]);
  }
  unsigned ones = 0x3f803f80u; PIN(ones);
  const int c1 = cg == 2 ? 0 : cg + 1, c2 = cg == 0 ? 2 : cg - 1;
  unsigned soB[3];
  soB[0] = (f0 + 48 * w) * 1152 + 384 * cg; soB[1] = ((3 * kg + c1) * 384 + 48 * w) * 1152 + 384 * cg; soB[2] = ((3 * kg + c2) * 384 + 48 * w) * 1152 + 384 * cg;
  const int p5 = w == 3 ? 13 : 5 * w;
  unsigned soO = (144 * kg + 8 * p5) * 1152 + 384 * cg, m5 = p5 * 1024;
  unsigned hso[3];
  hso[0] = H_OFF + ((rt * 12 + fs) * 4 + w) * 24576; hso[1] = H_OFF + ((rt * 12 + 3 * kg + c1) * 4 + w) * 24576; hso[2] = H_OFF + ((rt * 12 + 3 * kg + c2) * 4 + w) * 24576;
  unsigned oso = O_OFF + ((rt * 4 + kg) * 4 + w) * 9216;
  unsigned pso = P_OFF + (((rt * 3 + cg) * 4 + kg) * 4 + w) * 12288;
  unsigned os[3];
#pragma unroll
  for (int k = 0; k < 3; ++k) { const int un = 3 * kg + k; os[k] = (tokbase + 64 * w + 32 * (un & 1)) * 1152 + (192 * cg + 32 * (un >> 1)) * 2; }
#pragma unroll
  for (int k = 0; k < 3; ++k) { PINS(soB[k]); PINS(hso[k]); PINS(os[k]); }
  PINS(pso); PINS(oso); PINS(soO); PINS(m5);
  asm volatile("s_waitcnt lgkmcnt(0)" : "+s"(ka), "+s"(kw), "+s"(kt), "+s"(tent));
  const char *x = as_ptr(ka[0], ka[1]), *wo = as_ptr(ka[4], ka[5]), *b1 = as_ptr(ka[8], ka[9]), *w2 = as_ptr(ka[10], ka[11]), *b2 = as_ptr(ka[12], ka[13]);
  char *out = as_ptr(ka[14], ka[15]), *ws = as_ptr(kw[0], kw[1]);
  const u32x4 srdx = make_srd(x, S_ * D_ * 2);
  auto x_more = [&](auto T) { const unsigned b = sx + kro[T / 3] * 2; x_line<T>(m6, vx0, vx1, srdx, b, b + 8 * 2304, b + 16 * 2304, b + 24 * 2304); };
  sfor<3>(x_more);
  const unsigned vsel = 4 * w + (attn ? 16 : 0);
  visit<0, Sched<false>::NMARK, Sched<true>::NMARK>(vsel);
  u32x4 srdA = make_srd(attn ? as_ptr(ka[2], ka[3]) : as_ptr(ka[6], ka[7]), attn ? D_ * 3 * D_ * 2 : D_ * F_ * 2);
  PINS(srdA);
  unsigned qs[7], qd[7];
#pragma unroll
  for (int i = 0; i < 7; ++i) { qs[i] = kt[i]; qd[i] = kt[8 + i]; }
  auto va_of = [&](unsigned word) { return vab + ((word >> vsh) & 255) * 16; };
  unsigned va = va_of(attn ? qs[0] : 0x06040200u), so = soA[0] - 8 * strideA;
  auto w_piece = [&](auto K) {
    constexpr int n = K / 6, p = K % 6;
    if constexpr (p == 0 && n > 0) so = soA[n % 3] - 8 * strideA;
    if constexpr (K == 18) va = va_of(attn ? qs[1] : 0x0e0c0a08u);
    glds16r<n * SLOT + p * 1024>(m6, va, srdA, so, 8 * strideA);
  };
  sfor<6>(w_piece);
  warm(vcode, make_srd(code, code_bytes), ((bid >> 3) & 15) * 8192);
  visit<1, Sched<false>::NMARK, Sched<true>::NMARK>(vsel);
  const u32x4 srdws = make_srd(ws, WS_BYTES);
  const unsigned flo = FLAG_OFF + rt * FL_N * 128;
  auto misc_loads = [&]() {
    asm volatile("buffer_store_dword %0, %1, %2, %3 offen sc1" :: "v"((unsigned)tent), "v"(0), "s"(srdws), "s"((39u << 20) + bid * 4) : "memory");
    asm volatile("buffer_load_dwordx2 a[12:13], %0, %1, %2 offen sc1" :: "v"(0), "s"(srdws), "s"(flo + FL_EPOCH * 128) : "memory");
    if (attn) {
      int ht = tokbase + 2 * (r8 - 8); ht = ht < 0 ? 0 : ht;
      const unsigned vh = ht * 1152 + pos * 16;
      const unsigned kb = ((0x24924924u >> (2 * (rot + w))) & 3) * 384 | (w == 3 ? OOB : 0);
      glds16c<TAB_OFF, 0>(w * 3072, vh, srdx, kb); glds16c<TAB_OFF + 1024, 128>(w * 3072, vh, srdx, kb); glds16c<TAB_OFF + 2048, 256>(w * 3072, vh, srdx, kb);
    } else {
      asm volatile("buffer_load_dword a14, %0, %1, 0 offen" :: "v"(tid * 4), "s"(make_srd(b1 + f0 * 2, 768)) : "memory");
      asm volatile("buffer_load_dword a15, %0, %1, 0 offen" :: "v"(tid * 4), "s"(make_srd(b2 + 192 * cg * 2, 384)) : "memory");
    }
  };
  u32x4 xf[36];
  f32x4 nm[2];
  float rstd[2];
  {
    float sm[2] = {0.f, 0.f}, sq[2] = {0.f, 0.f};
    SB();
    sfor<10>([&](auto T) {
      constexpr int t = T;
      if constexpr (t == 0) x_more(IC(3));
      if constexpr (t + 4 < 9) x_more(IC(t + 4));
      if constexpr (t == 4) misc_loads();
      if constexpr (t == 0) visit<2, Sched<false>::NMARK, Sched<true>::NMARK>(vsel);
      if constexpr (t < 9) {
        waitv<4 * ((t + 4 > 8 ? 8 : t + 4) - t) + (t < 3 ? 6 : 0)>();
#pragma unroll
        for (int u = 0; u < 4; ++u) xf[4 * t + u] = *(LAS u32x4*)(xp[t / 3 == 1][u >> 1] + (t % 3 == 1 ? 4096 + (u & 1) * SLOT : (t % 3 == 2 ? SLOT : 0) + (u & 1) * 2048));
      }
      SB();
      if constexpr (t > 0) {
#pragma unroll
        for (int u = 0; u < 4; u += 2)
#pragma unroll
          for (int j = 0; j < 4; ++j) {
            const unsigned v0 = xf[4 * t - 4 + u][j], v1 = xf[4 * t - 3 + u][j];
            sm[0] = dot2(v0, ones, sm[0]); sm[1] = dot2(v1, ones, sm[1]); sq[0] = dot2(v0, v0, sq[0]); sq[1] = dot2(v1, v1, sq[1]);
          }
        PIN(sm[0]); PIN(sm[1]); PIN(sq[0]); PIN(sq[1]);
      }
      if constexpr (t == 8) sfor<6>([&](auto P) { w_piece(IC(6 + P)); });
      SB();
    });
#pragma unroll
    for (int tb = 0; tb < 2; ++tb) {
      const float negmu = xor32(xor16(sm[tb])) * (-1.0f / D_);
      nm[tb] = f32x4{negmu, negmu, negmu, negmu};
      rstd[tb] = __builtin_amdgcn_rsqf(fmaxf(xor32(xor16(sq[tb])) * (1.0f / D_) - negmu * negmu, 0.f) + 1e-5f);
    }
  }
  bf16x4 wl[NB], wu[NB];
  {
    f32x4 dl[3], dh[3];
    asm volatile("s_nop 1" : "+v"(nm[0]), "+v"(nm[1]));
    sfor<38>([&](auto B) {
      constexpr int b = B;
      if constexpr (b < 36) mfma_vvc(dl[b % 3], id0, xf[b], nm[b & 1]);
      if constexpr (b == 36) {
        asm volatile("s_waitcnt vmcnt(18)\n\ts_barrier" : "+v"(dl[1]), "+v"(dh[1]), "+v"(dl[2]), "+v"(dh[2]) : "v"(nm[0]), "v"(nm[1]) : "memory");
#pragma unroll
        for (int t = 0; t < LA; ++t) { wl[t] = rdhalf(rb[t & 1][0] + (t >> 1) * 4096); wu[t] = rdhalf(rb[t & 1][0] + (t >> 1) * 4096 + 2048); }
      }
      SB();
      if constexpr (b >= 2) { xf[b - 2][0] = cvtpk(dl[(b - 2) % 3][0], dl[(b - 2) % 3][1]); xf[b - 2][1] = cvtpk(dl[(b - 2) % 3][2], dl[(b - 2) % 3][3]); }
      SB();
      if constexpr (b < 36) mfma_vvc(dh[b % 3], id1, xf[b], nm[b & 1]);
      SB();
      if constexpr (b >= 2) { xf[b - 2][2] = cvtpk(dh[(b - 2) % 3][0], dh[(b - 2) % 3][1]); xf[b - 2][3] = cvtpk(dh[(b - 2) % 3][2], dh[(b - 2) % 3][3]); PIN(xf[b - 2]); }
      if constexpr (b % 2 == 0 && b < 24) w_piece(IC(12 + b / 2));
      SB();
    });
  }

  const u32x4 srdW2 = make_srd(w2, F_ * D_ * 2), srdWo = make_srd(wo, D_ * D_ * 2);
  const __amdgpu_buffer_rsrc_t rws = __builtin_amdgcn_make_buffer_rsrc(ws, 0, WS_BYTES, 0x00020000);
  gu64* flags = (gu64*)((char*)ws + flo);
  auto get_tag = [&]() __attribute__((always_inline)) { return (((u64)__float_as_uint(aread<13>()) << 32) | __float_as_uint(aread<12>())) * 0x9E3779B97F4A7C15ull + 0x632BE59BD9B4E019ull; };
  float t0, t1;
  f32x4 acc[2][2][2];
#define LANE_VALUES int tid_ = threadIdx.x; PIN(tid_); \
  const int tid = tid_, lane = tid & 63, n16 = lane & 15, r8 = lane >> 3, pos = lane & 7, G = lane >> 4, q4 = (lane >> 2) & 3, p4 = lane & 3; \
  const unsigned swz = pos ^ (((r8 >> 1) & 3) << 1), l16 = lane * 16, vflag = tid == 0 ? 0u : OOB, wa = rt * 72 + w * 18 + lane, wm = lane < 18 ? 0 : OOB; \
  (void)q4; (void)p4;

  if (!attn) {
    using SC = Sched<false>;
    LANE_VALUES
    constexpr bool attn_role = false;
    auto rd_a = [&](auto Sc, auto U) {
      constexpr int s = Sc, u = U;
      if constexpr (s < SC::NSTEP && !SC::skip(s) && !(u == 1 && s >= 864 && SC::a_ks(s) >= 8)) {
        constexpr int sl = SC::a_stage(s) % 6, ks = SC::a_ks(s);
        (u ? wu : wl)[s % NB] = rdhalf(rb[2 * SC::a_tile(s) + (ks & 1)][sl >> 1] + (sl & 1) * SLOT + (ks >> 1) * 4096 + u * 2048);
      }
    };
    auto mma1 = [&](auto Sc, auto H) {
      constexpr int s = Sc, k = s % 36, o = k & 1, tb = o ^ H;
      if constexpr (!SC::skip(s)) mfma_vv<k < 2>(acc[SC::a_tile(s)][o][tb], cat(wl[s % NB], wu[s % NB]), xf[(k & ~1) + tb]);
    };
    auto sync = [&](auto Sc, auto H) {
      constexpr int s = Sc, h = H;
      if constexpr (h == 0 && SC::need(s) > -25) waitv<SC::allow(s)>();
      if constexpr (h == 1 && (s == BMID || (attn_role && s == 480))) WAITL0();
      if constexpr (h == 1 && SC::bar(s)) BAR();
      if constexpr (h == 1 && SC::lwait(s)) __builtin_amdgcn_s_waitcnt(0xC07F | ((2 * (LA - (s < 468 && !attn_role ? WP1 : WP2))) << 8));
    };
    unsigned vA[3] = {va - 128, va - 128 + 8 * 9216 - 1024, va - 128 + 2 * (8 * 9216 - 1024)}, vB[3], lk[3], gso = sadd<128>(soA[1]), hst = hso[0];
    set_m0<4 * SLOT>(m6);
    u64 tag;
    unsigned vpoll, pso_;
    LAS char* tab = lds + TAB_OFF + G * 16;
    f32x4 bs[2], bn;
    f32x4 ac[6][2][2];
    unsigned e[8];
    u64 pollv = 0;
    const unsigned ov = n16 * 2304 + (G & 1) * 32 + (G >> 1) * 16;
    u32x4 xr[3];
    float u0[2], u1[2];
    auto op1 = [&](auto Tc, auto K) {
      constexpr int T = Tc, k = K;
      if constexpr (k == SC::ST0 || k == SC::ST1) { constexpr int q = k == SC::ST1; st16_sc1<(T & 1) * 2048 + q * 1024>(u32x4{e[4 * q], e[4 * q + 1], e[4 * q + 2], e[4 * q + 3]}, l16, srdws, hst); }
      else if constexpr (k == 58) { if constexpr (T & 1) hst = sadd<4096>(hst); }
      else {
        constexpr int kk = k > SC::ST0 ? k - 1 : k, r = (kk - 4) % 7;
        constexpr int j = kk < 4 ? kk : kk >= 53 ? kk - 49 : r == 0 ? 4 : r == 1 ? 0 : r == 2 ? 5 : r == 3 ? 1 : r == 4 ? 2 : r == 5 ? 3 : 6;
        constexpr int d = kk < 4 ? 0 : kk >= 53 ? 7 : (kk - 4) / 7 + (j < 4), hf = d & 1;
        const f32x4& a = acc[T & 1][(d >> 1) & 1][d >> 2];
        if constexpr (j == 0) u0[hf] = i_fma(a[2 * hf], rstd[d >> 2], bs[(d >> 1) & 1][2 * hf]);
        if constexpr (j == 1) u1[hf] = i_fma(a[2 * hf + 1], rstd[d >> 2], bs[(d >> 1) & 1][2 * hf + 1]);
        if constexpr (j == 2) u0[hf] = i_sqs(u0[hf]);
        if constexpr (j == 3) u1[hf] = i_sqs(u1[hf]);
        if constexpr (j == 4) e[d] = i_cvt(u0[hf], u1[hf]);
        if constexpr (j == 5) e[d] = i_relu(e[d]);
        if constexpr (j == 6) awrite<A_H + 8 * T + d>(e[d]);
      }
    };
    auto op2 = [&](auto Tc, auto K) {
      constexpr int T = Tc, k = K;
      if constexpr (k < 8) e[k] = i_cvt(ac[T][(k >> 1) & 1][k >> 2][2 * (k & 1)], ac[T][(k >> 1) & 1][k >> 2][2 * (k & 1) + 1]);
      else {
        constexpr int q = (k - 8) / 3, j = (k - 8) % 3;
        if constexpr (j < 2) i_swap16(e[4 * q + j], e[4 * q + 2 + j]);
        else st16_sc1<(T & 1) * 2048 + q * 1024>(u32x4{e[4 * q], e[4 * q + 1], e[4 * q + 2], e[4 * q + 3]}, lk[T >> 1], srdws, pso);
      }
    };
    auto slot = [&](auto Sc, auto H) {
      constexpr int s = Sc, h = H;
      sync(Sc, H);
      if constexpr (SC::g_set(s)) {
        constexpr int np = SC::g_stage(s + 3), p = SC::g_piece(s + 3), kb = (np - 18) / 3, src = kb < 2 ? 0 : 1 + (kb & 1), half = kb < 2 ? kb : (kb >= 4);
        if constexpr (h == 0) set_m0<(np % 6) * SLOT + p * 1024>(np < 36 ? m6 : m5);
        else if constexpr (np < 18) gso = sadd<8 * p * 9216 + 128 * (np / 3)>(soA[np % 3]);
        else if constexpr (np < 36) gso = sadd<(192 * half + 8 * p) * 1152 + 128 * ((np - 18) % 3)>(soB[src]);
        else gso = sadd<8 * p * 1152 + 128 * (np - 36)>(soO);
      }
      if constexpr (h == 0) {
        if constexpr (SC::g_stage(s) >= 0) { constexpr int np = SC::g_stage(s), j = SC::g_piece(s) % 3; glds3<j>(np < 18 ? vA[j] : vB[j], np < 18 ? srdA : np < 36 ? srdW2 : srdWo, gso); }
        if constexpr (SC::bias_rd(s) == 0) bn = *(LAS f32x4*)(tab + (s / 36) * 128);
        if constexpr (SC::bias_rd(s) == 1) { bs[0] = bn; bs[1] = *(LAS f32x4*)(tab + (s / 36) * 128 + 64); }
        if constexpr (SC::x_load(s) >= 0) { constexpr int k = SC::x_load(s); xr[k] = bload16(ov, srdx, os[k]); }
        if constexpr (SC::o_load(s) >= 0) { constexpr int f = SC::o_load(s); aload16_sc1<kb_areg(6) + 4 * f, (f & 3) * 1024>(lk[f >> 2], srdws, oso); }
        if constexpr (SC::h_load(s) >= 0) { constexpr int b = SC::h_load(s) / 12, f = SC::h_load(s) % 12; aload16_sc1<kb_areg(2 + b) + 4 * f, (f & 3) * 1024>(lk[f >> 2], srdws, hso[1 + (b & 1)]); }
      }
      if constexpr (SC::nop(s) > 0) {
        static_assert(SC::bias_rd(s) != 1 || s < 36 || SC::epi_from(s, 0) > 52, "the bias registers are free");
        constexpr int k0 = SC::epi_from(s, h), k1 = SC::epi_to(s, h);
        sfor<k1 - k0>([&](auto I) { if constexpr (s < 864) op1(IC(s / 36 - 1), IC(k0 + I)); else op2(IC((s - 864) / 10 - 1), IC(k0 + I)); if constexpr (s < 864 && k0 + I == SC::ST0 && k0 + I + 1 < k1) asm volatile("s_nop 1"); });
      }
    };
    sfor<77>([&](auto So) { sfor<12>([&](auto Si) {
      constexpr int s = 12 * So + Si;
      sfor<2>([&](auto H) {
        if constexpr (s < 432) mma1(IC(s), H);
        else {
          constexpr int n = SC::a_stage(s) - 18, kb = n / 3, T = 2 * (n % 3) + SC::a_tile(s), ks = SC::a_ks(s), o = ks & 1, tb = o ^ H, ar = kb_areg(kb) + 8 * (ks >> 1);
          if constexpr (kb == 6 && ks >= 8) mfma_k16<ar + 2 * tb>(ac[T][o][tb], wl[s % NB]);
          else mfma_va<kb == 0 && ks < 2, ar + 4 * tb>(ac[T][o][tb], cat(wl[s % NB], wu[s % NB]));
        }
        SB(); rd_a(IC(s + LA), H); SB();
        slot(IC(s), H); SB();
      });
      constexpr int k = SC::warm_k(s);
      if constexpr (SC::mark_k(s) >= 0) mark<0, SC::mark_k(s)>();
      if constexpr (k >= 0 && k < 2) warm(((wa >> 1) * 9216 + (wa & 1) * 128) | wm, srdA, f0 * 2 + 256 + 256 * k);
      if constexpr (k >= 2) warm((wa * 128) | wm, srdW2, f0 * 1152 + (k - 2) * 147456);
      if constexpr (s == SC::PUB_HA || s == SC::PUB_HB) bstore8_sc1(tag, vflag, srdws, flo + ((s == SC::PUB_HA ? FL_HA : FL_HB) + fs) * 128);
      if constexpr (s == SC::CHK_HA || s == SC::CHK_HB || s == SC::CHK_O) check_flag(pollv, tag, vpoll, srdws, pso_);
      if constexpr (s == 4) { const unsigned r = __float_as_uint(aread<14>()); *(LAS f32x2*)(lds + TAB_OFF + tid * 8) = f32x2{bflo(r), bfhi(r)}; }
      if constexpr (s == 8) { const unsigned r = __float_as_uint(aread<15>()); *(LAS f32x2*)(lds + TAB_OFF + 2048 + tid * 8) = f32x2{bflo(r), bfhi(r)}; }
      if constexpr (s == 12) { tag = get_tag(); PIN(tag); }
      if constexpr (s == 334) { vB[0] = r8 * 1152 + swz * 16; vB[1] = vB[0] + 8192; vB[2] = vB[0] + 16384; PIN(vB[0]); PIN(vB[1]); PIN(vB[2]); }
      if constexpr (s == 386) { vpoll = (3 * kg + ((lane & 1) ? c1 : c2)) * 128; PIN(vpoll); }
      if constexpr (s == 432) { lk[0] = l16; lk[1] = l16 + 4096; lk[2] = l16 + 8192; PIN(lk[1]); PIN(lk[2]); }
      if constexpr (s == 600) { hso[1] += 12288; hso[2] += 12288; PINS(hso[1]); PINS(hso[2]); }
      if constexpr (s == SC::POLL_HA || s == SC::POLL_HB || s == SC::POLL_O) {
        if constexpr (s == SC::POLL_O) vpoll = kg * 128;
        pso_ = flo + (s == SC::POLL_HA ? FL_HA : s == SC::POLL_HB ? FL_HB : FL_O) * 128;
        pollv = bload8_sc1(vpoll, srdws, pso_);
      }
      SB();
    }); });
    asm volatile("s_nop 7" : "+v"(ac[5][0][0]), "+v"(ac[5][0][1]), "+v"(ac[5][1][0]), "+v"(ac[5][1][1]));
    sfor<14>([&](auto K) { op2(IC(5), K); SB(); });
    asm volatile("s_nop 1" : "+v"(xr[0]), "+v"(xr[1]), "+v"(xr[2]));
    SB();
    float a[3][8];
    f32x4 cb[3][2];
#pragma unroll
    for (int k = 0; k < 3; ++k) {
      LAS char* bp = lds + TAB_OFF + 2048 + 128 * ((3 * kg + k) >> 1) + (G & 1) * 64 + (G >> 1) * 32;
      cb[k][0] = *(LAS f32x4*)bp; cb[k][1] = *(LAS f32x4*)(bp + 16);
    }
    gu64* fp = flags + (FL_FIN + 3 * (lane & 3) + cg) * 16;
    PIN(fp);
    SB();
#pragma unroll
    for (int k = 0; k < 3; ++k) {
      const f32x4 c0 = cb[k][0], c1 = cb[k][1];
#pragma unroll
      for (int j = 0; j < 4; ++j) { a[k][2 * j] = bflo(xr[k][j]) + (j < 2 ? c0 : c1)[(2 * j) & 3]; a[k][2 * j + 1] = bfhi(xr[k][j]) + (j < 2 ? c0 : c1)[(2 * j + 1) & 3]; PIN(a[k][2 * j]); PIN(a[k][2 * j + 1]); }
    }
    unsigned sel0 = 0x00003f80u, sel1 = 0x3f800000u, pb = P_OFF + ((rt * 3 + cg) * 16 + w) * 12288 + 3 * kg * 1024;
    PIN(sel0); PIN(sel1); PINS(pb);
    WAITV(0); BAR();
    bstore8_sc1(tag, vflag, srdws, flo + (FL_FIN + fs) * 128);
    spin_until(fp, tag);
    const __amdgpu_buffer_rsrc_t rout = __builtin_amdgcn_make_buffer_rsrc(out, 0, S_ * D_ * 2, 0x00020000);
    u32x4 v[3][4];
#pragma unroll
    for (int k = 0; k < 3; ++k)
#pragma unroll
      for (int s = 0; s < 4; ++s) v[k][s] = __builtin_amdgcn_raw_buffer_load_b128(rws, l16 + k * 1024, pb + s * 4 * 12288, 16);
#pragma unroll
    for (int k = 0; k < 3; ++k) {
#pragma unroll
      for (int s = 0; s < 4; ++s)
#pragma unroll
        for (int j = 0; j < 4; ++j) { a[k][2 * j] = dot2(v[k][s][j], sel0, a[k][2 * j]); a[k][2 * j + 1] = dot2(v[k][s][j], sel1, a[k][2 * j + 1]); }
      __builtin_amdgcn_raw_buffer_store_b128(u32x4{cvtpk(a[k][0], a[k][1]), cvtpk(a[k][2], a[k][3]), cvtpk(a[k][4], a[k][5]), cvtpk(a[k][6], a[k][7])}, rout, ov, os[k], 0);
    }
    if (fs == 0) bstore8_sc1(tag, vflag, srdws, flo + FL_EPOCH * 128);
    {
      u32x4 t4 = __builtin_amdgcn_raw_buffer_load_b128(rws, l16, 39u << 20, 16);
      const unsigned ref = (unsigned)tent;
      int mn = 1 << 30, mx = -(1 << 30);
      for (int j = 0; j < 4; ++j) { const int d = (int)(t4[j] - ref); mn = d < mn ? d : mn; mx = d > mx ? d : mx; }
      for (int o = 32; o; o >>= 1) { const int a = __shfl_xor(mn, o), b = __shfl_xor(mx, o); mn = a < mn ? a : mn; mx = b > mx ? b : mx; }
      unsigned skew = __builtin_amdgcn_readfirstlane(mx - mn);
      skew = skew > 2000 ? 2000 : skew;
      const unsigned base = ref + __builtin_amdgcn_readfirstlane(USEMIN ? mn : mx);
      u64 tn; do { asm volatile("s_memrealtime %0\n\ts_waitcnt lgkmcnt(0)" : "=s"(tn)); } while ((unsigned)tn - base < 4000 + KS * skew);
    }
    asm volatile(".Lk_end0:");
    __builtin_amdgcn_endpgm();
  }
  {
    using SC = Sched<true>;
    LANE_VALUES
    auto va_of = [&](unsigned word) { return r8 * 3456 + (swz & 1) * 16 + ((word >> (8 * (swz >> 1))) & 255) * 16; };
    constexpr bool attn_role = true;
    auto rd_a = [&](auto Sc, auto U) {
      constexpr int s = Sc, u = U;
      if constexpr (s < SC::NSTEP && !SC::skip(s) && !(u == 1 && s >= 864 && SC::a_ks(s) >= 8)) {
        constexpr int sl = SC::a_stage(s) % 6, ks = SC::a_ks(s);
        (u ? wu : wl)[s % NB] = rdhalf(rb[2 * SC::a_tile(s) + (ks & 1)][sl >> 1] + (sl & 1) * SLOT + (ks >> 1) * 4096 + u * 2048);
      }
    };
    auto mma1 = [&](auto Sc, auto H) {
      constexpr int s = Sc, k = s % 36, o = k & 1, tb = o ^ H;
      if constexpr (!SC::skip(s)) mfma_vv<k < 2>(acc[SC::a_tile(s)][o][tb], cat(wl[s % NB], wu[s % NB]), xf[(k & ~1) + tb]);
    };
    auto sync = [&](auto Sc, auto H) {
      constexpr int s = Sc, h = H;
      if constexpr (h == 0 && SC::need(s) > -25) waitv<SC::allow(s)>();
      if constexpr (h == 1 && (s == BMID || (attn_role && s == 480))) WAITL0();
      if constexpr (h == 1 && SC::bar(s)) BAR();
      if constexpr (h == 1 && SC::lwait(s)) __builtin_amdgcn_s_waitcnt(0xC07F | ((2 * (LA - (s < 468 && !attn_role ? WP1 : WP2))) << 8));
    };
    unsigned vA[3] = {va, va + 8 * 3456 - 1024, va + 2 * (8 * 3456 - 1024)}, gso = soA[1];
    set_m0<4 * SLOT>(m6);
    f32x4 gram;
    float rstdh, negmuh;
    const unsigned hrow = n16 < 8 ? n16 : 0;
    auto halo_val = [&](auto I) {
      f32x4 r;
      sfor<4>([&](auto K) { const float a = aread<A_HH + 4 * (I - 2) + K>(); r[(int)K] = fmaf(ror8(a), negmuh, a) * rstdh; });
      return r;
    };
    auto halo_col = [&](auto I) { return ((qd[I] >> (8 * w)) & 255); };
    LAS char* hb[3];
#pragma unroll
    for (int i = 0; i < 3; ++i) { hb[i] = (LAS char*)(uintptr_t)((unsigned)(uintptr_t)rb[0][i] ^ (w << 5)); PIN(hb[i]); }
    const unsigned ha = (unsigned)(uintptr_t)lds + TAB_OFF + hrow * 128 + G * 8;
    const unsigned qrow = (unsigned)(uintptr_t)lds + QK_OFF + (32 * w + n16 + 8) * QK_RS + G * 8, vrow = (unsigned)(uintptr_t)lds + (32 * w + n16 + 8) * V_RS + G * 16;
    bf16x4 hl[2], hu[2];
    auto opa = [&](auto Tc, auto K) {
      constexpr int T = Tc, d = K / 4, j = K % 4, p = 2 * T + (d >> 2), tb = (d >> 1) & 1, hf = d & 1;
      if constexpr (p != 7) {
        const f32x4& a = acc[T & 1][d >> 2][tb];
        if constexpr (j == 0) t0 = i_mul(a[2 * hf], rstd[tb]);
        if constexpr (j == 1) t1 = i_mul(a[2 * hf + 1], rstd[tb]);
        if constexpr (p < 19) {
          if constexpr (j == 2) t0 = __uint_as_float(i_cvt(t0, t1));
          if constexpr (j == 3) awrite<A_Q + 4 * p + 2 * tb + hf>(__float_as_uint(t0));
        } else if constexpr (j >= 2) awrite<A_V + 8 * (p - 19) + 4 * tb + 2 * hf + j - 2>(__float_as_uint(j == 2 ? t0 : t1));
      }
    };
    sfor<28>([&](auto So) { sfor<18>([&](auto Si) {
      constexpr int s = 18 * So + Si, i = s / 72, u = s % 72;
      sfor<2>([&](auto H) {
        constexpr int h = H;
        mma1(IC(s), H);
        SB(); rd_a(IC(s + LA), H); SB();
        sync(IC(s), H);
        if constexpr (SC::g_set(s)) {
          constexpr int np = SC::g_stage(s + 3), p = SC::g_piece(s + 3);
          if constexpr (h == 0) set_m0<(np % 6) * SLOT + p * 1024>(m6); else gso = sadd<8 * p * 3456>(soA[np % 3]);
        }
        if constexpr (h == 0 && SC::g_stage(s) >= 0) glds3<SC::g_piece(s) % 3>(vA[SC::g_piece(s) % 3], srdA, gso);
        if constexpr (SC::nop(s) > 0) { constexpr int k0 = SC::epi_from(s, h), k1 = SC::epi_to(s, h); sfor<k1 - k0>([&](auto I) { opa(IC(s / 36 - 1), IC(k0 + I)); }); }
        SB();
      });
      if constexpr (u % 4 == 2) {
        constexpr int sg = u / 4;
        if constexpr (s >= 46 && s <= 114) mfma16_aa<s == 46, A_HX + s - 46>(gram);
        if constexpr (i >= 2) mfma16_ava<sg == 0, A_HH + 4 * (i - 2), A_HX + 4 * sg>(cat(hl[sg & 1], hu[sg & 1]));
        SB();
        if constexpr (s >= 22 && s < 94) { constexpr int f = (s - 22) / 4; ds_read_a64<A_HX + 4 * f, (f >> 1) * 1024 + (f & 1) * 64>(ha); ds_read_a64<A_HX + 4 * f + 2, (f >> 1) * 1024 + (f & 1) * 64 + 32>(ha); }
        constexpr int s2 = s + 8, sl = (3 * (s2 / 72) + (s2 % 72) / 24) % 6;
        if constexpr (s2 >= 144 && s2 < 504) { LAS char* p = hb[sl >> 1] + (sl & 1) * SLOT + (((s2 % 72) / 4) % 6) * 4096; hl[sg & 1] = rdhalf(p); hu[sg & 1] = rdhalf(p + 2048); }
        if constexpr (s == 122) {
          const float dg = (p4 == 0) ? gram[0] : (p4 == 1) ? gram[1] : (p4 == 2) ? gram[2] : gram[3];
          rstdh = G == q4 ? dg : 0.f; negmuh = G == 2 ? gram[0] : 0.f;
          PIN(rstdh); PIN(negmuh);
        }
        if constexpr (s == 126) { rstdh = xor16(rstdh); negmuh = xor16(negmuh); PIN(rstdh); PIN(negmuh); }
        if constexpr (s == 130) { rstdh = xor32(rstdh); negmuh = xor32(negmuh); PIN(rstdh); PIN(negmuh); }
        if constexpr (s == 134) {
          negmuh *= -1.0f / D_;
          rstdh = __builtin_amdgcn_rsqf(fmaxf(rstdh * (1.0f / D_) - negmuh * negmuh, 0.f) + 1e-5f);
          PIN(rstdh); PIN(negmuh);
        }
      }
      constexpr int k = SC::warm_k(s);
      if constexpr (SC::mark_k(s) >= 0) mark<1, SC::mark_k(s)>();
      if constexpr (k >= 0 && k < 4) {
        const unsigned la = (288 * kg + ((qs[k < 3 ? 2 + k : 5] >> (k == 2 ? 16 : 0)) & 255) * 16) & ~127u, lb = (288 * kg + (k < 3 ? qs[2 + k] >> 24 : qs[6] & 255) * 16) & ~127u;
        warm(((wa >> 1) * 3456 + ((wa & 1) ? lb : la)) | wm, srdA, 0);
      }
      if constexpr (k == 4) warm(((rt * 81 + w * 21 + lane) * 128) | (lane < 21 ? 0 : OOB), srdWo, 144 * kg * 1152);
      if constexpr (s == 2) *(LAS u32x4*)(lds + TAB_OFF + w * 3072 + (r8 < 3 ? r8 : 0) * 1024 + pos * 16) = u32x4{0x3f803f80u, 0x3f803f80u, 0x3f803f80u, 0x3f803f80u};
      if constexpr (u == 47 && i < 5) { vA[0] = va_of(qs[i + 2]); vA[1] = vA[0] + 8 * 3456 - 1024; vA[2] = vA[0] + 2 * (8 * 3456 - 1024); PIN(vA[0]); PIN(vA[1]); PIN(vA[2]); }
      if constexpr (s > 432 && s < 471 && s % 2 == 1 && s != 447) {
        constexpr int p = (s - 433) / 2;
        const unsigned ad = qrow + ((qd[p / 4] >> (8 * (p % 4))) & 255) * 16;
        ds_write_a64<A_Q + 4 * p, 0>(ad); ds_write_a64<A_Q + 4 * p + 2, 16 * QK_RS>(ad);
      }
      if constexpr (s == 471 || s == 473 || s == 477) {
        constexpr int hi = s == 471 ? 2 : s == 473 ? 3 : 4;
        const f32x4 r = halo_val(IC(hi));
        const unsigned row = (hi == 4 && w == 3) ? 0 : hrow;
        *(LAS u32x2*)(lds + QK_OFF + row * QK_RS + halo_col(IC(hi)) * 16 + G * 8) = u32x2{cvtpk(r[0], r[1]), cvtpk(r[2], r[3])};
      }
      SB();
    }); });
    asm volatile("s_nop 7" : "+v"(acc[1][0][0]), "+v"(acc[1][0][1]), "+v"(acc[1][1][0]), "+v"(acc[1][1][1]));
    WAITL0(); BAR();
    {
      const int hd = lane & 7, i0 = 32 * w + 4 * r8;
      LAS char* rp[11];
#pragma unroll
      for (int jj = 0; jj < 11; ++jj) { rp[jj] = lds + QK_OFF + (i0 + 1 + jj) * QK_RS + hd * 36; PIN(rp[jj]); }
      unsigned qv[4][9], kv[11][9];
#pragma unroll
      for (int a = 0; a < 4; ++a)
#pragma unroll
        for (int c = 0; c < 9; ++c) qv[a][c] = *(LAS unsigned*)(rp[7 + a] + c * 4);
#pragma unroll
      for (int jj = 0; jj < 4; ++jj)
#pragma unroll
        for (int c = 0; c < 9; ++c) kv[jj][c] = *(LAS unsigned*)(rp[jj] + 288 + c * 4);
      {
#pragma unroll
        for (int k = 0; k < 4; ++k)
          *(LAS f32x4*)(uintptr_t)(vrow + ((qd[6] >> (16 + 8 * (k >> 1))) & 255) * 32 + (k & 1) * 16 * V_RS) = acc[1][k >> 1][k & 1] * rstd[k & 1];
        sfor<3>([&](auto I) {
          constexpr int hi = I + 4;
          const unsigned row = (hi == 4 && w != 3) ? 0 : hrow, col = (hi == 4 && w != 3) ? 0 : halo_col(IC(hi));
          *(LAS f32x4*)(lds + row * V_RS + col * 32 + G * 16) = halo_val(IC(hi));
        });
      }
      SB();
      float lg[4][8];
      sfor<8>([&](auto U) {
        constexpr int u = U;
        if constexpr (u + 4 < 11) {
#pragma unroll
          for (int c = 0; c < 9; ++c) kv[u + 4][c] = *(LAS unsigned*)(rp[u + 4] + 288 + c * 4);
        }
        if constexpr (u < 7) {
          constexpr int p = 19 + u;
          const unsigned ad = vrow + ((qd[p / 4] >> (8 * (p % 4))) & 255) * 32;
          ds_write_a128<A_V + 8 * u, 0>(ad); ds_write_a128<A_V + 8 * u + 4, 16 * V_RS>(ad);
        }
#pragma unroll
        for (int a = 0; a < 4; ++a) lg[a][u] = 0.f;
#pragma unroll
        for (int c = 0; c < 9; ++c)
#pragma unroll
          for (int a = 0; a < 4; ++a) lg[a][u] = dot2(qv[a][c], kv[a + u][c], lg[a][u]);
        SB();
      });
      WAITL0(); BAR();
      mark<1, 7>();
      LAS char* vp = lds + (i0 + 1) * V_RS + hd * 72;
      f32x2 vv[11][9];
#pragma unroll
      for (int jj = 0; jj < 3; ++jj)
#pragma unroll
        for (int c = 0; c < 9; ++c) vv[jj][c] = *(volatile LAS f32x2*)(vp + jj * V_RS + c * 8);
      if (rt < 2) {
#pragma unroll
        for (int a = 0; a < 4; ++a)
#pragma unroll
          for (int u = 0; u < 7 - a; ++u) lg[a][u] = (i0 + a + u < 7) ? -1e9f : lg[a][u];
      }
      constexpr float CS = 0.23570226039551584f * 1.4426950408889634f;
#pragma unroll
      for (int a = 0; a < 4; ++a) {
        float mx = lg[a][0];
#pragma unroll
        for (int u = 1; u < 8; ++u) mx = fmaxf(mx, lg[a][u]);
        const float nb = -mx * CS;
        float sum = 0.f;
#pragma unroll
        for (int u = 0; u < 8; ++u) { lg[a][u] = __builtin_amdgcn_exp2f(fmaf(lg[a][u], CS, nb)); sum += lg[a][u]; }
        const float inv = __builtin_amdgcn_rcpf(sum);
#pragma unroll
        for (int u = 0; u < 8; ++u) lg[a][u] *= inv;
      }
      SB();
      f32x2 o[4][9];
      sfor<11>([&](auto J) {
        constexpr int jj = J;
        if constexpr (jj + 3 < 11) {
#pragma unroll
          for (int c = 0; c < 9; ++c) vv[jj + 3][c] = *(volatile LAS f32x2*)(vp + (jj + 3) * V_RS + c * 8);
        }
#pragma unroll
        for (int c = 0; c < 9; ++c) {
#pragma unroll
          for (int a = 0; a < 4; ++a) {
            if (jj == a) o[a][c] = vv[jj][c] * lg[a][0];
            else if (jj > a && jj <= a + 7) o[a][c] = __builtin_elementwise_fma(vv[jj][c], f32x2{lg[a][jj - a], lg[a][jj - a]}, o[a][c]);
          }
        }
        if constexpr (jj == 7) mark<1, 8>();
        if constexpr (jj >= 7) {
#pragma unroll
          for (int c = 0; c < 9; ++c) *(LAS unsigned*)(rp[jj] + c * 4) = cvtpk(o[jj - 7][c][0], o[jj - 7][c][1]);
        }
        SB();
      });
    }
    {
      LAS char* rowp = (LAS char*)(uintptr_t)qrow;
#pragma unroll
      for (int f = 0; f < 9; ++f) {
        const u32x2 a = *(LAS u32x2*)(rowp + (f >> 1) * 64 + (f & 1) * 16 * QK_RS), b = *(LAS u32x2*)(rowp + (f < 8 ? (f >> 1) * 64 + (f & 1) * 16 * QK_RS + 32 : 256 + 16 * QK_RS));
        __builtin_amdgcn_raw_buffer_store_b128(u32x4{a[0], a[1], b[0], b[1]}, rws, l16, oso + f * 1024, 16);
      }
    }
    WAITV(0); BAR();
    bstore8_sc1(get_tag(), vflag, srdws, flo + (FL_O + kg) * 128);
  }
  asm volatile(".Lk_end1:");
}

extern "C" void kernel_launch(void* const* d_in, const int* in_sizes, int n_in, void* d_out, int out_size, void* d_ws, size_t ws_size, hipStream_t stream) {
  (void)hipFuncSetAttribute((const void*)main_k, hipFuncAttributeMaxDynamicSharedMemorySize, LDS_BYTES);
  const Args args = {d_in[0], d_in[1], d_in[2], d_in[4], d_in[5], d_in[6], d_in[7], d_out, d_ws, make_tab()};
  main_k<<<256, 256, LDS_BYTES, stream>>>(args);
}
```

```cpp
#define POINT 0
#include <hip/hip_runtime.h>
#include <stdint.h>
#include <utility>

typedef __bf16 bf16x8 __attribute__((ext_vector_type(8)));
typedef __bf16 bf16x4 __attribute__((ext_vector_type(4)));
typedef __bf16 bf16x2 __attribute__((ext_vector_type(2)));
typedef float f32x4 __attribute__((ext_vector_type(4)));
typedef float f32x2 __attribute__((ext_vector_type(2)));
typedef unsigned u32x4 __attribute__((ext_vector_type(4)));
typedef unsigned u32x2 __attribute__((ext_vector_type(2)));
typedef unsigned u32x16 __attribute__((ext_vector_type(16)));
typedef unsigned long long u64;
#define LAS __attribute__((address_space(3)))
#define GAS __attribute__((address_space(1)))
typedef GAS u64 gu64;
#define DI __device__ __forceinline__
#define SB() __builtin_amdgcn_sched_barrier(0)
#define IC(n) std::integral_constant<int, (n)>{}
#define RLX_AGENT __ATOMIC_RELAXED, __HIP_MEMORY_SCOPE_AGENT

template <class F, int... I> DI void sfor_(F&& f, std::integer_sequence<int, I...>) { (f(std::integral_constant<int, I>{}), ...); }
template <int N, class F> DI void sfor(F&& f) { sfor_(f, std::make_integer_sequence<int, N>{}); }

constexpr int S_ = 2048, D_ = 576, F_ = 4608;
constexpr int SLOT = 24576;
constexpr int TAB_OFF = 147456;
constexpr int LDS_BYTES = 163840;
constexpr int QK_OFF = 80512, QK_RS = 584, V_RS = 592;
constexpr unsigned H_OFF = 0, O_OFF = 20u << 20, P_OFF = 24u << 20, FLAG_OFF = 40u << 20, WS_BYTES = 41u << 20;
constexpr unsigned OOB = 0x40000000u;
constexpr int FL_HA = 0, FL_HB = 12, FL_O = 24, FL_FIN = 28, FL_EPOCH = 40, FL_N = 41;
constexpr int A_H = 56;
constexpr int A_Q = 16;
constexpr int A_V = 92;
constexpr int A_HH = 164;
constexpr int A_HX = 184;

DI int map_role(int bid) { return (bid >> 3) & 3; }
DI int map_kg(int bid) { return bid & 3; }
DI int map_rt(int bid) { return ((bid >> 2) & 1) * 8 + (bid >> 5); }

DI u32x4 make_srd(const void* p, unsigned bytes) {
  uint64_t a = (uint64_t)p;
  u32x4 r; r[0] = (unsigned)a; r[1] = (unsigned)(a >> 32) & 0xffffu; r[2] = bytes; r[3] = 0x00020000u;
  return r;
}
template <unsigned MC, unsigned SC> DI void glds16c(unsigned mb, unsigned voff, u32x4 srd, unsigned sb) {
  unsigned tmp;
  asm volatile("s_add_u32 m0, %1, %5\n\ts_add_u32 %0, %4, %6\n\tbuffer_load_dwordx4 %2, %3, %0 offen lds"
               : "=&s"(tmp) : "s"(mb), "v"(voff), "s"(srd), "s"(sb), "i"(MC), "i"(SC) : "memory", "scc");
}
template <unsigned C> DI void set_m0(unsigned b) { asm volatile("s_add_u32 m0, %0, %1" :: "s"(b), "i"(C) : "scc"); }
template <unsigned C> DI unsigned sadd(unsigned b) { unsigned r; asm volatile("s_add_u32 %0, %1, %2" : "=s"(r) : "s"(b), "i"(C) : "scc"); return r; }
template <int J> DI void glds3(unsigned v, u32x4 srd, unsigned so) { asm volatile("buffer_load_dwordx4 %0, %1, %2 offen offset:%c3 lds" :: "v"(v), "s"(srd), "s"(so), "i"(J * 1024) : "memory"); }
template <unsigned MC> DI void glds16r(unsigned mb, unsigned voff, u32x4 srd, unsigned& so, unsigned step) {
  asm volatile("s_add_u32 m0, %1, %5\n\ts_add_u32 %0, %0, %4\n\tbuffer_load_dwordx4 %2, %3, %0 offen lds"
               : "+s"(so) : "s"(mb), "v"(voff), "s"(srd), "s"(step), "i"(MC) : "memory", "scc");
}
constexpr int x_base(int t) { return (t / 3 == 1 ? 2 : 4) * SLOT + (t % 3 == 2 ? SLOT : 0); }
template <int T> DI void x_line(unsigned m6, unsigned vx0, unsigned vx1, u32x4 srd, unsigned s0, unsigned s1, unsigned s2, unsigned s3) {
  constexpr int d0 = x_base(T) + (T % 3 == 1 ? 4096 : 0) - (T % 3) * 128, d2 = T % 3 == 1 ? SLOT - 1024 : 1024;
  asm volatile("s_add_u32 m0, %0, %8\n\ts_nop 0\n\tbuffer_load_dwordx4 %1, %3, %4 offen offset:%c10 lds\n\t"
               "s_addk_i32 m0, 0x400\n\ts_nop 0\n\tbuffer_load_dwordx4 %2, %3, %5 offen offset:%c10 lds\n\t"
               "s_addk_i32 m0, %c9\n\ts_nop 0\n\tbuffer_load_dwordx4 %1, %3, %6 offen offset:%c10 lds\n\t"
               "s_addk_i32 m0, 0x400\n\ts_nop 0\n\tbuffer_load_dwordx4 %2, %3, %7 offen offset:%c10 lds"
               :: "s"(m6), "v"(vx0), "v"(vx1), "s"(srd), "s"(s0), "s"(s1), "s"(s2), "s"(s3), "i"(d0), "i"(d2), "i"((T % 3) * 128) : "memory", "scc");
}
DI u32x4 bload16(unsigned voff, u32x4 srd, unsigned soff) {
  u32x4 r;
  asm volatile("buffer_load_dwordx4 %0, %1, %2, %3 offen" : "=v"(r) : "v"(voff), "s"(srd), "s"(soff) : "memory");
  return r;
}
DI void warm(unsigned voff, u32x4 srd, unsigned soff) {
  asm volatile("buffer_load_dword a11, %0, %1, %2 offen" :: "v"(voff), "s"(srd), "s"(soff) : "memory");
}
DI u64 bload8_sc1(unsigned voff, u32x4 srd, unsigned soff) {
  u64 r;
  asm volatile("buffer_load_dwordx2 %0, %1, %2, %3 offen sc1" : "=v"(r) : "v"(voff), "s"(srd), "s"(soff) : "memory");
  return r;
}
template <int AR, int OFF> DI void aload16_sc1(unsigned voff, u32x4 srd, unsigned soff) {
  asm volatile("buffer_load_dwordx4 a[%c3:%c4], %0, %1, %2 offen offset:%c5 sc1" :: "v"(voff), "s"(srd), "s"(soff), "i"(AR), "i"(AR + 3), "i"(OFF) : "memory");
}
template <int ROLE, int K> DI void mark() { asm volatile("s_branch 1f\n\t.p2align 12\n.Lm%c0_%c1:\n\ts_setpc_b64 vcc\n1:" :: "i"(ROLE), "i"(K)); }
template <int R, int N0, int N1> DI void visit(unsigned sel) {
  constexpr auto c = [](int k, int n) { return k < n ? k : n - 1; };
  asm volatile("s_getpc_b64 vcc\n\ts_add_u32 s90, vcc_lo, %0\n\ts_addc_u32 s91, vcc_hi, 0\n\ts_add_u32 vcc_lo, vcc_lo, 52\n\ts_addc_u32 vcc_hi, vcc_hi, 0\n\ts_setpc_b64 s[90:91]\n\t"
               "s_branch .Lm0_%c1\n\ts_branch .Lm0_%c2\n\ts_branch .Lm0_%c3\n\ts_branch .Lm0_%c4\n\ts_branch .Lm1_%c5\n\ts_branch .Lm1_%c6\n\ts_branch .Lm1_%c7\n\ts_branch .Lm1_%c8"
               :: "s"(sel + 20), "i"(c(4 * R, N0)), "i"(c(4 * R + 1, N0)), "i"(c(4 * R + 2, N0)), "i"(c(4 * R + 3, N0)),
                  "i"(c(4 * R, N1)), "i"(c(4 * R + 1, N1)), "i"(c(4 * R + 2, N1)), "i"(c(4 * R + 3, N1)) : "vcc", "s90", "s91", "scc");
}
template <int N> DI void waitv() { asm volatile("s_waitcnt vmcnt(%c0)" :: "i"(N > 63 ? 63 : N) : "memory"); }
#define WAITV(n) asm volatile("s_waitcnt vmcnt(" #n ")" ::: "memory")
#define BAR() asm volatile("s_barrier" ::: "memory")
#define WAITL0() asm volatile("s_waitcnt lgkmcnt(0)" ::: "memory")
#define PIN(x) asm volatile("" : "+v"(x))
#define PINS(x) asm volatile("" : "+s"(x))

DI unsigned cvtpk(float lo, float hi) { f32x2 v = {lo, hi}; bf16x2 b = __builtin_convertvector(v, bf16x2); return __builtin_bit_cast(unsigned, b); }
DI float bflo(unsigned u) { return __uint_as_float(u << 16); }
DI float bfhi(unsigned u) { return __uint_as_float(u & 0xffff0000u); }
DI float dot2(unsigned a, unsigned b, float c) { return __builtin_amdgcn_fdot2_f32_bf16(__builtin_bit_cast(bf16x2, a), __builtin_bit_cast(bf16x2, b), c, false); }
DI float xor32(float v) { auto r = __builtin_amdgcn_permlane32_swap(__float_as_uint(v), __float_as_uint(v), false, false); return __uint_as_float(r[0]) + __uint_as_float(r[1]); }
DI float xor16(float v) { auto r = __builtin_amdgcn_permlane16_swap(__float_as_uint(v), __float_as_uint(v), false, false); return __uint_as_float(r[0]) + __uint_as_float(r[1]); }

DI bf16x4 rdhalf(LAS char* p) { return __builtin_amdgcn_ds_read_tr16_b64_v4bf16((LAS bf16x4*)p); }
DI bf16x8 cat(bf16x4 a, bf16x4 b) { return __builtin_shufflevector(a, b, 0, 1, 2, 3, 4, 5, 6, 7); }
template <bool FIRST> DI void mfma_vv(f32x4& acc, bf16x8 a, u32x4 b) {
  if constexpr (FIRST) asm volatile("v_mfma_f32_16x16x32_bf16 %0, %1, %2, 0" : "=&v"(acc) : "v"(a), "v"(b));
  else                 asm volatile("v_mfma_f32_16x16x32_bf16 %0, %1, %2, %0" : "+v"(acc) : "v"(a), "v"(b));
}
DI void mfma_vvc(f32x4& acc, u32x4 a, u32x4 b, const f32x4& c) { asm volatile("v_mfma_f32_16x16x32_bf16 %0, %1, %2, %3" : "=&v"(acc) : "v"(a), "v"(b), "v"(c)); }
template <bool FIRST, int AR> DI void mfma_va(f32x4& acc, bf16x8 a) {
  if constexpr (FIRST) asm volatile("v_mfma_f32_16x16x32_bf16 %0, %1, a[%c2:%c3], 0" : "=&v"(acc) : "v"(a), "i"(AR), "i"(AR + 3));
  else                 asm volatile("v_mfma_f32_16x16x32_bf16 %0, %1, a[%c2:%c3], %0" : "+v"(acc) : "v"(a), "i"(AR), "i"(AR + 3));
}
template <int AR> DI void mfma_k16(f32x4& acc, bf16x4 a) { asm volatile("v_mfma_f32_16x16x16_bf16 %0, %1, a[%c2:%c3], %0" : "+v"(acc) : "v"(a), "i"(AR), "i"(AR + 1)); }
template <bool FIRST, int AR> DI void mfma16_aa(f32x4& acc) {
  if constexpr (FIRST) asm volatile("v_mfma_f32_16x16x32_bf16 %0, a[%c1:%c2], a[%c1:%c2], 0" : "=&v"(acc) : "i"(AR), "i"(AR + 3));
  else                 asm volatile("v_mfma_f32_16x16x32_bf16 %0, a[%c1:%c2], a[%c1:%c2], %0" : "+v"(acc) : "i"(AR), "i"(AR + 3));
}
template <bool FIRST, int AD, int AR> DI void mfma16_ava(bf16x8 a) {
  if constexpr (FIRST) asm volatile("v_mfma_f32_16x16x32_bf16 a[%c1:%c2], %0, a[%c3:%c4], 0" :: "v"(a), "i"(AD), "i"(AD + 3), "i"(AR), "i"(AR + 3));
  else                 asm volatile("v_mfma_f32_16x16x32_bf16 a[%c1:%c2], %0, a[%c3:%c4], a[%c1:%c2]" :: "v"(a), "i"(AD), "i"(AD + 3), "i"(AR), "i"(AR + 3));
}
template <int AR> DI float aread(void) { float r; asm volatile("v_accvgpr_read_b32 %0, a[%c1]" : "=v"(r) : "i"(AR)); return r; }
template <int AR, int OFF> DI void ds_write_a128(unsigned addr) { asm volatile("ds_write_b128 %0, a[%c1:%c2] offset:%c3" :: "v"(addr), "i"(AR), "i"(AR + 3), "i"(OFF) : "memory"); }
DI float ror8(float v) { return __int_as_float(__builtin_amdgcn_update_dpp(0, __float_as_int(v), 0x128, 0xf, 0xf, false)); }
template <int AR> DI void awrite(unsigned v) { asm volatile("v_accvgpr_write_b32 a[%c1], %0" :: "v"(v), "i"(AR)); }
DI float i_fma(float a, float b, float c) { float r; asm volatile("v_fma_f32 %0, %1, %2, %3" : "=v"(r) : "v"(a), "v"(b), "v"(c)); return r; }
DI float i_mul(float a, float b) { float r; asm volatile("v_mul_f32_e32 %0, %1, %2" : "=v"(r) : "v"(a), "v"(b)); return r; }
DI float i_sqs(float a) { float r; asm volatile("v_mul_f32_e64 %0, %1, |%1|" : "=v"(r) : "v"(a)); return r; }
DI unsigned i_cvt(float a, float b) { unsigned r; asm volatile("v_cvt_pk_bf16_f32 %0, %1, %2" : "=v"(r) : "v"(a), "v"(b)); return r; }
DI unsigned i_relu(unsigned a) { unsigned r; asm volatile("v_pk_max_i16 %0, %1, 0" : "=v"(r) : "v"(a)); return r; }
DI void i_swap16(unsigned& a, unsigned& b) { asm volatile("v_permlane16_swap_b32 %0, %1" : "+v"(a), "+v"(b)); }
template <int OFF> DI void st16_sc1(u32x4 v, unsigned voff, u32x4 srd, unsigned soff) {
  asm volatile("buffer_store_dwordx4 %0, %1, %2, %3 offen offset:%c4 sc1" :: "v"(v), "v"(voff), "s"(srd), "s"(soff), "i"(OFF) : "memory");
}
template <int AR, int OFF> DI void ds_read_a64(unsigned addr) { asm volatile("ds_read_b64 a[%c1:%c2], %0 offset:%c3" :: "v"(addr), "i"(AR), "i"(AR + 1), "i"(OFF) : "memory"); }
DI void bstore8_sc1(u64 v, unsigned voff, u32x4 srd, unsigned soff) { asm volatile("buffer_store_dwordx2 %0, %1, %2, %3 offen sc1" :: "v"(v), "v"(voff), "s"(srd), "s"(soff) : "memory"); }
template <int AR, int OFF> DI void ds_write_a64(unsigned addr) { asm volatile("ds_write_b64 %0, a[%c1:%c2] offset:%c3" :: "v"(addr), "i"(AR), "i"(AR + 1), "i"(OFF) : "memory"); }

constexpr int LA = 7;
constexpr int WP1 = 4, WP2 = 2;
constexpr int NB = LA + 1, BMID = (23 - LA) & ~1;
template <bool ATTN> struct Sched {
  static constexpr int P1 = ATTN ? 504 : 432, NSTEP = ATTN ? 504 : 924, NSTAGE = ATTN ? 21 : 39;
  static constexpr int a_stage(int s) { return s < P1 ? 3 * (s / 72) + (s % 36) / 12 : s < 864 ? s / 24 : 36 + (s - 864) / 20; }
  static constexpr int a_tile(int s) { return s < P1 ? (s % 72) / 36 : s < 864 ? (s % 24) / 12 : ((s - 864) % 20) / 10; }
  static constexpr int a_ks(int s) { return s < 864 ? s % 12 : (s - 864) % 10; }
  static constexpr bool skip(int s) { return ATTN && s >= 108 && s < 144 && s % 2 == 1; }
  static constexpr int g_stage(int s) {
    if (s < 0) return (s + 24) / 6;
    if (s >= 864 || s % 4 != 1) return -1;
    const int st = s / 24 + 4;
    return st >= NSTAGE || (st >= 36 && (s % 24) / 4 == 5) ? -1 : st;
  }
  static constexpr int g_piece(int s) { return (s % 24) / 4; }
  static constexpr int g_last(int st) { int r = -25; for (int s = -24; s < NSTEP; ++s) if (g_stage(s) == st) r = s; return r; }
  static constexpr int warm_k(int s) { return s >= 0 && s % 48 == 0 && s / 48 < 5 ? s / 48 : -1; }
  static constexpr int PUB_HA = 288 + BMID, PUB_HB = 504, POLL_HA = 388, CHK_HA = 432, POLL_HB = 580, CHK_HB = 624, POLL_O = 792, CHK_O = 828;
  static constexpr int h_load(int s) {
    if (ATTN || s % 4 != 3) return -1;
    return s >= 432 && s < 480 ? (s - 432) / 4 : s >= 504 && s < 600 ? (s % 8 == 3 ? 12 + (s - 504) / 8 : -1) : s >= 624 && s < 720 ? 24 + (s - 624) / 4 : -1;
  }
  static constexpr int o_load(int s) { return !ATTN && s > CHK_O && s < CHK_O + 19 && s % 4 > 1 ? (s - CHK_O) / 4 * 2 + s % 4 - 2 : -1; }
  static constexpr int x_load(int s) { return !ATTN && s >= 723 && s < 771 && s % 16 == 3 ? (s - 723) / 16 : -1; }
  static constexpr int NMARK = ATTN ? 9 : 11;
  static constexpr int mark_k(int s) {
    constexpr int M[] = {2, 84, 162, 240, 318, 396, 474, 574, 674, 774, 876}, A[] = {16, 98, 190, 268, 348, 430, 492};
    if (ATTN) { for (int k = 0; k < 7; ++k) if (s == A[k]) return k; }
    else { for (int k = 0; k < 11; ++k) if (s == M[k]) return k; }
    return -1;
  }
  static constexpr bool bar(int s) { return s < P1 ? (s % 72 == 0 || s % 72 == BMID || s % 72 == 48) : (s <= 864 && s % 24 == 0); }
  static constexpr bool lwait(int s) { return (s + 1) % (s < 468 && !ATTN ? WP1 : WP2) == 0; }
  static constexpr int bias_rd(int s) { return !ATTN && s < 432 ? (s % 36 == 28 ? 0 : s % 36 == 35 ? 1 : -1) : -1; }
  static constexpr bool g_set(int s) { return g_stage(s + 3) >= 0 && g_piece(s + 3) % 3 == 0; }
  static constexpr bool fixed(int s, int h) {
    if (bar(s) || g_set(s)) return true;
    return h == 0 ? (g_stage(s) >= 0 || h_load(s) >= 0 || o_load(s) >= 0 || x_load(s) >= 0 || bias_rd(s) >= 0) : lwait(s);
  }
  static constexpr int ST0 = 30, ST1 = 57;
  static constexpr int win0(int s) { return s < 864 ? s / 36 * 36 : 864 + (s - 864) / 10 * 10; }
  static constexpr int wlen(int s) { return s < 864 ? 36 : 10; }
  static constexpr int nop(int s) { return ATTN ? (s >= 36 ? 32 : 0) : s >= 36 && s < 468 ? 59 : s >= 874 ? 14 : 0; }
  static constexpr int epi_to(int s, int h) {
    int f = 0, u = 0;
    for (int q = 2 * win0(s) + 2; q < 2 * (win0(s) + wlen(s)); ++q) if (!fixed(q / 2, q % 2)) { ++f; if (q <= 2 * s + h) ++u; }
    return f >= nop(s) ? (u < nop(s) ? u : nop(s)) : u * nop(s) / f;
  }
  static constexpr int epi_from(int s, int h) { return 2 * s + h <= 2 * win0(s) + 2 ? 0 : h ? epi_to(s, 0) : epi_to(s - 1, 1); }
  static constexpr bool has_op(int s, int k) { return epi_from(s, 0) <= k && k < epi_to(s, 1); }
  static constexpr int stores(int s) { return ATTN || s < 0 || !nop(s) ? 0 : s < 864 ? has_op(s, ST0) + has_op(s, ST1) : has_op(s, 10) + has_op(s, 13); }
  static constexpr int vm(int s) {
    int c = (g_stage(s) >= 0) + (warm_k(s) >= 0) + stores(s);
    if (!ATTN && s >= 0) c += (h_load(s) >= 0) + (o_load(s) >= 0) + (s == POLL_HA) + (s == POLL_HB) + (s == POLL_O) + (x_load(s) >= 0);
    return c;
  }
  static constexpr int need(int s) {
    int r = -25;
    if (bar(s) && !(ATTN && s == 480)) r = g_last(s < P1 ? 3 * (s / 72) + (s % 72 == 0 ? 1 : s % 72 == BMID ? 2 : 3) : s < 864 ? s / 24 + 1 : 38);
    if (!ATTN) {
      const int e = s == PUB_HA || s == PUB_HB ? s - 1 : s == CHK_HA ? POLL_HA : s == CHK_HB ? POLL_HB : s == 646 ? 595 : s == CHK_O ? POLL_O : s == 720 ? 671 : s == 768 ? 719 : s == 863 ? 862 : -25;
      if (e > r) r = e;
    }
    return r;
  }
  static constexpr int allow(int s) { int c = 0; for (int k = need(s) + 1; k < s; ++k) c += vm(k); return c; }
  static constexpr bool ring_ok(int st) {
    int first = NSTEP, last = -1, refill = NSTEP;
    for (int s = 0; s < NSTEP; ++s) {
      if (a_stage(s) == st) { if (s - LA < first) first = s - LA; last = s; }
      if (ATTN && s >= 144 && s % 4 == 2 && 3 * (s / 72) + (s % 72) / 24 == st) { if (s - 8 < first) first = s - 8; if (s + 1 > last) last = s + 1; }
      if (g_stage(s) == st + 6 && s < refill) refill = s;
    }
    bool vis = st == 0, fre = refill == NSTEP;
    for (int b = 0; b < NSTEP; b += 2) if (bar(b)) { if (b < first && b + 72 > first && need(b) >= g_last(st)) vis = true; if (b >= last && b < refill) fre = true; }
    return vis && fre;
  }
};
template <bool ATTN, int... I> constexpr bool rings_ok(std::integer_sequence<int, I...>) { return (std::bool_constant<Sched<ATTN>::ring_ok(I)>::value && ...); }
static_assert(rings_ok<false>(std::make_integer_sequence<int, 39>{}) && rings_ok<true>(std::make_integer_sequence<int, 21>{}), "LDS ring protocol");
constexpr int kb_areg(int kb) { return kb == 0 ? A_H : kb == 1 ? A_H + 48 : kb == 2 ? A_H + 96 : kb == 3 ? A_H + 144 : kb == 4 ? A_H : kb == 5 ? A_H + 48 : A_H + 96; }
constexpr int qkv_pair(int kg, int p) {
  constexpr signed char T[4][28] = {
    {0,2,4,6, 16,8,10,-1, 18,20,22,24, 26,28,30,32, 12,14,34,52, 36,38,40,42, 44,46,48,50},
    {6,8,10,12, 0,2,4,-1, 24,26,28,30, 14,16,32,34, 18,20,22,50, 42,44,46,48, 36,38,40,52},
    {4,6,8,10, 12,14,16,-1, 22,24,26,28, 0,2,18,20, 30,32,34,36, 40,42,44,46, 48,50,52,38},
    {2,4,6,8, 0,10,12,-1, 20,22,24,26, 28,30,32,34, 14,16,18,36, 38,40,42,44, 46,48,50,52}};
  return T[kg][p];
}
struct QkvTab { unsigned v[4][16]; };
constexpr QkvTab make_tab() {
  QkvTab t = {};
  for (int kg = 0; kg < 4; ++kg)
    for (int p = 0; p < 28; ++p) {
      const int v = qkv_pair(kg, p), sv = v < 0 ? qkv_pair(kg, p - 1) : v;
      t.v[kg][p / 4] |= (unsigned)((sv / 18) * 72 + sv % 18) << (8 * (p % 4));
      t.v[kg][8 + p / 4] |= (unsigned)(sv >= 36 ? sv - 36 : sv) << (8 * (p % 4));
    }
  return t;
}

DI void check_flag(u64 f, u64 tag, unsigned voff, u32x4 srd, unsigned soff) {
  unsigned n;
  asm volatile("v_cmp_ne_u64_e32 vcc, %1, %2\n\ts_cbranch_vccz 1f\n\ts_mov_b32 %0, 0x40000\n2:\n\ts_sleep 1\n\t"
               "buffer_load_dwordx2 %1, %3, %4, %5 offen sc1\n\ts_sub_u32 %0, %0, 1\n\ts_waitcnt vmcnt(0)\n\ts_cbranch_scc1 1f\n\t"
               "v_cmp_ne_u64_e32 vcc, %1, %2\n\ts_cbranch_vccnz 2b\n1:"
               : "=&s"(n), "+v"(f) : "v"(tag), "v"(voff), "s"(srd), "s"(soff) : "vcc", "scc", "memory");
}
DI void spin_until(gu64* p, u64 tag) {
#pragma unroll 1
  for (unsigned spins = 0; spins < (1u << 18); ++spins) {
    const u64 f = __hip_atomic_load(p, RLX_AGENT);
    if (__all(f == tag)) break;
  }
}

struct Args { const void *x, *wqkv, *wo, *w1, *b1, *w2, *b2; void *out, *ws; QkvTab qt; };
static_assert(sizeof(Args) == 72 + 256, "no padding");
DI char* as_ptr(unsigned lo, unsigned hi) { return (char*)(((u64)hi << 32) | lo); }

__global__ __launch_bounds__(256, 1) void main_k(const Args) {
  extern __shared__ __attribute__((aligned(16))) char smem[];
  LAS char* lds = (LAS char*)smem;
  const int tid = threadIdx.x, lane = tid & 63, bid = blockIdx.x;
  const int kg = map_kg(bid), cg = map_role(bid), rt = map_rt(bid);
  u64 tent, tA = 0; asm volatile("s_memrealtime %0" : "=s"(tent));
#define PT(i) do { if (POINT == i) asm volatile("s_memrealtime %0\n\ts_waitcnt lgkmcnt(0)" : "=s"(tA) :: "memory"); } while (0)
  u32x16 ka, kt; u32x2 kw;
  asm volatile("s_load_dwordx16 %0, %3, 0x0\n\ts_load_dwordx2 %1, %3, 0x40\n\ts_load_dwordx16 %2, %3, %4 offset:0x48"
               : "=&s"(ka), "=&s"(kw), "=&s"(kt) : "s"(__builtin_amdgcn_kernarg_segment_ptr()), "s"(kg * 64));
  const void* code;
  unsigned code_bytes;
  asm volatile("s_getpc_b64 %0\n.Lk_pc:\n\ts_mov_b32 %1, .Lk_end0-.Lk_pc\n\ts_max_i32 %1, %1, .Lk_end1-.Lk_pc" : "=s"(code), "=s"(code_bytes) :: "scc", "a255");
  const int w = __builtin_amdgcn_readfirstlane(tid >> 6);
  const bool attn = cg == 3;
  const int tokbase = 256 * (rt >> 1) + (rt & 1);
  const int n16 = lane & 15, r8 = lane >> 3, pos = lane & 7;
  const int G = lane >> 4, q4 = (lane >> 2) & 3, p4 = lane & 3;

  const unsigned rot = (0x24924924u >> (2 * ((rt & 7) + cg))) & 3;
  unsigned kro[3] = {rot * 192, rot == 2 ? 0 : rot * 192 + 192, rot == 0 ? 384 : rot * 192 - 192};
  unsigned m6 = 6 * w * 1024, sx = tokbase * 1152 + w * 32 * 2304;
  unsigned vx0 = r8 * 2304 + ((pos ^ G) << 4), vx1 = vx0 ^ 64;
  PINS(m6); PINS(sx); PIN(vx0); PIN(vx1); PINS(kro[0]); PINS(kro[1]); PINS(kro[2]);
  const unsigned swz = pos ^ (((r8 >> 1) & 3) << 1);
  const int fs = 3 * kg + cg, f0 = fs * 384;
  unsigned strideA = attn ? 3456u : 9216u;
  PINS(strideA);
  unsigned soA[3];
#pragma unroll
  for (int j = 0; j < 3; ++j) { soA[j] = (kro[j] + 48 * w) * strideA + (attn ? 288 * kg : f0 * 2); PINS(soA[j]); }
  unsigned vab = r8 * strideA + (swz & 1) * 16, vsh = 8 * (swz >> 1), vcode = tid < 64 ? tid * 128 : OOB;
  PIN(vab); PIN(vsh); PIN(vcode);
  u32x4 id0, id1;
  {
    const unsigned one = 0x3f80u << (16 * (n16 & 1));
    const int e = (n16 >> 1) - 4 * G;
#pragma unroll
    for (int j = 0; j < 4; ++j) { id0[j] = e == j ? one : 0u; id1[j] = e + 8 == j ? one : 0u; }
  }
  PIN(id0); PIN(id1);
  unsigned po0 = (4 * G + q4) * 128 + (((2 * G + (q4 >> 1)) & 3) << 5) + p4 * 8;
  LAS char* rb[4][3];
#pragma unroll
  for (int i = 0; i < 3; ++i)
#pragma unroll
    for (int c = 0; c < 4; ++c) { rb[c][i] = lds + (po0 ^ (c << 5)) + 2 * i * SLOT; PIN(rb[c][i]); }
  LAS char* xp[2][2];
#pragma unroll
  for (int u = 0; u < 2; ++u) {
    const unsigned xr0 = w * 6144 + (n16 >> 3) * 1024 + (n16 & 7) * 128 + (((G ^ (n16 >> 1)) << 4) ^ (u << 6));
    xp[0][u] = lds + xr0 + 4 * SLOT; xp[1][u] = lds + xr0 + 2 * SLOT; PIN(xp[0][u]); PIN(xp[1][u]);
  }
  unsigned ones = 0x3f803f80u; PIN(ones);
  const int c1 = cg == 2 ? 0 : cg + 1, c2 = cg == 0 ? 2 : cg - 1;
  unsigned soB[3];
  soB[0] = (f0 + 48 * w) * 1152 + 384 * cg; soB[1] = ((3 * kg + c1) * 384 + 48 * w) * 1152 + 384 * cg; soB[2] = ((3 * kg + c2) * 384 + 48 * w) * 1152 + 384 * cg;
  const int p5 = w == 3 ? 13 : 5 * w;
  unsigned soO = (144 * kg + 8 * p5) * 1152 + 384 * cg, m5 = p5 * 1024;
  unsigned hso[3];
  hso[0] = H_OFF + ((rt * 12 + fs) * 4 + w) * 24576; hso[1] = H_OFF + ((rt * 12 + 3 * kg + c1) * 4 + w) * 24576; hso[2] = H_OFF + ((rt * 12 + 3 * kg + c2) * 4 + w) * 24576;
  unsigned oso = O_OFF + ((rt * 4 + kg) * 4 + w) * 9216;
  unsigned pso = P_OFF + (((rt * 3 + cg) * 4 + kg) * 4 + w) * 12288;
  unsigned os[3];
#pragma unroll
  for (int k = 0; k < 3; ++k) { const int un = 3 * kg + k; os[k] = (tokbase + 64 * w + 32 * (un & 1)) * 1152 + (192 * cg + 32 * (un >> 1)) * 2; }
#pragma unroll
  for (int k = 0; k < 3; ++k) { PINS(soB[k]); PINS(hso[k]); PINS(os[k]); }
  PINS(pso); PINS(oso); PINS(soO); PINS(m5);
  asm volatile("s_waitcnt lgkmcnt(0)" : "+s"(ka), "+s"(kw), "+s"(kt), "+s"(tent));
  PT(0);
  const char *x = as_ptr(ka[0], ka[1]), *wo = as_ptr(ka[4], ka[5]), *b1 = as_ptr(ka[8], ka[9]), *w2 = as_ptr(ka[10], ka[11]), *b2 = as_ptr(ka[12], ka[13]);
  char *out = as_ptr(ka[14], ka[15]), *ws = as_ptr(kw[0], kw[1]);
  const u32x4 srdx = make_srd(x, S_ * D_ * 2);
  auto x_more = [&](auto T) { const unsigned b = sx + kro[T / 3] * 2; x_line<T>(m6, vx0, vx1, srdx, b, b + 8 * 2304, b + 16 * 2304, b + 24 * 2304); };
  sfor<3>(x_more);
  const unsigned vsel = 4 * w + (attn ? 16 : 0);
  visit<0, Sched<false>::NMARK, Sched<true>::NMARK>(vsel);
  u32x4 srdA = make_srd(attn ? as_ptr(ka[2], ka[3]) : as_ptr(ka[6], ka[7]), attn ? D_ * 3 * D_ * 2 : D_ * F_ * 2);
  PINS(srdA);
  unsigned qs[7], qd[7];
#pragma unroll
  for (int i = 0; i < 7; ++i) { qs[i] = kt[i]; qd[i] = kt[8 + i]; }
  auto va_of = [&](unsigned word) { return vab + ((word >> vsh) & 255) * 16; };
  unsigned va = va_of(attn ? qs[0] : 0x06040200u), so = soA[0] - 8 * strideA;
  auto w_piece = [&](auto K) {
    constexpr int n = K / 6, p = K % 6;
    if constexpr (p == 0 && n > 0) so = soA[n % 3] - 8 * strideA;
    if constexpr (K == 18) va = va_of(attn ? qs[1] : 0x0e0c0a08u);
    glds16r<n * SLOT + p * 1024>(m6, va, srdA, so, 8 * strideA);
  };
  sfor<6>(w_piece);
  warm(vcode, make_srd(code, code_bytes), ((bid >> 3) & 15) * 8192);
  visit<1, Sched<false>::NMARK, Sched<true>::NMARK>(vsel);
  const u32x4 srdws = make_srd(ws, WS_BYTES);
  const unsigned flo = FLAG_OFF + rt * FL_N * 128;
  auto misc_loads = [&]() {
    asm volatile("buffer_load_dwordx2 a[12:13], %0, %1, %2 offen sc1" :: "v"(0), "s"(srdws), "s"(flo + FL_EPOCH * 128) : "memory");
    if (attn) {
      int ht = tokbase + 2 * (r8 - 8); ht = ht < 0 ? 0 : ht;
      const unsigned vh = ht * 1152 + pos * 16;
      const unsigned kb = ((0x24924924u >> (2 * (rot + w))) & 3) * 384 | (w == 3 ? OOB : 0);
      glds16c<TAB_OFF, 0>(w * 3072, vh, srdx, kb); glds16c<TAB_OFF + 1024, 128>(w * 3072, vh, srdx, kb); glds16c<TAB_OFF + 2048, 256>(w * 3072, vh, srdx, kb);
    } else {
      asm volatile("buffer_load_dword a14, %0, %1, 0 offen" :: "v"(tid * 4), "s"(make_srd(b1 + f0 * 2, 768)) : "memory");
      asm volatile("buffer_load_dword a15, %0, %1, 0 offen" :: "v"(tid * 4), "s"(make_srd(b2 + 192 * cg * 2, 384)) : "memory");
    }
  };
  u32x4 xf[36];
  f32x4 nm[2];
  float rstd[2];
  {
    float sm[2] = {0.f, 0.f}, sq[2] = {0.f, 0.f};
    SB();
    sfor<10>([&](auto T) {
      constexpr int t = T;
      if constexpr (t == 0) x_more(IC(3));
      if constexpr (t + 4 < 9) x_more(IC(t + 4));
      if constexpr (t == 4) misc_loads();
      if constexpr (t == 0) visit<2, Sched<false>::NMARK, Sched<true>::NMARK>(vsel);
      if constexpr (t < 9) {
        waitv<4 * ((t + 4 > 8 ? 8 : t + 4) - t) + (t < 3 ? 6 : 0)>();
#pragma unroll
        for (int u = 0; u < 4; ++u) xf[4 * t + u] = *(LAS u32x4*)(xp[t / 3 == 1][u >> 1] + (t % 3 == 1 ? 4096 + (u & 1) * SLOT : (t % 3 == 2 ? SLOT : 0) + (u & 1) * 2048));
      }
      SB();
      if constexpr (t > 0) {
#pragma unroll
        for (int u = 0; u < 4; u += 2)
#pragma unroll
          for (int j = 0; j < 4; ++j) {
            const unsigned v0 = xf[4 * t - 4 + u][j], v1 = xf[4 * t - 3 + u][j];
            sm[0] = dot2(v0, ones, sm[0]); sm[1] = dot2(v1, ones, sm[1]); sq[0] = dot2(v0, v0, sq[0]); sq[1] = dot2(v1, v1, sq[1]);
          }
        PIN(sm[0]); PIN(sm[1]); PIN(sq[0]); PIN(sq[1]);
      }
      if constexpr (t == 8) sfor<6>([&](auto P) { w_piece(IC(6 + P)); });
      SB();
    });
#pragma unroll
    for (int tb = 0; tb < 2; ++tb) {
      const float negmu = xor32(xor16(sm[tb])) * (-1.0f / D_);
      nm[tb] = f32x4{negmu, negmu, negmu, negmu};
      rstd[tb] = __builtin_amdgcn_rsqf(fmaxf(xor32(xor16(sq[tb])) * (1.0f / D_) - negmu * negmu, 0.f) + 1e-5f);
    }
  }
  PT(1);
  bf16x4 wl[NB], wu[NB];
  {
    f32x4 dl[3], dh[3];
    asm volatile("s_nop 1" : "+v"(nm[0]), "+v"(nm[1]));
    sfor<38>([&](auto B) {
      constexpr int b = B;
      if constexpr (b < 36) mfma_vvc(dl[b % 3], id0, xf[b], nm[b & 1]);
      if constexpr (b == 36) {
        asm volatile("s_waitcnt vmcnt(18)\n\ts_barrier" : "+v"(dl[1]), "+v"(dh[1]), "+v"(dl[2]), "+v"(dh[2]) : "v"(nm[0]), "v"(nm[1]) : "memory");
#pragma unroll
        for (int t = 0; t < LA; ++t) { wl[t] = rdhalf(rb[t & 1][0] + (t >> 1) * 4096); wu[t] = rdhalf(rb[t & 1][0] + (t >> 1) * 4096 + 2048); }
      }
      SB();
      if constexpr (b >= 2) { xf[b - 2][0] = cvtpk(dl[(b - 2) % 3][0], dl[(b - 2) % 3][1]); xf[b - 2][1] = cvtpk(dl[(b - 2) % 3][2], dl[(b - 2) % 3][3]); }
      SB();
      if constexpr (b < 36) mfma_vvc(dh[b % 3], id1, xf[b], nm[b & 1]);
      SB();
      if constexpr (b >= 2) { xf[b - 2][2] = cvtpk(dh[(b - 2) % 3][0], dh[(b - 2) % 3][1]); xf[b - 2][3] = cvtpk(dh[(b - 2) % 3][2], dh[(b - 2) % 3][3]); PIN(xf[b - 2]); }
      if constexpr (b % 2 == 0 && b < 24) w_piece(IC(12 + b / 2));
      SB();
    });
  }

  PT(2);
  const u32x4 srdW2 = make_srd(w2, F_ * D_ * 2), srdWo = make_srd(wo, D_ * D_ * 2);
  const __amdgpu_buffer_rsrc_t rws = __builtin_amdgcn_make_buffer_rsrc(ws, 0, WS_BYTES, 0x00020000);
  gu64* flags = (gu64*)((char*)ws + flo);
  auto get_tag = [&]() __attribute__((always_inline)) { return (((u64)__float_as_uint(aread<13>()) << 32) | __float_as_uint(aread<12>())) * 0x9E3779B97F4A7C15ull + 0x632BE59BD9B4E019ull; };
  float t0, t1;
  f32x4 acc[2][2][2];
#define LANE_VALUES int tid_ = threadIdx.x; PIN(tid_); \
  const int tid = tid_, lane = tid & 63, n16 = lane & 15, r8 = lane >> 3, pos = lane & 7, G = lane >> 4, q4 = (lane >> 2) & 3, p4 = lane & 3; \
  const unsigned swz = pos ^ (((r8 >> 1) & 3) << 1), l16 = lane * 16, vflag = tid == 0 ? 0u : OOB, wa = rt * 72 + w * 18 + lane, wm = lane < 18 ? 0 : OOB; \
  (void)q4; (void)p4;

  if (!attn) {
    using SC = Sched<false>;
    LANE_VALUES
    constexpr bool attn_role = false;
    auto rd_a = [&](auto Sc, auto U) {
      constexpr int s = Sc, u = U;
      if constexpr (s < SC::NSTEP && !SC::skip(s) && !(u == 1 && s >= 864 && SC::a_ks(s) >= 8)) {
        constexpr int sl = SC::a_stage(s) % 6, ks = SC::a_ks(s);
        (u ? wu : wl)[s % NB] = rdhalf(rb[2 * SC::a_tile(s) + (ks & 1)][sl >> 1] + (sl & 1) * SLOT + (ks >> 1) * 4096 + u * 2048);
      }
    };
    auto mma1 = [&](auto Sc, auto H) {
      constexpr int s = Sc, k = s % 36, o = k & 1, tb = o ^ H;
      if constexpr (!SC::skip(s)) mfma_vv<k < 2>(acc[SC::a_tile(s)][o][tb], cat(wl[s % NB], wu[s % NB]), xf[(k & ~1) + tb]);
    };
    auto sync = [&](auto Sc, auto H) {
      constexpr int s = Sc, h = H;
      if constexpr (h == 0 && SC::need(s) > -25) waitv<SC::allow(s)>();
      if constexpr (h == 1 && (s == BMID || (attn_role && s == 480))) WAITL0();
      if constexpr (h == 1 && SC::bar(s)) BAR();
      if constexpr (h == 1 && SC::lwait(s)) __builtin_amdgcn_s_waitcnt(0xC07F | ((2 * (LA - (s < 468 && !attn_role ? WP1 : WP2))) << 8));
    };
    unsigned vA[3] = {va - 128, va - 128 + 8 * 9216 - 1024, va - 128 + 2 * (8 * 9216 - 1024)}, vB[3], lk[3], gso = sadd<128>(soA[1]), hst = hso[0];
    set_m0<4 * SLOT>(m6);
    u64 tag;
    unsigned vpoll, pso_;
    LAS char* tab = lds + TAB_OFF + G * 16;
    f32x4 bs[2], bn;
    f32x4 ac[6][2][2];
    unsigned e[8];
    u64 pollv = 0;
    const unsigned ov = n16 * 2304 + (G & 1) * 32 + (G >> 1) * 16;
    u32x4 xr[3];
    float u0[2], u1[2];
    auto op1 = [&](auto Tc, auto K) {
      constexpr int T = Tc, k = K;
      if constexpr (k == SC::ST0 || k == SC::ST1) { constexpr int q = k == SC::ST1; st16_sc1<(T & 1) * 2048 + q * 1024>(u32x4{e[4 * q], e[4 * q + 1], e[4 * q + 2], e[4 * q + 3]}, l16, srdws, hst); }
      else if constexpr (k == 58) { if constexpr (T & 1) hst = sadd<4096>(hst); }
      else {
        constexpr int kk = k > SC::ST0 ? k - 1 : k, r = (kk - 4) % 7;
        constexpr int j = kk < 4 ? kk : kk >= 53 ? kk - 49 : r == 0 ? 4 : r == 1 ? 0 : r == 2 ? 5 : r == 3 ? 1 : r == 4 ? 2 : r == 5 ? 3 : 6;
        constexpr int d = kk < 4 ? 0 : kk >= 53 ? 7 : (kk - 4) / 7 + (j < 4), hf = d & 1;
        const f32x4& a = acc[T & 1][(d >> 1) & 1][d >> 2];
        if constexpr (j == 0) u0[hf] = i_fma(a[2 * hf], rstd[d >> 2], bs[(d >> 1) & 1][2 * hf]);
        if constexpr (j == 1) u1[hf] = i_fma(a[2 * hf + 1], rstd[d >> 2], bs[(d >> 1) & 1][2 * hf + 1]);
        if constexpr (j == 2) u0[hf] = i_sqs(u0[hf]);
        if constexpr (j == 3) u1[hf] = i_sqs(u1[hf]);
        if constexpr (j == 4) e[d] = i_cvt(u0[hf], u1[hf]);
        if constexpr (j == 5) e[d] = i_relu(e[d]);
        if constexpr (j == 6) awrite<A_H + 8 * T + d>(e[d]);
      }
    };
    auto op2 = [&](auto Tc, auto K) {
      constexpr int T = Tc, k = K;
      if constexpr (k < 8) e[k] = i_cvt(ac[T][(k >> 1) & 1][k >> 2][2 * (k & 1)], ac[T][(k >> 1) & 1][k >> 2][2 * (k & 1) + 1]);
      else {
        constexpr int q = (k - 8) / 3, j = (k - 8) % 3;
        if constexpr (j < 2) i_swap16(e[4 * q + j], e[4 * q + 2 + j]);
        else st16_sc1<(T & 1) * 2048 + q * 1024>(u32x4{e[4 * q], e[4 * q + 1], e[4 * q + 2], e[4 * q + 3]}, lk[T >> 1], srdws, pso);
      }
    };
    auto slot = [&](auto Sc, auto H) {
      constexpr int s = Sc, h = H;
      sync(Sc, H);
      if constexpr (SC::g_set(s)) {
        constexpr int np = SC::g_stage(s + 3), p = SC::g_piece(s + 3), kb = (np - 18) / 3, src = kb < 2 ? 0 : 1 + (kb & 1), half = kb < 2 ? kb : (kb >= 4);
        if constexpr (h == 0) set_m0<(np % 6) * SLOT + p * 1024>(np < 36 ? m6 : m5);
        else if constexpr (np < 18) gso = sadd<8 * p * 9216 + 128 * (np / 3)>(soA[np % 3]);
        else if constexpr (np < 36) gso = sadd<(192 * half + 8 * p) * 1152 + 128 * ((np - 18) % 3)>(soB[src]);
        else gso = sadd<8 * p * 1152 + 128 * (np - 36)>(soO);
      }
      if constexpr (h == 0) {
        if constexpr (SC::g_stage(s) >= 0) { constexpr int np = SC::g_stage(s), j = SC::g_piece(s) % 3; glds3<j>(np < 18 ? vA[j] : vB[j], np < 18 ? srdA : np < 36 ? srdW2 : srdWo, gso); }
        if constexpr (SC::bias_rd(s) == 0) bn = *(LAS f32x4*)(tab + (s / 36) * 128);
        if constexpr (SC::bias_rd(s) == 1) { bs[0] = bn; bs[1] = *(LAS f32x4*)(tab + (s / 36) * 128 + 64); }
        if constexpr (SC::x_load(s) >= 0) { constexpr int k = SC::x_load(s); xr[k] = bload16(ov, srdx, os[k]); }
        if constexpr (SC::o_load(s) >= 0) { constexpr int f = SC::o_load(s); aload16_sc1<kb_areg(6) + 4 * f, (f & 3) * 1024>(lk[f >> 2], srdws, oso); }
        if constexpr (SC::h_load(s) >= 0) { constexpr int b = SC::h_load(s) / 12, f = SC::h_load(s) % 12; aload16_sc1<kb_areg(2 + b) + 4 * f, (f & 3) * 1024>(lk[f >> 2], srdws, hso[1 + (b & 1)]); }
      }
      if constexpr (SC::nop(s) > 0) {
        static_assert(SC::bias_rd(s) != 1 || s < 36 || SC::epi_from(s, 0) > 52, "the bias registers are free");
        constexpr int k0 = SC::epi_from(s, h), k1 = SC::epi_to(s, h);
        sfor<k1 - k0>([&](auto I) { if constexpr (s < 864) op1(IC(s / 36 - 1), IC(k0 + I)); else op2(IC((s - 864) / 10 - 1), IC(k0 + I)); if constexpr (s < 864 && k0 + I == SC::ST0 && k0 + I + 1 < k1) asm volatile("s_nop 1"); });
      }
    };
    sfor<77>([&](auto So) { sfor<12>([&](auto Si) {
      constexpr int s = 12 * So + Si;
      sfor<2>([&](auto H) {
        if constexpr (s < 432) mma1(IC(s), H);
        else {
          constexpr int n = SC::a_stage(s) - 18, kb = n / 3, T = 2 * (n % 3) + SC::a_tile(s), ks = SC::a_ks(s), o = ks & 1, tb = o ^ H, ar = kb_areg(kb) + 8 * (ks >> 1);
          if constexpr (kb == 6 && ks >= 8) mfma_k16<ar + 2 * tb>(ac[T][o][tb], wl[s % NB]);
          else mfma_va<kb == 0 && ks < 2, ar + 4 * tb>(ac[T][o][tb], cat(wl[s % NB], wu[s % NB]));
        }
        SB(); rd_a(IC(s + LA), H); SB();
        slot(IC(s), H); SB();
      });
      if constexpr (s == 216) PT(3);
      if constexpr (s == 432) PT(4);
      constexpr int k = SC::warm_k(s);
      if constexpr (SC::mark_k(s) >= 0) mark<0, SC::mark_k(s)>();
      if constexpr (k >= 0 && k < 2) warm(((wa >> 1) * 9216 + (wa & 1) * 128) | wm, srdA, f0 * 2 + 256 + 256 * k);
      if constexpr (k >= 2) warm((wa * 128) | wm, srdW2, f0 * 1152 + (k - 2) * 147456);
      if constexpr (s == SC::PUB_HA || s == SC::PUB_HB) bstore8_sc1(tag, vflag, srdws, flo + ((s == SC::PUB_HA ? FL_HA : FL_HB) + fs) * 128);
      if constexpr (s == SC::CHK_HA || s == SC::CHK_HB || s == SC::CHK_O) check_flag(pollv, tag, vpoll, srdws, pso_);
      if constexpr (s == 4) { const unsigned r = __float_as_uint(aread<14>()); *(LAS f32x2*)(lds + TAB_OFF + tid * 8) = f32x2{bflo(r), bfhi(r)}; }
      if constexpr (s == 8) { const unsigned r = __float_as_uint(aread<15>()); *(LAS f32x2*)(lds + TAB_OFF + 2048 + tid * 8) = f32x2{bflo(r), bfhi(r)}; }
      if constexpr (s == 12) { tag = get_tag(); PIN(tag); }
      if constexpr (s == 334) { vB[0] = r8 * 1152 + swz * 16; vB[1] = vB[0] + 8192; vB[2] = vB[0] + 16384; PIN(vB[0]); PIN(vB[1]); PIN(vB[2]); }
      if constexpr (s == 386) { vpoll = (3 * kg + ((lane & 1) ? c1 : c2)) * 128; PIN(vpoll); }
      if constexpr (s == 432) { lk[0] = l16; lk[1] = l16 + 4096; lk[2] = l16 + 8192; PIN(lk[1]); PIN(lk[2]); }
      if constexpr (s == 600) { hso[1] += 12288; hso[2] += 12288; PINS(hso[1]); PINS(hso[2]); }
      if constexpr (s == SC::POLL_HA || s == SC::POLL_HB || s == SC::POLL_O) {
        if constexpr (s == SC::POLL_O) vpoll = kg * 128;
        pso_ = flo + (s == SC::POLL_HA ? FL_HA : s == SC::POLL_HB ? FL_HB : FL_O) * 128;
        pollv = bload8_sc1(vpoll, srdws, pso_);
      }
      SB();
    }); });
    asm volatile("s_nop 7" : "+v"(ac[5][0][0]), "+v"(ac[5][0][1]), "+v"(ac[5][1][0]), "+v"(ac[5][1][1]));
    sfor<14>([&](auto K) { op2(IC(5), K); SB(); });
    asm volatile("s_nop 1" : "+v"(xr[0]), "+v"(xr[1]), "+v"(xr[2]));
    SB();
    PT(5);
    float a[3][8];
    f32x4 cb[3][2];
#pragma unroll
    for (int k = 0; k < 3; ++k) {
      LAS char* bp = lds + TAB_OFF + 2048 + 128 * ((3 * kg + k) >> 1) + (G & 1) * 64 + (G >> 1) * 32;
      cb[k][0] = *(LAS f32x4*)bp; cb[k][1] = *(LAS f32x4*)(bp + 16);
    }
    gu64* fp = flags + (FL_FIN + 3 * (lane & 3) + cg) * 16;
    PIN(fp);
    SB();
#pragma unroll
    for (int k = 0; k < 3; ++k) {
      const f32x4 c0 = cb[k][0], c1 = cb[k][1];
#pragma unroll
      for (int j = 0; j < 4; ++j) { a[k][2 * j] = bflo(xr[k][j]) + (j < 2 ? c0 : c1)[(2 * j) & 3]; a[k][2 * j + 1] = bfhi(xr[k][j]) + (j < 2 ? c0 : c1)[(2 * j + 1) & 3]; PIN(a[k][2 * j]); PIN(a[k][2 * j + 1]); }
    }
    unsigned sel0 = 0x00003f80u, sel1 = 0x3f800000u, pb = P_OFF + ((rt * 3 + cg) * 16 + w) * 12288 + 3 * kg * 1024;
    PIN(sel0); PIN(sel1); PINS(pb);
    WAITV(0); BAR();
    bstore8_sc1(tag, vflag, srdws, flo + (FL_FIN + fs) * 128);
    spin_until(fp, tag);
    const __amdgpu_buffer_rsrc_t rout = __builtin_amdgcn_make_buffer_rsrc(out, 0, S_ * D_ * 2, 0x00020000);
    u32x4 v[3][4];
#pragma unroll
    for (int k = 0; k < 3; ++k)
#pragma unroll
      for (int s = 0; s < 4; ++s) v[k][s] = __builtin_amdgcn_raw_buffer_load_b128(rws, l16 + k * 1024, pb + s * 4 * 12288, 16);
#pragma unroll
    for (int k = 0; k < 3; ++k) {
#pragma unroll
      for (int s = 0; s < 4; ++s)
#pragma unroll
        for (int j = 0; j < 4; ++j) { a[k][2 * j] = dot2(v[k][s][j], sel0, a[k][2 * j]); a[k][2 * j + 1] = dot2(v[k][s][j], sel1, a[k][2 * j + 1]); }
      __builtin_amdgcn_raw_buffer_store_b128(u32x4{cvtpk(a[k][0], a[k][1]), cvtpk(a[k][2], a[k][3]), cvtpk(a[k][4], a[k][5]), cvtpk(a[k][6], a[k][7])}, rout, ov, os[k], 0);
    }
    if (fs == 0) bstore8_sc1(tag, vflag, srdws, flo + FL_EPOCH * 128);
    { u64 tn; do { asm volatile("s_memrealtime %0\n\ts_waitcnt lgkmcnt(0)" : "=s"(tn)); } while (tn - tent < 4000 + (tA - tent)); }
    asm volatile(".Lk_end0:");
    __builtin_amdgcn_endpgm();
  }
  {
    using SC = Sched<true>;
    LANE_VALUES
    auto va_of = [&](unsigned word) { return r8 * 3456 + (swz & 1) * 16 + ((word >> (8 * (swz >> 1))) & 255) * 16; };
    constexpr bool attn_role = true;
    auto rd_a = [&](auto Sc, auto U) {
      constexpr int s = Sc, u = U;
      if constexpr (s < SC::NSTEP && !SC::skip(s) && !(u == 1 && s >= 864 && SC::a_ks(s) >= 8)) {
        constexpr int sl = SC::a_stage(s) % 6, ks = SC::a_ks(s);
        (u ? wu : wl)[s % NB] = rdhalf(rb[2 * SC::a_tile(s) + (ks & 1)][sl >> 1] + (sl & 1) * SLOT + (ks >> 1) * 4096 + u * 2048);
      }
    };
    auto mma1 = [&](auto Sc, auto H) {
      constexpr int s = Sc, k = s % 36, o = k & 1, tb = o ^ H;
      if constexpr (!SC::skip(s)) mfma_vv<k < 2>(acc[SC::a_tile(s)][o][tb], cat(wl[s % NB], wu[s % NB]), xf[(k & ~1) + tb]);
    };
    auto sync = [&](auto Sc, auto H) {
      constexpr int s = Sc, h = H;
      if constexpr (h == 0 && SC::need(s) > -25) waitv<SC::allow(s)>();
      if constexpr (h == 1 && (s == BMID || (attn_role && s == 480))) WAITL0();
      if constexpr (h == 1 && SC::bar(s)) BAR();
      if constexpr (h == 1 && SC::lwait(s)) __builtin_amdgcn_s_waitcnt(0xC07F | ((2 * (LA - (s < 468 && !attn_role ? WP1 : WP2))) << 8));
    };
    unsigned vA[3] = {va, va + 8 * 3456 - 1024, va + 2 * (8 * 3456 - 1024)}, gso = soA[1];
    set_m0<4 * SLOT>(m6);
    f32x4 gram;
    float rstdh, negmuh;
    const unsigned hrow = n16 < 8 ? n16 : 0;
    auto halo_val = [&](auto I) {
      f32x4 r;
      sfor<4>([&](auto K) { const float a = aread<A_HH + 4 * (I - 2) + K>(); r[(int)K] = fmaf(ror8(a), negmuh, a) * rstdh; });
      return r;
    };
    auto halo_col = [&](auto I) { return ((qd[I] >> (8 * w)) & 255); };
    LAS char* hb[3];
#pragma unroll
    for (int i = 0; i < 3; ++i) { hb[i] = (LAS char*)(uintptr_t)((unsigned)(uintptr_t)rb[0][i] ^ (w << 5)); PIN(hb[i]); }
    const unsigned ha = (unsigned)(uintptr_t)lds + TAB_OFF + hrow * 128 + G * 8;
    const unsigned qrow = (unsigned)(uintptr_t)lds + QK_OFF + (32 * w + n16 + 8) * QK_RS + G * 8, vrow = (unsigned)(uintptr_t)lds + (32 * w + n16 + 8) * V_RS + G * 16;
    bf16x4 hl[2], hu[2];
    auto opa = [&](auto Tc, auto K) {
      constexpr int T = Tc, d = K / 4, j = K % 4, p = 2 * T + (d >> 2), tb = (d >> 1) & 1, hf = d & 1;
      if constexpr (p != 7) {
        const f32x4& a = acc[T & 1][d >> 2][tb];
        if constexpr (j == 0) t0 = i_mul(a[2 * hf], rstd[tb]);
        if constexpr (j == 1) t1 = i_mul(a[2 * hf + 1], rstd[tb]);
        if constexpr (p < 19) {
          if constexpr (j == 2) t0 = __uint_as_float(i_cvt(t0, t1));
          if constexpr (j == 3) awrite<A_Q + 4 * p + 2 * tb + hf>(__float_as_uint(t0));
        } else if constexpr (j >= 2) awrite<A_V + 8 * (p - 19) + 4 * tb + 2 * hf + j - 2>(__float_as_uint(j == 2 ? t0 : t1));
      }
    };
    sfor<28>([&](auto So) { sfor<18>([&](auto Si) {
      constexpr int s = 18 * So + Si, i = s / 72, u = s % 72;
      sfor<2>([&](auto H) {
        constexpr int h = H;
        mma1(IC(s), H);
        SB(); rd_a(IC(s + LA), H); SB();
        sync(IC(s), H);
        if constexpr (SC::g_set(s)) {
          constexpr int np = SC::g_stage(s + 3), p = SC::g_piece(s + 3);
          if constexpr (h == 0) set_m0<(np % 6) * SLOT + p * 1024>(m6); else gso = sadd<8 * p * 3456>(soA[np % 3]);
        }
        if constexpr (h == 0 && SC::g_stage(s) >= 0) glds3<SC::g_piece(s) % 3>(vA[SC::g_piece(s) % 3], srdA, gso);
        if constexpr (SC::nop(s) > 0) { constexpr int k0 = SC::epi_from(s, h), k1 = SC::epi_to(s, h); sfor<k1 - k0>([&](auto I) { opa(IC(s / 36 - 1), IC(k0 + I)); }); }
        SB();
      });
      if constexpr (u % 4 == 2) {
        constexpr int sg = u / 4;
        if constexpr (s >= 46 && s <= 114) mfma16_aa<s == 46, A_HX + s - 46>(gram);
        if constexpr (i >= 2) mfma16_ava<sg == 0, A_HH + 4 * (i - 2), A_HX + 4 * sg>(cat(hl[sg & 1], hu[sg & 1]));
        SB();
        if constexpr (s >= 22 && s < 94) { constexpr int f = (s - 22) / 4; ds_read_a64<A_HX + 4 * f, (f >> 1) * 1024 + (f & 1) * 64>(ha); ds_read_a64<A_HX + 4 * f + 2, (f >> 1) * 1024 + (f & 1) * 64 + 32>(ha); }
        constexpr int s2 = s + 8, sl = (3 * (s2 / 72) + (s2 % 72) / 24) % 6;
        if constexpr (s2 >= 144 && s2 < 504) { LAS char* p = hb[sl >> 1] + (sl & 1) * SLOT + (((s2 % 72) / 4) % 6) * 4096; hl[sg & 1] = rdhalf(p); hu[sg & 1] = rdhalf(p + 2048); }
        if constexpr (s == 122) {
          const float dg = (p4 == 0) ? gram[0] : (p4 == 1) ? gram[1] : (p4 == 2) ? gram[2] : gram[3];
          rstdh = G == q4 ? dg : 0.f; negmuh = G == 2 ? gram[0] : 0.f;
          PIN(rstdh); PIN(negmuh);
        }
        if constexpr (s == 126) { rstdh = xor16(rstdh); negmuh = xor16(negmuh); PIN(rstdh); PIN(negmuh); }
        if constexpr (s == 130) { rstdh = xor32(rstdh); negmuh = xor32(negmuh); PIN(rstdh); PIN(negmuh); }
        if constexpr (s == 134) {
          negmuh *= -1.0f / D_;
          rstdh = __builtin_amdgcn_rsqf(fmaxf(rstdh * (1.0f / D_) - negmuh * negmuh, 0.f) + 1e-5f);
          PIN(rstdh); PIN(negmuh);
        }
      }
      constexpr int k = SC::warm_k(s);
      if constexpr (SC::mark_k(s) >= 0) mark<1, SC::mark_k(s)>();
      if constexpr (k >= 0 && k < 4) {
        const unsigned la = (288 * kg + ((qs[k < 3 ? 2 + k : 5] >> (k == 2 ? 16 : 0)) & 255) * 16) & ~127u, lb = (288 * kg + (k < 3 ? qs[2 + k] >> 24 : qs[6] & 255) * 16) & ~127u;
        warm(((wa >> 1) * 3456 + ((wa & 1) ? lb : la)) | wm, srdA, 0);
      }
      if constexpr (k == 4) warm(((rt * 81 + w * 21 + lane) * 128) | (lane < 21 ? 0 : OOB), srdWo, 144 * kg * 1152);
      if constexpr (s == 2) *(LAS u32x4*)(lds + TAB_OFF + w * 3072 + (r8 < 3 ? r8 : 0) * 1024 + pos * 16) = u32x4{0x3f803f80u, 0x3f803f80u, 0x3f803f80u, 0x3f803f80u};
      if constexpr (u == 47 && i < 5) { vA[0] = va_of(qs[i + 2]); vA[1] = vA[0] + 8 * 3456 - 1024; vA[2] = vA[0] + 2 * (8 * 3456 - 1024); PIN(vA[0]); PIN(vA[1]); PIN(vA[2]); }
      if constexpr (s > 432 && s < 471 && s % 2 == 1 && s != 447) {
        constexpr int p = (s - 433) / 2;
        const unsigned ad = qrow + ((qd[p / 4] >> (8 * (p % 4))) & 255) * 16;
        ds_write_a64<A_Q + 4 * p, 0>(ad); ds_write_a64<A_Q + 4 * p + 2, 16 * QK_RS>(ad);
      }
      if constexpr (s == 471 || s == 473 || s == 477) {
        constexpr int hi = s == 471 ? 2 : s == 473 ? 3 : 4;
        const f32x4 r = halo_val(IC(hi));
        const unsigned row = (hi == 4 && w == 3) ? 0 : hrow;
        *(LAS u32x2*)(lds + QK_OFF + row * QK_RS + halo_col(IC(hi)) * 16 + G * 8) = u32x2{cvtpk(r[0], r[1]), cvtpk(r[2], r[3])};
      }
      SB();
    }); });
    asm volatile("s_nop 7" : "+v"(acc[1][0][0]), "+v"(acc[1][0][1]), "+v"(acc[1][1][0]), "+v"(acc[1][1][1]));
    WAITL0(); BAR();
    {
      const int hd = lane & 7, i0 = 32 * w + 4 * r8;
      LAS char* rp[11];
#pragma unroll
      for (int jj = 0; jj < 11; ++jj) { rp[jj] = lds + QK_OFF + (i0 + 1 + jj) * QK_RS + hd * 36; PIN(rp[jj]); }
      unsigned qv[4][9], kv[11][9];
#pragma unroll
      for (int a = 0; a < 4; ++a)
#pragma unroll
        for (int c = 0; c < 9; ++c) qv[a][c] = *(LAS unsigned*)(rp[7 + a] + c * 4);
#pragma unroll
      for (int jj = 0; jj < 4; ++jj)
#pragma unroll
        for (int c = 0; c < 9; ++c) kv[jj][c] = *(LAS unsigned*)(rp[jj] + 288 + c * 4);
      {
#pragma unroll
        for (int k = 0; k < 4; ++k)
          *(LAS f32x4*)(uintptr_t)(vrow + ((qd[6] >> (16 + 8 * (k >> 1))) & 255) * 32 + (k & 1) * 16 * V_RS) = acc[1][k >> 1][k & 1] * rstd[k & 1];
        sfor<3>([&](auto I) {
          constexpr int hi = I + 4;
          const unsigned row = (hi == 4 && w != 3) ? 0 : hrow, col = (hi == 4 && w != 3) ? 0 : halo_col(IC(hi));
          *(LAS f32x4*)(lds + row * V_RS + col * 32 + G * 16) = halo_val(IC(hi));
        });
      }
      SB();
      float lg[4][8];
      sfor<8>([&](auto U) {
        constexpr int u = U;
        if constexpr (u + 4 < 11) {
#pragma unroll
          for (int c = 0; c < 9; ++c) kv[u + 4][c] = *(LAS unsigned*)(rp[u + 4] + 288 + c * 4);
        }
        if constexpr (u < 7) {
          constexpr int p = 19 + u;
          const unsigned ad = vrow + ((qd[p / 4] >> (8 * (p % 4))) & 255) * 32;
          ds_write_a128<A_V + 8 * u, 0>(ad); ds_write_a128<A_V + 8 * u + 4, 16 * V_RS>(ad);
        }
#pragma unroll
        for (int a = 0; a < 4; ++a) lg[a][u] = 0.f;
#pragma unroll
        for (int c = 0; c < 9; ++c)
#pragma unroll
          for (int a = 0; a < 4; ++a) lg[a][u] = dot2(qv[a][c], kv[a + u][c], lg[a][u]);
        SB();
      });
      WAITL0(); BAR();
      mark<1, 7>();
      LAS char* vp = lds + (i0 + 1) * V_RS + hd * 72;
      f32x2 vv[11][9];
#pragma unroll
      for (int jj = 0; jj < 3; ++jj)
#pragma unroll
        for (int c = 0; c < 9; ++c) vv[jj][c] = *(volatile LAS f32x2*)(vp + jj * V_RS + c * 8);
      if (rt < 2) {
#pragma unroll
        for (int a = 0; a < 4; ++a)
#pragma unroll
          for (int u = 0; u < 7 - a; ++u) lg[a][u] = (i0 + a + u < 7) ? -1e9f : lg[a][u];
      }
      constexpr float CS = 0.23570226039551584f * 1.4426950408889634f;
#pragma unroll
      for (int a = 0; a < 4; ++a) {
        float mx = lg[a][0];
#pragma unroll
        for (int u = 1; u < 8; ++u) mx = fmaxf(mx, lg[a][u]);
        const float nb = -mx * CS;
        float sum = 0.f;
#pragma unroll
        for (int u = 0; u < 8; ++u) { lg[a][u] = __builtin_amdgcn_exp2f(fmaf(lg[a][u], CS, nb)); sum += lg[a][u]; }
        const float inv = __builtin_amdgcn_rcpf(sum);
#pragma unroll
        for (int u = 0; u < 8; ++u) lg[a][u] *= inv;
      }
      SB();
      f32x2 o[4][9];
      sfor<11>([&](auto J) {
        constexpr int jj = J;
        if constexpr (jj + 3 < 11) {
#pragma unroll
          for (int c = 0; c < 9; ++c) vv[jj + 3][c] = *(volatile LAS f32x2*)(vp + (jj + 3) * V_RS + c * 8);
        }
#pragma unroll
        for (int c = 0; c < 9; ++c) {
#pragma unroll
          for (int a = 0; a < 4; ++a) {
            if (jj == a) o[a][c] = vv[jj][c] * lg[a][0];
            else if (jj > a && jj <= a + 7) o[a][c] = __builtin_elementwise_fma(vv[jj][c], f32x2{lg[a][jj - a], lg[a][jj - a]}, o[a][c]);
          }
        }
        if constexpr (jj == 7) mark<1, 8>();
        if constexpr (jj >= 7) {
#pragma unroll
          for (int c = 0; c < 9; ++c) *(LAS unsigned*)(rp[jj] + c * 4) = cvtpk(o[jj - 7][c][0], o[jj - 7][c][1]);
        }
        SB();
      });
    }
    {
      LAS char* rowp = (LAS char*)(uintptr_t)qrow;
#pragma unroll
      for (int f = 0; f < 9; ++f) {
        const u32x2 a = *(LAS u32x2*)(rowp + (f >> 1) * 64 + (f & 1) * 16 * QK_RS), b = *(LAS u32x2*)(rowp + (f < 8 ? (f >> 1) * 64 + (f & 1) * 16 * QK_RS + 32 : 256 + 16 * QK_RS));
        __builtin_amdgcn_raw_buffer_store_b128(u32x4{a[0], a[1], b[0], b[1]}, rws, l16, oso + f * 1024, 16);
      }
    }
    WAITV(0); BAR();
    bstore8_sc1(get_tag(), vflag, srdws, flo + (FL_O + kg) * 128);
  }
  asm volatile(".Lk_end1:");
}

extern "C" void kernel_launch(void* const* d_in, const int* in_sizes, int n_in, void* d_out, int out_size, void* d_ws, size_t ws_size, hipStream_t stream) {
  (void)hipFuncSetAttribute((const void*)main_k, hipFuncAttributeMaxDynamicSharedMemorySize, LDS_BYTES);
  const Args args = {d_in[0], d_in[1], d_in[2], d_in[4], d_in[5], d_in[6], d_in[7], d_out, d_ws, make_tab()};
  main_k<<<256, 256, LDS_BYTES, stream>>>(args);
}
```

```cpp
#define POINT 4
#define WST 0,48,96,144,192
#define LOCAL 0
#define EARLY 99
#include <hip/hip_runtime.h>
#include <stdint.h>
#include <utility>

typedef __bf16 bf16x8 __attribute__((ext_vector_type(8)));
typedef __bf16 bf16x4 __attribute__((ext_vector_type(4)));
typedef __bf16 bf16x2 __attribute__((ext_vector_type(2)));
typedef float f32x4 __attribute__((ext_vector_type(4)));
typedef float f32x2 __attribute__((ext_vector_type(2)));
typedef unsigned u32x4 __attribute__((ext_vector_type(4)));
typedef unsigned u32x2 __attribute__((ext_vector_type(2)));
typedef unsigned u32x16 __attribute__((ext_vector_type(16)));
typedef unsigned long long u64;
#define LAS __attribute__((address_space(3)))
#define GAS __attribute__((address_space(1)))
typedef GAS u64 gu64;
#define DI __device__ __forceinline__
#define SB() __builtin_amdgcn_sched_barrier(0)
#define IC(n) std::integral_constant<int, (n)>{}
#define RLX_AGENT __ATOMIC_RELAXED, __HIP_MEMORY_SCOPE_AGENT

template <class F, int... I> DI void sfor_(F&& f, std::integer_sequence<int, I...>) { (f(std::integral_constant<int, I>{}), ...); }
template <int N, class F> DI void sfor(F&& f) { sfor_(f, std::make_integer_sequence<int, N>{}); }

constexpr int S_ = 2048, D_ = 576, F_ = 4608;
constexpr int SLOT = 24576;
constexpr int TAB_OFF = 147456;
constexpr int LDS_BYTES = 163840;
constexpr int QK_OFF = 80512, QK_RS = 584, V_RS = 592;
constexpr unsigned H_OFF = 0, O_OFF = 20u << 20, P_OFF = 24u << 20, FLAG_OFF = 40u << 20, WS_BYTES = 41u << 20;
constexpr unsigned OOB = 0x40000000u;
constexpr int FL_HA = 0, FL_HB = 12, FL_O = 24, FL_FIN = 28, FL_EPOCH = 40, FL_N = 41;
constexpr int A_H = 56;
constexpr int A_Q = 16;
constexpr int A_V = 92;
constexpr int A_HH = 164;
constexpr int A_HX = 184;

DI int map_role(int bid) { return (bid >> 3) & 3; }
DI int map_kg(int bid) { return bid & 3; }
DI int map_rt(int bid) { return ((bid >> 2) & 1) * 8 + (bid >> 5); }

DI u32x4 make_srd(const void* p, unsigned bytes) {
  uint64_t a = (uint64_t)p;
  u32x4 r; r[0] = (unsigned)a; r[1] = (unsigned)(a >> 32) & 0xffffu; r[2] = bytes; r[3] = 0x00020000u;
  return r;
}
template <unsigned MC, unsigned SC> DI void glds16c(unsigned mb, unsigned voff, u32x4 srd, unsigned sb) {
  unsigned tmp;
  asm volatile("s_add_u32 m0, %1, %5\n\ts_add_u32 %0, %4, %6\n\tbuffer_load_dwordx4 %2, %3, %0 offen lds"
               : "=&s"(tmp) : "s"(mb), "v"(voff), "s"(srd), "s"(sb), "i"(MC), "i"(SC) : "memory", "scc");
}
template <unsigned C> DI void set_m0(unsigned b) { asm volatile("s_add_u32 m0, %0, %1" :: "s"(b), "i"(C) : "scc"); }
template <unsigned C> DI unsigned sadd(unsigned b) { unsigned r; asm volatile("s_add_u32 %0, %1, %2" : "=s"(r) : "s"(b), "i"(C) : "scc"); return r; }
template <int J> DI void glds3(unsigned v, u32x4 srd, unsigned so) { asm volatile("buffer_load_dwordx4 %0, %1, %2 offen offset:%c3 lds" :: "v"(v), "s"(srd), "s"(so), "i"(J * 1024) : "memory"); }
template <unsigned MC> DI void glds16r(unsigned mb, unsigned voff, u32x4 srd, unsigned& so, unsigned step) {
  asm volatile("s_add_u32 m0, %1, %5\n\ts_add_u32 %0, %0, %4\n\tbuffer_load_dwordx4 %2, %3, %0 offen lds"
               : "+s"(so) : "s"(mb), "v"(voff), "s"(srd), "s"(step), "i"(MC) : "memory", "scc");
}
constexpr int x_base(int t) { return (t / 3 == 1 ? 2 : 4) * SLOT + (t % 3 == 2 ? SLOT : 0); }
template <int T> DI void x_line(unsigned m6, unsigned vx0, unsigned vx1, u32x4 srd, unsigned s0, unsigned s1, unsigned s2, unsigned s3) {
  constexpr int d0 = x_base(T) + (T % 3 == 1 ? 4096 : 0) - (T % 3) * 128, d2 = T % 3 == 1 ? SLOT - 1024 : 1024;
  asm volatile("s_add_u32 m0, %0, %8\n\ts_nop 0\n\tbuffer_load_dwordx4 %1, %3, %4 offen offset:%c10 lds\n\t"
               "s_addk_i32 m0, 0x400\n\ts_nop 0\n\tbuffer_load_dwordx4 %2, %3, %5 offen offset:%c10 lds\n\t"
               "s_addk_i32 m0, %c9\n\ts_nop 0\n\tbuffer_load_dwordx4 %1, %3, %6 offen offset:%c10 lds\n\t"
               "s_addk_i32 m0, 0x400\n\ts_nop 0\n\tbuffer_load_dwordx4 %2, %3, %7 offen offset:%c10 lds"
               :: "s"(m6), "v"(vx0), "v"(vx1), "s"(srd), "s"(s0), "s"(s1), "s"(s2), "s"(s3), "i"(d0), "i"(d2), "i"((T % 3) * 128) : "memory", "scc");
}
DI u32x4 bload16(unsigned voff, u32x4 srd, unsigned soff) {
  u32x4 r;
  asm volatile("buffer_load_dwordx4 %0, %1, %2, %3 offen" : "=v"(r) : "v"(voff), "s"(srd), "s"(soff) : "memory");
  return r;
}
DI void warm(unsigned voff, u32x4 srd, unsigned soff) {
  asm volatile("buffer_load_dword a11, %0, %1, %2 offen" :: "v"(voff), "s"(srd), "s"(soff) : "memory");
}
DI u64 bload8_sc1(unsigned voff, u32x4 srd, unsigned soff) {
  u64 r;
  asm volatile("buffer_load_dwordx2 %0, %1, %2, %3 offen sc1" : "=v"(r) : "v"(voff), "s"(srd), "s"(soff) : "memory");
  return r;
}
template <int AR, int OFF> DI void aload16_sc1(unsigned voff, u32x4 srd, unsigned soff) {
  asm volatile("buffer_load_dwordx4 a[%c3:%c4], %0, %1, %2 offen offset:%c5 sc1" :: "v"(voff), "s"(srd), "s"(soff), "i"(AR), "i"(AR + 3), "i"(OFF) : "memory");
}
template <int ROLE, int K> DI void mark() { asm volatile("s_branch 1f\n\t.p2align 12\n.Lm%c0_%c1:\n\ts_setpc_b64 vcc\n1:" :: "i"(ROLE), "i"(K)); }
template <int R, int N0, int N1> DI void visit(unsigned sel) {
  constexpr auto c = [](int k, int n) { return k < n ? k : n - 1; };
  asm volatile("s_getpc_b64 vcc\n\ts_add_u32 s90, vcc_lo, %0\n\ts_addc_u32 s91, vcc_hi, 0\n\ts_add_u32 vcc_lo, vcc_lo, 52\n\ts_addc_u32 vcc_hi, vcc_hi, 0\n\ts_setpc_b64 s[90:91]\n\t"
               "s_branch .Lm0_%c1\n\ts_branch .Lm0_%c2\n\ts_branch .Lm0_%c3\n\ts_branch .Lm0_%c4\n\ts_branch .Lm1_%c5\n\ts_branch .Lm1_%c6\n\ts_branch .Lm1_%c7\n\ts_branch .Lm1_%c8"
               :: "s"(sel + 20), "i"(c(4 * R, N0)), "i"(c(4 * R + 1, N0)), "i"(c(4 * R + 2, N0)), "i"(c(4 * R + 3, N0)),
                  "i"(c(4 * R, N1)), "i"(c(4 * R + 1, N1)), "i"(c(4 * R + 2, N1)), "i"(c(4 * R + 3, N1)) : "vcc", "s90", "s91", "scc");
}
template <int N> DI void waitv() { asm volatile("s_waitcnt vmcnt(%c0)" :: "i"(N > 63 ? 63 : N) : "memory"); }
#define WAITV(n) asm volatile("s_waitcnt vmcnt(" #n ")" ::: "memory")
#define BAR() asm volatile("s_barrier" ::: "memory")
#define WAITL0() asm volatile("s_waitcnt lgkmcnt(0)" ::: "memory")
#define PIN(x) asm volatile("" : "+v"(x))
#define PINS(x) asm volatile("" : "+s"(x))

DI unsigned cvtpk(float lo, float hi) { f32x2 v = {lo, hi}; bf16x2 b = __builtin_convertvector(v, bf16x2); return __builtin_bit_cast(unsigned, b); }
DI float bflo(unsigned u) { return __uint_as_float(u << 16); }
DI float bfhi(unsigned u) { return __uint_as_float(u & 0xffff0000u); }
DI float dot2(unsigned a, unsigned b, float c) { return __builtin_amdgcn_fdot2_f32_bf16(__builtin_bit_cast(bf16x2, a), __builtin_bit_cast(bf16x2, b), c, false); }
DI float xor32(float v) { auto r = __builtin_amdgcn_permlane32_swap(__float_as_uint(v), __float_as_uint(v), false, false); return __uint_as_float(r[0]) + __uint_as_float(r[1]); }
DI float xor16(float v) { auto r = __builtin_amdgcn_permlane16_swap(__float_as_uint(v), __float_as_uint(v), false, false); return __uint_as_float(r[0]) + __uint_as_float(r[1]); }

DI bf16x4 rdhalf(LAS char* p) { return __builtin_amdgcn_ds_read_tr16_b64_v4bf16((LAS bf16x4*)p); }
DI bf16x8 cat(bf16x4 a, bf16x4 b) { return __builtin_shufflevector(a, b, 0, 1, 2, 3, 4, 5, 6, 7); }
template <bool FIRST> DI void mfma_vv(f32x4& acc, bf16x8 a, u32x4 b) {
  if constexpr (FIRST) asm volatile("v_mfma_f32_16x16x32_bf16 %0, %1, %2, 0" : "=&v"(acc) : "v"(a), "v"(b));
  else                 asm volatile("v_mfma_f32_16x16x32_bf16 %0, %1, %2, %0" : "+v"(acc) : "v"(a), "v"(b));
}
DI void mfma_vvc(f32x4& acc, u32x4 a, u32x4 b, const f32x4& c) { asm volatile("v_mfma_f32_16x16x32_bf16 %0, %1, %2, %3" : "=&v"(acc) : "v"(a), "v"(b), "v"(c)); }
template <bool FIRST, int AR> DI void mfma_va(f32x4& acc, bf16x8 a) {
  if constexpr (FIRST) asm volatile("v_mfma_f32_16x16x32_bf16 %0, %1, a[%c2:%c3], 0" : "=&v"(acc) : "v"(a), "i"(AR), "i"(AR + 3));
  else                 asm volatile("v_mfma_f32_16x16x32_bf16 %0, %1, a[%c2:%c3], %0" : "+v"(acc) : "v"(a), "i"(AR), "i"(AR + 3));
}
template <int AR> DI void mfma_k16(f32x4& acc, bf16x4 a) { asm volatile("v_mfma_f32_16x16x16_bf16 %0, %1, a[%c2:%c3], %0" : "+v"(acc) : "v"(a), "i"(AR), "i"(AR + 1)); }
template <bool FIRST, int AR> DI void mfma16_aa(f32x4& acc) {
  if constexpr (FIRST) asm volatile("v_mfma_f32_16x16x32_bf16 %0, a[%c1:%c2], a[%c1:%c2], 0" : "=&v"(acc) : "i"(AR), "i"(AR + 3));
  else                 asm volatile("v_mfma_f32_16x16x32_bf16 %0, a[%c1:%c2], a[%c1:%c2], %0" : "+v"(acc) : "i"(AR), "i"(AR + 3));
}
template <bool FIRST, int AD, int AR> DI void mfma16_ava(bf16x8 a) {
  if constexpr (FIRST) asm volatile("v_mfma_f32_16x16x32_bf16 a[%c1:%c2], %0, a[%c3:%c4], 0" :: "v"(a), "i"(AD), "i"(AD + 3), "i"(AR), "i"(AR + 3));
  else                 asm volatile("v_mfma_f32_16x16x32_bf16 a[%c1:%c2], %0, a[%c3:%c4], a[%c1:%c2]" :: "v"(a), "i"(AD), "i"(AD + 3), "i"(AR), "i"(AR + 3));
}
template <int AR> DI float aread(void) { float r; asm volatile("v_accvgpr_read_b32 %0, a[%c1]" : "=v"(r) : "i"(AR)); return r; }
template <int AR, int OFF> DI void ds_write_a128(unsigned addr) { asm volatile("ds_write_b128 %0, a[%c1:%c2] offset:%c3" :: "v"(addr), "i"(AR), "i"(AR + 3), "i"(OFF) : "memory"); }
DI float ror8(float v) { return __int_as_float(__builtin_amdgcn_update_dpp(0, __float_as_int(v), 0x128, 0xf, 0xf, false)); }
template <int AR> DI void awrite(unsigned v) { asm volatile("v_accvgpr_write_b32 a[%c1], %0" :: "v"(v), "i"(AR)); }
DI float i_fma(float a, float b, float c) { float r; asm volatile("v_fma_f32 %0, %1, %2, %3" : "=v"(r) : "v"(a), "v"(b), "v"(c)); return r; }
DI float i_mul(float a, float b) { float r; asm volatile("v_mul_f32_e32 %0, %1, %2" : "=v"(r) : "v"(a), "v"(b)); return r; }
DI float i_sqs(float a) { float r; asm volatile("v_mul_f32_e64 %0, %1, |%1|" : "=v"(r) : "v"(a)); return r; }
DI unsigned i_cvt(float a, float b) { unsigned r; asm volatile("v_cvt_pk_bf16_f32 %0, %1, %2" : "=v"(r) : "v"(a), "v"(b)); return r; }
DI unsigned i_relu(unsigned a) { unsigned r; asm volatile("v_pk_max_i16 %0, %1, 0" : "=v"(r) : "v"(a)); return r; }
DI void i_swap16(unsigned& a, unsigned& b) { asm volatile("v_permlane16_swap_b32 %0, %1" : "+v"(a), "+v"(b)); }
template <int OFF> DI void st16_sc1(u32x4 v, unsigned voff, u32x4 srd, unsigned soff) {
  asm volatile("buffer_store_dwordx4 %0, %1, %2, %3 offen offset:%c4 sc1" :: "v"(v), "v"(voff), "s"(srd), "s"(soff), "i"(OFF) : "memory");
}
template <int AR, int OFF> DI void ds_read_a64(unsigned addr) { asm volatile("ds_read_b64 a[%c1:%c2], %0 offset:%c3" :: "v"(addr), "i"(AR), "i"(AR + 1), "i"(OFF) : "memory"); }
DI void bstore8_sc1(u64 v, unsigned voff, u32x4 srd, unsigned soff) { asm volatile("buffer_store_dwordx2 %0, %1, %2, %3 offen sc1" :: "v"(v), "v"(voff), "s"(srd), "s"(soff) : "memory"); }
template <int AR, int OFF> DI void ds_write_a64(unsigned addr) { asm volatile("ds_write_b64 %0, a[%c1:%c2] offset:%c3" :: "v"(addr), "i"(AR), "i"(AR + 1), "i"(OFF) : "memory"); }

constexpr int LA = 7;
constexpr int WP1 = 4, WP2 = 2;
constexpr int NB = LA + 1, BMID = (23 - LA) & ~1;
template <bool ATTN> struct Sched {
  static constexpr int P1 = ATTN ? 504 : 432, NSTEP = ATTN ? 504 : 924, NSTAGE = ATTN ? 21 : 39;
  static constexpr int a_stage(int s) { return s < P1 ? 3 * (s / 72) + (s % 36) / 12 : s < 864 ? s / 24 : 36 + (s - 864) / 20; }
  static constexpr int a_tile(int s) { return s < P1 ? (s % 72) / 36 : s < 864 ? (s % 24) / 12 : ((s - 864) % 20) / 10; }
  static constexpr int a_ks(int s) { return s < 864 ? s % 12 : (s - 864) % 10; }
  static constexpr bool skip(int s) { return ATTN && s >= 108 && s < 144 && s % 2 == 1; }
  static constexpr int g_stage(int s) {
    if (s < 0) return (s + 24) / 6;
    if (s >= 864 || s % 4 != 1) return -1;
    const int st = s / 24 + 4;
    return st >= NSTAGE || (st >= 36 && (s % 24) / 4 == 5) ? -1 : st;
  }
  static constexpr int g_piece(int s) { return (s % 24) / 4; }
  static constexpr int g_last(int st) { int r = -25; for (int s = -24; s < NSTEP; ++s) if (g_stage(s) == st) r = s; return r; }
  static constexpr int warm_k(int s) { constexpr int T[5] = {WST}; for (int k = 0; k < 5; ++k) if (s >= 0 && s == T[k]) return k; return -1; }
  static constexpr int PUB_HA = 288 + BMID, PUB_HB = 504, POLL_HA = 388, CHK_HA = 432, POLL_HB = 580, CHK_HB = 624, POLL_O = 792, CHK_O = 828;
  static constexpr int h_load(int s) {
    if (ATTN || s % 4 != 3) return -1;
    return s >= 432 && s < 480 ? (s - 432) / 4 : s >= 504 && s < 600 ? (s % 8 == 3 ? 12 + (s - 504) / 8 : -1) : s >= 624 && s < 720 ? 24 + (s - 624) / 4 : -1;
  }
  static constexpr int o_load(int s) { return !ATTN && s > CHK_O && s < CHK_O + 19 && s % 4 > 1 ? (s - CHK_O) / 4 * 2 + s % 4 - 2 : -1; }
  static constexpr int x_load(int s) { return !ATTN && s >= 723 && s < 771 && s % 16 == 3 ? (s - 723) / 16 : -1; }
  static constexpr int NMARK = ATTN ? 9 : 11;
  static constexpr int mark_k(int s) {
    constexpr int M[] = {2, 84, 162, 240, 318, 396, 474, 574, 674, 774, 876}, A[] = {16, 98, 190, 268, 348, 430, 492};
    if (ATTN) { for (int k = 0; k < 7; ++k) if (s == A[k]) return k; }
    else { for (int k = 0; k < 11; ++k) if (s == M[k]) return k; }
    return -1;
  }
  static constexpr bool bar(int s) { return s < P1 ? (s % 72 == 0 || s % 72 == BMID || s % 72 == 48) : (s <= 864 && s % 24 == 0); }
  static constexpr bool lwait(int s) { return (s + 1) % (s < 468 && !ATTN ? WP1 : WP2) == 0; }
  static constexpr int bias_rd(int s) { return !ATTN && s < 432 ? (s % 36 == 28 ? 0 : s % 36 == 35 ? 1 : -1) : -1; }
  static constexpr bool g_set(int s) { return g_stage(s + 3) >= 0 && g_piece(s + 3) % 3 == 0; }
  static constexpr bool fixed(int s, int h) {
    if (bar(s) || g_set(s)) return true;
    return h == 0 ? (g_stage(s) >= 0 || h_load(s) >= 0 || o_load(s) >= 0 || x_load(s) >= 0 || bias_rd(s) >= 0) : lwait(s);
  }
  static constexpr int ST0 = 30, ST1 = 57;
  static constexpr int win0(int s) { return s < 864 ? s / 36 * 36 : 864 + (s - 864) / 10 * 10; }
  static constexpr int wlen(int s) { return s < 864 ? 36 : 10; }
  static constexpr int nop(int s) { return ATTN ? (s >= 36 ? 32 : 0) : s >= 36 && s < 468 ? 59 : s >= 874 ? 14 : 0; }
  static constexpr int epi_to(int s, int h) {
    int f = 0, u = 0;
    for (int q = 2 * win0(s) + 2; q < 2 * (win0(s) + wlen(s)); ++q) if (!fixed(q / 2, q % 2)) { ++f; if (q <= 2 * s + h) ++u; }
    return f >= nop(s) ? (u < nop(s) ? u : nop(s)) : u * nop(s) / f;
  }
  static constexpr int epi_from(int s, int h) { return 2 * s + h <= 2 * win0(s) + 2 ? 0 : h ? epi_to(s, 0) : epi_to(s - 1, 1); }
  static constexpr bool has_op(int s, int k) { return epi_from(s, 0) <= k && k < epi_to(s, 1); }
  static constexpr int stores(int s) { return ATTN || s < 0 || !nop(s) ? 0 : s < 864 ? has_op(s, ST0) + has_op(s, ST1) : has_op(s, 10) + has_op(s, 13); }
  static constexpr int vm(int s) {
    int c = (g_stage(s) >= 0) + (warm_k(s) >= 0) + stores(s);
    if (!ATTN && s >= 0) c += (h_load(s) >= 0) + (o_load(s) >= 0) + (s == POLL_HA) + (s == POLL_HB) + (s == POLL_O) + (x_load(s) >= 0);
    return c;
  }
  static constexpr int need(int s) {
    int r = -25;
    if (bar(s) && !(ATTN && s == 480)) r = g_last(s < P1 ? 3 * (s / 72) + (s % 72 == 0 ? 1 : s % 72 == BMID ? 2 : 3) : s < 864 ? s / 24 + 1 : 38);
    if (!ATTN) {
      const int e = s == PUB_HA || s == PUB_HB ? s - 1 : s == CHK_HA ? POLL_HA : s == CHK_HB ? POLL_HB : s == 646 ? 595 : s == CHK_O ? POLL_O : s == 720 ? 671 : s == 768 ? 719 : s == 863 ? 862 : -25;
      if (e > r) r = e;
    }
    return r;
  }
  static constexpr int allow(int s) { int c = 0; for (int k = need(s) + 1; k < s; ++k) c += vm(k); return c; }
  static constexpr bool ring_ok(int st) {
    int first = NSTEP, last = -1, refill = NSTEP;
    for (int s = 0; s < NSTEP; ++s) {
      if (a_stage(s) == st) { if (s - LA < first) first = s - LA; last = s; }
      if (ATTN && s >= 144 && s % 4 == 2 && 3 * (s / 72) + (s % 72) / 24 == st) { if (s - 8 < first) first = s - 8; if (s + 1 > last) last = s + 1; }
      if (g_stage(s) == st + 6 && s < refill) refill = s;
    }
    bool vis = st == 0, fre = refill == NSTEP;
    for (int b = 0; b < NSTEP; b += 2) if (bar(b)) { if (b < first && b + 72 > first && need(b) >= g_last(st)) vis = true; if (b >= last && b < refill) fre = true; }
    return vis && fre;
  }
};
template <bool ATTN, int... I> constexpr bool rings_ok(std::integer_sequence<int, I...>) { return (std::bool_constant<Sched<ATTN>::ring_ok(I)>::value && ...); }
static_assert(rings_ok<false>(std::make_integer_sequence<int, 39>{}) && rings_ok<true>(std::make_integer_sequence<int, 21>{}), "LDS ring protocol");
constexpr int kb_areg(int kb) { return kb == 0 ? A_H : kb == 1 ? A_H + 48 : kb == 2 ? A_H + 96 : kb == 3 ? A_H + 144 : kb == 4 ? A_H : kb == 5 ? A_H + 48 : A_H + 96; }
constexpr int qkv_pair(int kg, int p) {
  constexpr signed char T[4][28] = {
    {0,2,4,6, 16,8,10,-1, 18,20,22,24, 26,28,30,32, 12,14,34,52, 36,38,40,42, 44,46,48,50},
    {6,8,10,12, 0,2,4,-1, 24,26,28,30, 14,16,32,34, 18,20,22,50, 42,44,46,48, 36,38,40,52},
    {4,6,8,10, 12,14,16,-1, 22,24,26,28, 0,2,18,20, 30,32,34,36, 40,42,44,46, 48,50,52,38},
    {2,4,6,8, 0,10,12,-1, 20,22,24,26, 28,30,32,34, 14,16,18,36, 38,40,42,44, 46,48,50,52}};
  return T[kg][p];
}
struct QkvTab { unsigned v[4][16]; };
constexpr QkvTab make_tab() {
  QkvTab t = {};
  for (int kg = 0; kg < 4; ++kg)
    for (int p = 0; p < 28; ++p) {
      const int v = qkv_pair(kg, p), sv = v < 0 ? qkv_pair(kg, p - 1) : v;
      t.v[kg][p / 4] |= (unsigned)((sv / 18) * 72 + sv % 18) << (8 * (p % 4));
      t.v[kg][8 + p / 4] |= (unsigned)(sv >= 36 ? sv - 36 : sv) << (8 * (p % 4));
    }
  return t;
}

DI void check_flag(u64 f, u64 tag, unsigned voff, u32x4 srd, unsigned soff) {
  unsigned n;
  asm volatile("v_cmp_ne_u64_e32 vcc, %1, %2\n\ts_cbranch_vccz 1f\n\ts_mov_b32 %0, 0x40000\n2:\n\ts_sleep 1\n\t"
               "buffer_load_dwordx2 %1, %3, %4, %5 offen sc1\n\ts_sub_u32 %0, %0, 1\n\ts_waitcnt vmcnt(0)\n\ts_cbranch_scc1 1f\n\t"
               "v_cmp_ne_u64_e32 vcc, %1, %2\n\ts_cbranch_vccnz 2b\n1:"
               : "=&s"(n), "+v"(f) : "v"(tag), "v"(voff), "s"(srd), "s"(soff) : "vcc", "scc", "memory");
}
DI void spin_until(gu64* p, u64 tag) {
#pragma unroll 1
  for (unsigned spins = 0; spins < (1u << 18); ++spins) {
    const u64 f = __hip_atomic_load(p, RLX_AGENT);
    if (__all(f == tag)) break;
  }
}

struct Args { const void *x, *wqkv, *wo, *w1, *b1, *w2, *b2; void *out, *ws; QkvTab qt; };
static_assert(sizeof(Args) == 72 + 256, "no padding");
DI char* as_ptr(unsigned lo, unsigned hi) { return (char*)(((u64)hi << 32) | lo); }

__global__ __launch_bounds__(256, 1) void main_k(const Args) {
  extern __shared__ __attribute__((aligned(16))) char smem[];
  LAS char* lds = (LAS char*)smem;
  const int tid = threadIdx.x, lane = tid & 63, bid = blockIdx.x;
  const int kg = map_kg(bid), cg = map_role(bid), rt = map_rt(bid);
  u64 tent, tA = 0; asm volatile("s_memrealtime %0" : "=s"(tent));
#define PT(i) do { if (POINT == i) asm volatile("s_memrealtime %0\n\ts_waitcnt lgkmcnt(0)" : "=s"(tA) :: "memory"); } while (0)
  u32x16 ka, kt; u32x2 kw;
  asm volatile("s_load_dwordx16 %0, %3, 0x0\n\ts_load_dwordx2 %1, %3, 0x40\n\ts_load_dwordx16 %2, %3, %4 offset:0x48"
               : "=&s"(ka), "=&s"(kw), "=&s"(kt) : "s"(__builtin_amdgcn_kernarg_segment_ptr()), "s"(kg * 64));
  const void* code;
  unsigned code_bytes;
  asm volatile("s_getpc_b64 %0\n.Lk_pc:\n\ts_mov_b32 %1, .Lk_end0-.Lk_pc\n\ts_max_i32 %1, %1, .Lk_end1-.Lk_pc" : "=s"(code), "=s"(code_bytes) :: "scc", "a255");
  const int w = __builtin_amdgcn_readfirstlane(tid >> 6);
  const bool attn = cg == 3;
  const int tokbase = 256 * (rt >> 1) + (rt & 1);
  const int n16 = lane & 15, r8 = lane >> 3, pos = lane & 7;
  const int G = lane >> 4, q4 = (lane >> 2) & 3, p4 = lane & 3;

  const unsigned rot = (0x24924924u >> (2 * ((rt & 7) + cg))) & 3;
  unsigned kro[3] = {rot * 192, rot == 2 ? 0 : rot * 192 + 192, rot == 0 ? 384 : rot * 192 - 192};
  unsigned m6 = 6 * w * 1024, sx = tokbase * 1152 + w * 32 * 2304;
  unsigned vx0 = r8 * 2304 + ((pos ^ G) << 4), vx1 = vx0 ^ 64;
  PINS(m6); PINS(sx); PIN(vx0); PIN(vx1); PINS(kro[0]); PINS(kro[1]); PINS(kro[2]);
  const unsigned swz = pos ^ (((r8 >> 1) & 3) << 1);
  const int fs = 3 * kg + cg, f0 = fs * 384;
  unsigned strideA = attn ? 3456u : 9216u;
  PINS(strideA);
  unsigned soA[3];
#pragma unroll
  for (int j = 0; j < 3; ++j) { soA[j] = (kro[j] + 48 * w) * strideA + (attn ? 288 * kg : f0 * 2); PINS(soA[j]); }
  unsigned vab = r8 * strideA + (swz & 1) * 16, vsh = 8 * (swz >> 1), vcode = tid < 64 ? tid * 128 : OOB;
  PIN(vab); PIN(vsh); PIN(vcode);
  u32x4 id0, id1;
  {
    const unsigned one = 0x3f80u << (16 * (n16 & 1));
    const int e = (n16 >> 1) - 4 * G;
#pragma unroll
    for (int j = 0; j < 4; ++j) { id0[j] = e == j ? one : 0u; id1[j] = e + 8 == j ? one : 0u; }
  }
  PIN(id0); PIN(id1);
  unsigned po0 = (4 * G + q4) * 128 + (((2 * G + (q4 >> 1)) & 3) << 5) + p4 * 8;
  LAS char* rb[4][3];
#pragma unroll
  for (int i = 0; i < 3; ++i)
#pragma unroll
    for (int c = 0; c < 4; ++c) { rb[c][i] = lds + (po0 ^ (c << 5)) + 2 * i * SLOT; PIN(rb[c][i]); }
  LAS char* xp[2][2];
#pragma unroll
  for (int u = 0; u < 2; ++u) {
    const unsigned xr0 = w * 6144 + (n16 >> 3) * 1024 + (n16 & 7) * 128 + (((G ^ (n16 >> 1)) << 4) ^ (u << 6));
    xp[0][u] = lds + xr0 + 4 * SLOT; xp[1][u] = lds + xr0 + 2 * SLOT; PIN(xp[0][u]); PIN(xp[1][u]);
  }
  unsigned ones = 0x3f803f80u; PIN(ones);
  const int c1 = cg == 2 ? 0 : cg + 1, c2 = cg == 0 ? 2 : cg - 1;
  unsigned soB[3];
  soB[0] = (f0 + 48 * w) * 1152 + 384 * cg; soB[1] = ((3 * kg + c1) * 384 + 48 * w) * 1152 + 384 * cg; soB[2] = ((3 * kg + c2) * 384 + 48 * w) * 1152 + 384 * cg;
  const int p5 = w == 3 ? 13 : 5 * w;
  unsigned soO = (144 * kg + 8 * p5) * 1152 + 384 * cg, m5 = p5 * 1024;
  unsigned hso[3];
  hso[0] = H_OFF + ((rt * 12 + fs) * 4 + w) * 24576; hso[1] = H_OFF + ((rt * 12 + 3 * kg + c1) * 4 + w) * 24576; hso[2] = H_OFF + ((rt * 12 + 3 * kg + c2) * 4 + w) * 24576;
  unsigned oso = O_OFF + ((rt * 4 + kg) * 4 + w) * 9216;
  unsigned pso = P_OFF + (((rt * 3 + cg) * 4 + kg) * 4 + w) * 12288;
  unsigned os[3];
#pragma unroll
  for (int k = 0; k < 3; ++k) { const int un = 3 * kg + k; os[k] = (tokbase + 64 * w + 32 * (un & 1)) * 1152 + (192 * cg + 32 * (un >> 1)) * 2; }
#pragma unroll
  for (int k = 0; k < 3; ++k) { PINS(soB[k]); PINS(hso[k]); PINS(os[k]); }
  PINS(pso); PINS(oso); PINS(soO); PINS(m5);
  asm volatile("s_waitcnt lgkmcnt(0)" : "+s"(ka), "+s"(kw), "+s"(kt), "+s"(tent));
  PT(0);
  const char *x = as_ptr(ka[0], ka[1]), *wo = as_ptr(ka[4], ka[5]), *b1 = as_ptr(ka[8], ka[9]), *w2 = as_ptr(ka[10], ka[11]), *b2 = as_ptr(ka[12], ka[13]);
  char *out = as_ptr(ka[14], ka[15]), *ws = as_ptr(kw[0], kw[1]);
  const u32x4 srdx = make_srd(x, S_ * D_ * 2);
  auto x_more = [&](auto T) { const unsigned b = sx + kro[T / 3] * 2; x_line<T>(m6, vx0, vx1, srdx, b, b + 8 * 2304, b + 16 * 2304, b + 24 * 2304); };
  sfor<3>(x_more);
  const unsigned vsel = 4 * w + (attn ? 16 : 0);
  visit<0, Sched<false>::NMARK, Sched<true>::NMARK>(vsel);
  u32x4 srdA = make_srd(attn ? as_ptr(ka[2], ka[3]) : as_ptr(ka[6], ka[7]), attn ? D_ * 3 * D_ * 2 : D_ * F_ * 2);
  PINS(srdA);
  unsigned qs[7], qd[7];
#pragma unroll
  for (int i = 0; i < 7; ++i) { qs[i] = kt[i]; qd[i] = kt[8 + i]; }
  auto va_of = [&](unsigned word) { return vab + ((word >> vsh) & 255) * 16; };
  unsigned va = va_of(attn ? qs[0] : 0x06040200u), so = soA[0] - 8 * strideA;
  auto w_piece = [&](auto K) {
    constexpr int n = K / 6, p = K % 6;
    if constexpr (p == 0 && n > 0) so = soA[n % 3] - 8 * strideA;
    if constexpr (K == 18) va = va_of(attn ? qs[1] : 0x0e0c0a08u);
    glds16r<n * SLOT + p * 1024>(m6, va, srdA, so, 8 * strideA);
  };
  sfor<6>(w_piece);
  warm(vcode, make_srd(code, code_bytes), ((bid >> 3) & 15) * 8192);
  visit<1, Sched<false>::NMARK, Sched<true>::NMARK>(vsel);
  const u32x4 srdws = make_srd(ws, WS_BYTES);
  const unsigned flo = FLAG_OFF + rt * FL_N * 128;
  auto misc_loads = [&]() {
    asm volatile("buffer_load_dwordx2 a[12:13], %0, %1, %2 offen sc1" :: "v"(0), "s"(srdws), "s"(flo + FL_EPOCH * 128) : "memory");
    if (attn) {
      int ht = tokbase + 2 * (r8 - 8); ht = ht < 0 ? 0 : ht;
      const unsigned vh = ht * 1152 + pos * 16;
      const unsigned kb = ((0x24924924u >> (2 * (rot + w))) & 3) * 384 | (w == 3 ? OOB : 0);
      glds16c<TAB_OFF, 0>(w * 3072, vh, srdx, kb); glds16c<TAB_OFF + 1024, 128>(w * 3072, vh, srdx, kb); glds16c<TAB_OFF + 2048, 256>(w * 3072, vh, srdx, kb);
    } else {
      asm volatile("buffer_load_dword a14, %0, %1, 0 offen" :: "v"(tid * 4), "s"(make_srd(b1 + f0 * 2, 768)) : "memory");
      asm volatile("buffer_load_dword a15, %0, %1, 0 offen" :: "v"(tid * 4), "s"(make_srd(b2 + 192 * cg * 2, 384)) : "memory");
    }
  };
  u32x4 xf[36];
  f32x4 nm[2];
  float rstd[2];
  {
    float sm[2] = {0.f, 0.f}, sq[2] = {0.f, 0.f};
    SB();
    sfor<10>([&](auto T) {
      constexpr int t = T;
      if constexpr (t == 0) x_more(IC(3));
      if constexpr (t + 4 < 9) x_more(IC(t + 4));
      if constexpr (t == 4) misc_loads();
      if constexpr (t == EARLY) {
        const unsigned a = rt * 36 + w * 9 + (lane & 15), j = lane >> 4;
        const unsigned l = attn ? ((j == 0 ? qs[1] : j == 1 ? qs[1] >> 16 : j == 2 ? qs[2] : qs[3]) & 255) * 16 : 128 + 128 * j;
        warm((a * strideA + l) | ((lane & 15) < 9 ? 0 : OOB), srdA, attn ? 288 * kg : f0 * 2);
      }
      if constexpr (t == 0) visit<2, Sched<false>::NMARK, Sched<true>::NMARK>(vsel);
      if constexpr (t < 9) {
        waitv<4 * ((t + 4 > 8 ? 8 : t + 4) - t) + (t < 3 ? 6 : 0)>();
#pragma unroll
        for (int u = 0; u < 4; ++u) xf[4 * t + u] = *(LAS u32x4*)(xp[t / 3 == 1][u >> 1] + (t % 3 == 1 ? 4096 + (u & 1) * SLOT : (t % 3 == 2 ? SLOT : 0) + (u & 1) * 2048));
      }
      SB();
      if constexpr (t > 0) {
#pragma unroll
        for (int u = 0; u < 4; u += 2)
#pragma unroll
          for (int j = 0; j < 4; ++j) {
            const unsigned v0 = xf[4 * t - 4 + u][j], v1 = xf[4 * t - 3 + u][j];
            sm[0] = dot2(v0, ones, sm[0]); sm[1] = dot2(v1, ones, sm[1]); sq[0] = dot2(v0, v0, sq[0]); sq[1] = dot2(v1, v1, sq[1]);
          }
        PIN(sm[0]); PIN(sm[1]); PIN(sq[0]); PIN(sq[1]);
      }
      if constexpr (t == 8) sfor<6>([&](auto P) { w_piece(IC(6 + P)); });
      SB();
    });
#pragma unroll
    for (int tb = 0; tb < 2; ++tb) {
      const float negmu = xor32(xor16(sm[tb])) * (-1.0f / D_);
      nm[tb] = f32x4{negmu, negmu, negmu, negmu};
      rstd[tb] = __builtin_amdgcn_rsqf(fmaxf(xor32(xor16(sq[tb])) * (1.0f / D_) - negmu * negmu, 0.f) + 1e-5f);
    }
  }
  PT(1);
  bf16x4 wl[NB], wu[NB];
  {
    f32x4 dl[3], dh[3];
    asm volatile("s_nop 1" : "+v"(nm[0]), "+v"(nm[1]));
    sfor<38>([&](auto B) {
      constexpr int b = B;
      if constexpr (b < 36) mfma_vvc(dl[b % 3], id0, xf[b], nm[b & 1]);
      if constexpr (b == 36) {
        asm volatile("s_waitcnt vmcnt(18)\n\ts_barrier" : "+v"(dl[1]), "+v"(dh[1]), "+v"(dl[2]), "+v"(dh[2]) : "v"(nm[0]), "v"(nm[1]) : "memory");
#pragma unroll
        for (int t = 0; t < LA; ++t) { wl[t] = rdhalf(rb[t & 1][0] + (t >> 1) * 4096); wu[t] = rdhalf(rb[t & 1][0] + (t >> 1) * 4096 + 2048); }
      }
      SB();
      if constexpr (b >= 2) { xf[b - 2][0] = cvtpk(dl[(b - 2) % 3][0], dl[(b - 2) % 3][1]); xf[b - 2][1] = cvtpk(dl[(b - 2) % 3][2], dl[(b - 2) % 3][3]); }
      SB();
      if constexpr (b < 36) mfma_vvc(dh[b % 3], id1, xf[b], nm[b & 1]);
      SB();
      if constexpr (b >= 2) { xf[b - 2][2] = cvtpk(dh[(b - 2) % 3][0], dh[(b - 2) % 3][1]); xf[b - 2][3] = cvtpk(dh[(b - 2) % 3][2], dh[(b - 2) % 3][3]); PIN(xf[b - 2]); }
      if constexpr (b % 2 == 0 && b < 24) w_piece(IC(12 + b / 2));
      SB();
    });
  }

  PT(2);
  const u32x4 srdW2 = make_srd(w2, F_ * D_ * 2), srdWo = make_srd(wo, D_ * D_ * 2);
  const __amdgpu_buffer_rsrc_t rws = __builtin_amdgcn_make_buffer_rsrc(ws, 0, WS_BYTES, 0x00020000);
  gu64* flags = (gu64*)((char*)ws + flo);
  auto get_tag = [&]() __attribute__((always_inline)) { return (((u64)__float_as_uint(aread<13>()) << 32) | __float_as_uint(aread<12>())) * 0x9E3779B97F4A7C15ull + 0x632BE59BD9B4E019ull; };
  float t0, t1;
  f32x4 acc[2][2][2];
#define LANE_VALUES int tid_ = threadIdx.x; PIN(tid_); \
  const int tid = tid_, lane = tid & 63, n16 = lane & 15, r8 = lane >> 3, pos = lane & 7, G = lane >> 4, q4 = (lane >> 2) & 3, p4 = lane & 3; \
  const unsigned swz = pos ^ (((r8 >> 1) & 3) << 1), l16 = lane * 16, vflag = tid == 0 ? 0u : OOB, wa = rt * 72 + w * 18 + lane, wm = lane < 18 ? 0 : OOB, wa0 = LOCAL ? (rt ^ ((lane & 32) >> 2)) * 72 + w * 18 + (lane & 31) : wa, wm0 = LOCAL ? ((lane & 31) < 18 ? 0 : OOB) : wm; \
  (void)q4; (void)p4;

  if (!attn) {
    using SC = Sched<false>;
    LANE_VALUES
    constexpr bool attn_role = false;
    auto rd_a = [&](auto Sc, auto U) {
      constexpr int s = Sc, u = U;
      if constexpr (s < SC::NSTEP && !SC::skip(s) && !(u == 1 && s >= 864 && SC::a_ks(s) >= 8)) {
        constexpr int sl = SC::a_stage(s) % 6, ks = SC::a_ks(s);
        (u ? wu : wl)[s % NB] = rdhalf(rb[2 * SC::a_tile(s) + (ks & 1)][sl >> 1] + (sl & 1) * SLOT + (ks >> 1) * 4096 + u * 2048);
      }
    };
    auto mma1 = [&](auto Sc, auto H) {
      constexpr int s = Sc, k = s % 36, o = k & 1, tb = o ^ H;
      if constexpr (!SC::skip(s)) mfma_vv<k < 2>(acc[SC::a_tile(s)][o][tb], cat(wl[s % NB], wu[s % NB]), xf[(k & ~1) + tb]);
    };
    auto sync = [&](auto Sc, auto H) {
      constexpr int s = Sc, h = H;
      if constexpr (h == 0 && SC::need(s) > -25) waitv<SC::allow(s)>();
      if constexpr (h == 1 && (s == BMID || (attn_role && s == 480))) WAITL0();
      if constexpr (h == 1 && SC::bar(s)) BAR();
      if constexpr (h == 1 && SC::lwait(s)) __builtin_amdgcn_s_waitcnt(0xC07F | ((2 * (LA - (s < 468 && !attn_role ? WP1 : WP2))) << 8));
    };
    unsigned vA[3] = {va - 128, va - 128 + 8 * 9216 - 1024, va - 128 + 2 * (8 * 9216 - 1024)}, vB[3], lk[3], gso = sadd<128>(soA[1]), hst = hso[0];
    set_m0<4 * SLOT>(m6);
    u64 tag;
    unsigned vpoll, pso_;
    LAS char* tab = lds + TAB_OFF + G * 16;
    f32x4 bs[2], bn;
    f32x4 ac[6][2][2];
    unsigned e[8];
    u64 pollv = 0;
    const unsigned ov = n16 * 2304 + (G & 1) * 32 + (G >> 1) * 16;
    u32x4 xr[3];
    float u0[2], u1[2];
    auto op1 = [&](auto Tc, auto K) {
      constexpr int T = Tc, k = K;
      if constexpr (k == SC::ST0 || k == SC::ST1) { constexpr int q = k == SC::ST1; st16_sc1<(T & 1) * 2048 + q * 1024>(u32x4{e[4 * q], e[4 * q + 1], e[4 * q + 2], e[4 * q + 3]}, l16, srdws, hst); }
      else if constexpr (k == 58) { if constexpr (T & 1) hst = sadd<4096>(hst); }
      else {
        constexpr int kk = k > SC::ST0 ? k - 1 : k, r = (kk - 4) % 7;
        constexpr int j = kk < 4 ? kk : kk >= 53 ? kk - 49 : r == 0 ? 4 : r == 1 ? 0 : r == 2 ? 5 : r == 3 ? 1 : r == 4 ? 2 : r == 5 ? 3 : 6;
        constexpr int d = kk < 4 ? 0 : kk >= 53 ? 7 : (kk - 4) / 7 + (j < 4), hf = d & 1;
        const f32x4& a = acc[T & 1][(d >> 1) & 1][d >> 2];
        if constexpr (j == 0) u0[hf] = i_fma(a[2 * hf], rstd[d >> 2], bs[(d >> 1) & 1][2 * hf]);
        if constexpr (j == 1) u1[hf] = i_fma(a[2 * hf + 1], rstd[d >> 2], bs[(d >> 1) & 1][2 * hf + 1]);
        if constexpr (j == 2) u0[hf] = i_sqs(u0[hf]);
        if constexpr (j == 3) u1[hf] = i_sqs(u1[hf]);
        if constexpr (j == 4) e[d] = i_cvt(u0[hf], u1[hf]);
        if constexpr (j == 5) e[d] = i_relu(e[d]);
        if constexpr (j == 6) awrite<A_H + 8 * T + d>(e[d]);
      }
    };
    auto op2 = [&](auto Tc, auto K) {
      constexpr int T = Tc, k = K;
      if constexpr (k < 8) e[k] = i_cvt(ac[T][(k >> 1) & 1][k >> 2][2 * (k & 1)], ac[T][(k >> 1) & 1][k >> 2][2 * (k & 1) + 1]);
      else {
        constexpr int q = (k - 8) / 3, j = (k - 8) % 3;
        if constexpr (j < 2) i_swap16(e[4 * q + j], e[4 * q + 2 + j]);
        else st16_sc1<(T & 1) * 2048 + q * 1024>(u32x4{e[4 * q], e[4 * q + 1], e[4 * q + 2], e[4 * q + 3]}, lk[T >> 1], srdws, pso);
      }
    };
    auto slot = [&](auto Sc, auto H) {
      constexpr int s = Sc, h = H;
      sync(Sc, H);
      if constexpr (SC::g_set(s)) {
        constexpr int np = SC::g_stage(s + 3), p = SC::g_piece(s + 3), kb = (np - 18) / 3, src = kb < 2 ? 0 : 1 + (kb & 1), half = kb < 2 ? kb : (kb >= 4);
        if constexpr (h == 0) set_m0<(np % 6) * SLOT + p * 1024>(np < 36 ? m6 : m5);
        else if constexpr (np < 18) gso = sadd<8 * p * 9216 + 128 * (np / 3)>(soA[np % 3]);
        else if constexpr (np < 36) gso = sadd<(192 * half + 8 * p) * 1152 + 128 * ((np - 18) % 3)>(soB[src]);
        else gso = sadd<8 * p * 1152 + 128 * (np - 36)>(soO);
      }
      if constexpr (h == 0) {
        if constexpr (SC::g_stage(s) >= 0) { constexpr int np = SC::g_stage(s), j = SC::g_piece(s) % 3; glds3<j>(np < 18 ? vA[j] : vB[j], np < 18 ? srdA : np < 36 ? srdW2 : srdWo, gso); }
        if constexpr (SC::bias_rd(s) == 0) bn = *(LAS f32x4*)(tab + (s / 36) * 128);
        if constexpr (SC::bias_rd(s) == 1) { bs[0] = bn; bs[1] = *(LAS f32x4*)(tab + (s / 36) * 128 + 64); }
        if constexpr (SC::x_load(s) >= 0) { constexpr int k = SC::x_load(s); xr[k] = bload16(ov, srdx, os[k]); }
        if constexpr (SC::o_load(s) >= 0) { constexpr int f = SC::o_load(s); aload16_sc1<kb_areg(6) + 4 * f, (f & 3) * 1024>(lk[f >> 2], srdws, oso); }
        if constexpr (SC::h_load(s) >= 0) { constexpr int b = SC::h_load(s) / 12, f = SC::h_load(s) % 12; aload16_sc1<kb_areg(2 + b) + 4 * f, (f & 3) * 1024>(lk[f >> 2], srdws, hso[1 + (b & 1)]); }
      }
      if constexpr (SC::nop(s) > 0) {
        static_assert(SC::bias_rd(s) != 1 || s < 36 || SC::epi_from(s, 0) > 52, "the bias registers are free");
        constexpr int k0 = SC::epi_from(s, h), k1 = SC::epi_to(s, h);
        sfor<k1 - k0>([&](auto I) { if constexpr (s < 864) op1(IC(s / 36 - 1), IC(k0 + I)); else op2(IC((s - 864) / 10 - 1), IC(k0 + I)); if constexpr (s < 864 && k0 + I == SC::ST0 && k0 + I + 1 < k1) asm volatile("s_nop 1"); });
      }
    };
    sfor<77>([&](auto So) { sfor<12>([&](auto Si) {
      constexpr int s = 12 * So + Si;
      sfor<2>([&](auto H) {
        if constexpr (s < 432) mma1(IC(s), H);
        else {
          constexpr int n = SC::a_stage(s) - 18, kb = n / 3, T = 2 * (n % 3) + SC::a_tile(s), ks = SC::a_ks(s), o = ks & 1, tb = o ^ H, ar = kb_areg(kb) + 8 * (ks >> 1);
          if constexpr (kb == 6 && ks >= 8) mfma_k16<ar + 2 * tb>(ac[T][o][tb], wl[s % NB]);
          else mfma_va<kb == 0 && ks < 2, ar + 4 * tb>(ac[T][o][tb], cat(wl[s % NB], wu[s % NB]));
        }
        SB(); rd_a(IC(s + LA), H); SB();
        slot(IC(s), H); SB();
      });
      if constexpr (s == 216) PT(3);
      if constexpr (s == 432) PT(4);
      constexpr int k = SC::warm_k(s);
      if constexpr (SC::mark_k(s) >= 0) mark<0, SC::mark_k(s)>();
      if constexpr (k >= 0 && k < 2) warm(((wa0 >> 1) * 9216 + (wa0 & 1) * 128) | wm0, srdA, f0 * 2 + 256 + 256 * k);
      if constexpr (k >= 2) warm((wa * 128) | wm, srdW2, f0 * 1152 + (k - 2) * 147456);
      if constexpr (s == SC::PUB_HA || s == SC::PUB_HB) bstore8_sc1(tag, vflag, srdws, flo + ((s == SC::PUB_HA ? FL_HA : FL_HB) + fs) * 128);
      if constexpr (s == SC::CHK_HA || s == SC::CHK_HB || s == SC::CHK_O) check_flag(pollv, tag, vpoll, srdws, pso_);
      if constexpr (s == 4) { const unsigned r = __float_as_uint(aread<14>()); *(LAS f32x2*)(lds + TAB_OFF + tid * 8) = f32x2{bflo(r), bfhi(r)}; }
      if constexpr (s == 8) { const unsigned r = __float_as_uint(aread<15>()); *(LAS f32x2*)(lds + TAB_OFF + 2048 + tid * 8) = f32x2{bflo(r), bfhi(r)}; }
      if constexpr (s == 12) { tag = get_tag(); PIN(tag); }
      if constexpr (s == 334) { vB[0] = r8 * 1152 + swz * 16; vB[1] = vB[0] + 8192; vB[2] = vB[0] + 16384; PIN(vB[0]); PIN(vB[1]); PIN(vB[2]); }
      if constexpr (s == 386) { vpoll = (3 * kg + ((lane & 1) ? c1 : c2)) * 128; PIN(vpoll); }
      if constexpr (s == 432) { lk[0] = l16; lk[1] = l16 + 4096; lk[2] = l16 + 8192; PIN(lk[1]); PIN(lk[2]); }
      if constexpr (s == 600) { hso[1] += 12288; hso[2] += 12288; PINS(hso[1]); PINS(hso[2]); }
      if constexpr (s == SC::POLL_HA || s == SC::POLL_HB || s == SC::POLL_O) {
        if constexpr (s == SC::POLL_O) vpoll = kg * 128;
        pso_ = flo + (s == SC::POLL_HA ? FL_HA : s == SC::POLL_HB ? FL_HB : FL_O) * 128;
        pollv = bload8_sc1(vpoll, srdws, pso_);
      }
      SB();
    }); });
    asm volatile("s_nop 7" : "+v"(ac[5][0][0]), "+v"(ac[5][0][1]), "+v"(ac[5][1][0]), "+v"(ac[5][1][1]));
    sfor<14>([&](auto K) { op2(IC(5), K); SB(); });
    asm volatile("s_nop 1" : "+v"(xr[0]), "+v"(xr[1]), "+v"(xr[2]));
    SB();
    PT(5);
    float a[3][8];
    f32x4 cb[3][2];
#pragma unroll
    for (int k = 0; k < 3; ++k) {
      LAS char* bp = lds + TAB_OFF + 2048 + 128 * ((3 * kg + k) >> 1) + (G & 1) * 64 + (G >> 1) * 32;
      cb[k][0] = *(LAS f32x4*)bp; cb[k][1] = *(LAS f32x4*)(bp + 16);
    }
    gu64* fp = flags + (FL_FIN + 3 * (lane & 3) + cg) * 16;
    PIN(fp);
    SB();
#pragma unroll
    for (int k = 0; k < 3; ++k) {
      const f32x4 c0 = cb[k][0], c1 = cb[k][1];
#pragma unroll
      for (int j = 0; j < 4; ++j) { a[k][2 * j] = bflo(xr[k][j]) + (j < 2 ? c0 : c1)[(2 * j) & 3]; a[k][2 * j + 1] = bfhi(xr[k][j]) + (j < 2 ? c0 : c1)[(2 * j + 1) & 3]; PIN(a[k][2 * j]); PIN(a[k][2 * j + 1]); }
    }
    unsigned sel0 = 0x00003f80u, sel1 = 0x3f800000u, pb = P_OFF + ((rt * 3 + cg) * 16 + w) * 12288 + 3 * kg * 1024;
    PIN(sel0); PIN(sel1); PINS(pb);
    WAITV(0); BAR();
    bstore8_sc1(tag, vflag, srdws, flo + (FL_FIN + fs) * 128);
    spin_until(fp, tag);
    const __amdgpu_buffer_rsrc_t rout = __builtin_amdgcn_make_buffer_rsrc(out, 0, S_ * D_ * 2, 0x00020000);
    u32x4 v[3][4];
#pragma unroll
    for (int k = 0; k < 3; ++k)
#pragma unroll
      for (int s = 0; s < 4; ++s) v[k][s] = __builtin_amdgcn_raw_buffer_load_b128(rws, l16 + k * 1024, pb + s * 4 * 12288, 16);
#pragma unroll
    for (int k = 0; k < 3; ++k) {
#pragma unroll
      for (int s = 0; s < 4; ++s)
#pragma unroll
        for (int j = 0; j < 4; ++j) { a[k][2 * j] = dot2(v[k][s][j], sel0, a[k][2 * j]); a[k][2 * j + 1] = dot2(v[k][s][j], sel1, a[k][2 * j + 1]); }
      __builtin_amdgcn_raw_buffer_store_b128(u32x4{cvtpk(a[k][0], a[k][1]), cvtpk(a[k][2], a[k][3]), cvtpk(a[k][4], a[k][5]), cvtpk(a[k][6], a[k][7])}, rout, ov, os[k], 0);
    }
    if (fs == 0) bstore8_sc1(tag, vflag, srdws, flo + FL_EPOCH * 128);
    { u64 tn; do { asm volatile("s_memrealtime %0\n\ts_waitcnt lgkmcnt(0)" : "=s"(tn)); } while (tn - tent < 4000 + (tA - tent)); }
    asm volatile(".Lk_end0:");
    __builtin_amdgcn_endpgm();
  }
  {
    using SC = Sched<true>;
    LANE_VALUES
    auto va_of = [&](unsigned word) { return r8 * 3456 + (swz & 1) * 16 + ((word >> (8 * (swz >> 1))) & 255) * 16; };
    constexpr bool attn_role = true;
    auto rd_a = [&](auto Sc, auto U) {
      constexpr int s = Sc, u = U;
      if constexpr (s < SC::NSTEP && !SC::skip(s) && !(u == 1 && s >= 864 && SC::a_ks(s) >= 8)) {
        constexpr int sl = SC::a_stage(s) % 6, ks = SC::a_ks(s);
        (u ? wu : wl)[s % NB] = rdhalf(rb[2 * SC::a_tile(s) + (ks & 1)][sl >> 1] + (sl & 1) * SLOT + (ks >> 1) * 4096 + u * 2048);
      }
    };
    auto mma1 = [&](auto Sc, auto H) {
      constexpr int s = Sc, k = s % 36, o = k & 1, tb = o ^ H;
      if constexpr (!SC::skip(s)) mfma_vv<k < 2>(acc[SC::a_tile(s)][o][tb], cat(wl[s % NB], wu[s % NB]), xf[(k & ~1) + tb]);
    };
    auto sync = [&](auto Sc, auto H) {
      constexpr int s = Sc, h = H;
      if constexpr (h == 0 && SC::need(s) > -25) waitv<SC::allow(s)>();
      if constexpr (h == 1 && (s == BMID || (attn_role && s == 480))) WAITL0();
      if constexpr (h == 1 && SC::bar(s)) BAR();
      if constexpr (h == 1 && SC::lwait(s)) __builtin_amdgcn_s_waitcnt(0xC07F | ((2 * (LA - (s < 468 && !attn_role ? WP1 : WP2))) << 8));
    };
    unsigned vA[3] = {va, va + 8 * 3456 - 1024, va + 2 * (8 * 3456 - 1024)}, gso = soA[1];
    set_m0<4 * SLOT>(m6);
    f32x4 gram;
    float rstdh, negmuh;
    const unsigned hrow = n16 < 8 ? n16 : 0;
    auto halo_val = [&](auto I) {
      f32x4 r;
      sfor<4>([&](auto K) { const float a = aread<A_HH + 4 * (I - 2) + K>(); r[(int)K] = fmaf(ror8(a), negmuh, a) * rstdh; });
      return r;
    };
    auto halo_col = [&](auto I) { return ((qd[I] >> (8 * w)) & 255); };
    LAS char* hb[3];
#pragma unroll
    for (int i = 0; i < 3; ++i) { hb[i] = (LAS char*)(uintptr_t)((unsigned)(uintptr_t)rb[0][i] ^ (w << 5)); PIN(hb[i]); }
    const unsigned ha = (unsigned)(uintptr_t)lds + TAB_OFF + hrow * 128 + G * 8;
    const unsigned qrow = (unsigned)(uintptr_t)lds + QK_OFF + (32 * w + n16 + 8) * QK_RS + G * 8, vrow = (unsigned)(uintptr_t)lds + (32 * w + n16 + 8) * V_RS + G * 16;
    bf16x4 hl[2], hu[2];
    auto opa = [&](auto Tc, auto K) {
      constexpr int T = Tc, d = K / 4, j = K % 4, p = 2 * T + (d >> 2), tb = (d >> 1) & 1, hf = d & 1;
      if constexpr (p != 7) {
        const f32x4& a = acc[T & 1][d >> 2][tb];
        if constexpr (j == 0) t0 = i_mul(a[2 * hf], rstd[tb]);
        if constexpr (j == 1) t1 = i_mul(a[2 * hf + 1], rstd[tb]);
        if constexpr (p < 19) {
          if constexpr (j == 2) t0 = __uint_as_float(i_cvt(t0, t1));
          if constexpr (j == 3) awrite<A_Q + 4 * p + 2 * tb + hf>(__float_as_uint(t0));
        } else if constexpr (j >= 2) awrite<A_V + 8 * (p - 19) + 4 * tb + 2 * hf + j - 2>(__float_as_uint(j == 2 ? t0 : t1));
      }
    };
    sfor<28>([&](auto So) { sfor<18>([&](auto Si) {
      constexpr int s = 18 * So + Si, i = s / 72, u = s % 72;
      sfor<2>([&](auto H) {
        constexpr int h = H;
        mma1(IC(s), H);
        SB(); rd_a(IC(s + LA), H); SB();
        sync(IC(s), H);
        if constexpr (SC::g_set(s)) {
          constexpr int np = SC::g_stage(s + 3), p = SC::g_piece(s + 3);
          if constexpr (h == 0) set_m0<(np % 6) * SLOT + p * 1024>(m6); else gso = sadd<8 * p * 3456>(soA[np % 3]);
        }
        if constexpr (h == 0 && SC::g_stage(s) >= 0) glds3<SC::g_piece(s) % 3>(vA[SC::g_piece(s) % 3], srdA, gso);
        if constexpr (SC::nop(s) > 0) { constexpr int k0 = SC::epi_from(s, h), k1 = SC::epi_to(s, h); sfor<k1 - k0>([&](auto I) { opa(IC(s / 36 - 1), IC(k0 + I)); }); }
        SB();
      });
      if constexpr (u % 4 == 2) {
        constexpr int sg = u / 4;
        if constexpr (s >= 46 && s <= 114) mfma16_aa<s == 46, A_HX + s - 46>(gram);
        if constexpr (i >= 2) mfma16_ava<sg == 0, A_HH + 4 * (i - 2), A_HX + 4 * sg>(cat(hl[sg & 1], hu[sg & 1]));
        SB();
        if constexpr (s >= 22 && s < 94) { constexpr int f = (s - 22) / 4; ds_read_a64<A_HX + 4 * f, (f >> 1) * 1024 + (f & 1) * 64>(ha); ds_read_a64<A_HX + 4 * f + 2, (f >> 1) * 1024 + (f & 1) * 64 + 32>(ha); }
        constexpr int s2 = s + 8, sl = (3 * (s2 / 72) + (s2 % 72) / 24) % 6;
        if constexpr (s2 >= 144 && s2 < 504) { LAS char* p = hb[sl >> 1] + (sl & 1) * SLOT + (((s2 % 72) / 4) % 6) * 4096; hl[sg & 1] = rdhalf(p); hu[sg & 1] = rdhalf(p + 2048); }
        if constexpr (s == 122) {
          const float dg = (p4 == 0) ? gram[0] : (p4 == 1) ? gram[1] : (p4 == 2) ? gram[2] : gram[3];
          rstdh = G == q4 ? dg : 0.f; negmuh = G == 2 ? gram[0] : 0.f;
          PIN(rstdh); PIN(negmuh);
        }
        if constexpr (s == 126) { rstdh = xor16(rstdh); negmuh = xor16(negmuh); PIN(rstdh); PIN(negmuh); }
        if constexpr (s == 130) { rstdh = xor32(rstdh); negmuh = xor32(negmuh); PIN(rstdh); PIN(negmuh); }
        if constexpr (s == 134) {
          negmuh *= -1.0f / D_;
          rstdh = __builtin_amdgcn_rsqf(fmaxf(rstdh * (1.0f / D_) - negmuh * negmuh, 0.f) + 1e-5f);
          PIN(rstdh); PIN(negmuh);
        }
      }
      constexpr int k = SC::warm_k(s);
      if constexpr (SC::mark_k(s) >= 0) mark<1, SC::mark_k(s)>();
      if constexpr (k >= 0 && k < 4) {
        const unsigned la = (288 * kg + ((qs[k < 3 ? 2 + k : 5] >> (k == 2 ? 16 : 0)) & 255) * 16) & ~127u, lb = (288 * kg + (k < 3 ? qs[2 + k] >> 24 : qs[6] & 255) * 16) & ~127u;
        warm(((wa0 >> 1) * 3456 + ((wa0 & 1) ? lb : la)) | wm0, srdA, 0);
      }
      if constexpr (k == 4) warm(((rt * 81 + w * 21 + lane) * 128) | (lane < 21 ? 0 : OOB), srdWo, 144 * kg * 1152);
      if constexpr (s == 2) *(LAS u32x4*)(lds + TAB_OFF + w * 3072 + (r8 < 3 ? r8 : 0) * 1024 + pos * 16) = u32x4{0x3f803f80u, 0x3f803f80u, 0x3f803f80u, 0x3f803f80u};
      if constexpr (u == 47 && i < 5) { vA[0] = va_of(qs[i + 2]); vA[1] = vA[0] + 8 * 3456 - 1024; vA[2] = vA[0] + 2 * (8 * 3456 - 1024); PIN(vA[0]); PIN(vA[1]); PIN(vA[2]); }
      if constexpr (s > 432 && s < 471 && s % 2 == 1 && s != 447) {
        constexpr int p = (s - 433) / 2;
        const unsigned ad = qrow + ((qd[p / 4] >> (8 * (p % 4))) & 255) * 16;
        ds_write_a64<A_Q + 4 * p, 0>(ad); ds_write_a64<A_Q + 4 * p + 2, 16 * QK_RS>(ad);
      }
      if constexpr (s == 471 || s == 473 || s == 477) {
        constexpr int hi = s == 471 ? 2 : s == 473 ? 3 : 4;
        const f32x4 r = halo_val(IC(hi));
        const unsigned row = (hi == 4 && w == 3) ? 0 : hrow;
        *(LAS u32x2*)(lds + QK_OFF + row * QK_RS + halo_col(IC(hi)) * 16 + G * 8) = u32x2{cvtpk(r[0], r[1]), cvtpk(r[2], r[3])};
      }
      SB();
    }); });
    asm volatile("s_nop 7" : "+v"(acc[1][0][0]), "+v"(acc[1][0][1]), "+v"(acc[1][1][0]), "+v"(acc[1][1][1]));
    WAITL0(); BAR();
    {
      const int hd = lane & 7, i0 = 32 * w + 4 * r8;
      LAS char* rp[11];
#pragma unroll
      for (int jj = 0; jj < 11; ++jj) { rp[jj] = lds + QK_OFF + (i0 + 1 + jj) * QK_RS + hd * 36; PIN(rp[jj]); }
      unsigned qv[4][9], kv[11][9];
#pragma unroll
      for (int a = 0; a < 4; ++a)
#pragma unroll
        for (int c = 0; c < 9; ++c) qv[a][c] = *(LAS unsigned*)(rp[7 + a] + c * 4);
#pragma unroll
      for (int jj = 0; jj < 4; ++jj)
#pragma unroll
        for (int c = 0; c < 9; ++c) kv[jj][c] = *(LAS unsigned*)(rp[jj] + 288 + c * 4);
      {
#pragma unroll
        for (int k = 0; k < 4; ++k)
          *(LAS f32x4*)(uintptr_t)(vrow + ((qd[6] >> (16 + 8 * (k >> 1))) & 255) * 32 + (k & 1) * 16 * V_RS) = acc[1][k >> 1][k & 1] * rstd[k & 1];
        sfor<3>([&](auto I) {
          constexpr int hi = I + 4;
          const unsigned row = (hi == 4 && w != 3) ? 0 : hrow, col = (hi == 4 && w != 3) ? 0 : halo_col(IC(hi));
          *(LAS f32x4*)(lds + row * V_RS + col * 32 + G * 16) = halo_val(IC(hi));
        });
      }
      SB();
      float lg[4][8];
      sfor<8>([&](auto U) {
        constexpr int u = U;
        if constexpr (u + 4 < 11) {
#pragma unroll
          for (int c = 0; c < 9; ++c) kv[u + 4][c] = *(LAS unsigned*)(rp[u + 4] + 288 + c * 4);
        }
        if constexpr (u < 7) {
          constexpr int p = 19 + u;
          const unsigned ad = vrow + ((qd[p / 4] >> (8 * (p % 4))) & 255) * 32;
          ds_write_a128<A_V + 8 * u, 0>(ad); ds_write_a128<A_V + 8 * u + 4, 16 * V_RS>(ad);
        }
#pragma unroll
        for (int a = 0; a < 4; ++a) lg[a][u] = 0.f;
#pragma unroll
        for (int c = 0; c < 9; ++c)
#pragma unroll
          for (int a = 0; a < 4; ++a) lg[a][u] = dot2(qv[a][c], kv[a + u][c], lg[a][u]);
        SB();
      });
      WAITL0(); BAR();
      mark<1, 7>();
      LAS char* vp = lds + (i0 + 1) * V_RS + hd * 72;
      f32x2 vv[11][9];
#pragma unroll
      for (int jj = 0; jj < 3; ++jj)
#pragma unroll
        for (int c = 0; c < 9; ++c) vv[jj][c] = *(volatile LAS f32x2*)(vp + jj * V_RS + c * 8);
      if (rt < 2) {
#pragma unroll
        for (int a = 0; a < 4; ++a)
#pragma unroll
          for (int u = 0; u < 7 - a; ++u) lg[a][u] = (i0 + a + u < 7) ? -1e9f : lg[a][u];
      }
      constexpr float CS = 0.23570226039551584f * 1.4426950408889634f;
#pragma unroll
      for (int a = 0; a < 4; ++a) {
        float mx = lg[a][0];
#pragma unroll
        for (int u = 1; u < 8; ++u) mx = fmaxf(mx, lg[a][u]);
        const float nb = -mx * CS;
        float sum = 0.f;
#pragma unroll
        for (int u = 0; u < 8; ++u) { lg[a][u] = __builtin_amdgcn_exp2f(fmaf(lg[a][u], CS, nb)); sum += lg[a][u]; }
        const float inv = __builtin_amdgcn_rcpf(sum);
#pragma unroll
        for (int u = 0; u < 8; ++u) lg[a][u] *= inv;
      }
      SB();
      f32x2 o[4][9];
      sfor<11>([&](auto J) {
        constexpr int jj = J;
        if constexpr (jj + 3 < 11) {
#pragma unroll
          for (int c = 0; c < 9; ++c) vv[jj + 3][c] = *(volatile LAS f32x2*)(vp + (jj + 3) * V_RS + c * 8);
        }
#pragma unroll
        for (int c = 0; c < 9; ++c) {
#pragma unroll
          for (int a = 0; a < 4; ++a) {
            if (jj == a) o[a][c] = vv[jj][c] * lg[a][0];
            else if (jj > a && jj <= a + 7) o[a][c] = __builtin_elementwise_fma(vv[jj][c], f32x2{lg[a][jj - a], lg[a][jj - a]}, o[a][c]);
          }
        }
        if constexpr (jj == 7) mark<1, 8>();
        if constexpr (jj >= 7) {
#pragma unroll
          for (int c = 0; c < 9; ++c) *(LAS unsigned*)(rp[jj] + c * 4) = cvtpk(o[jj - 7][c][0], o[jj - 7][c][1]);
        }
        SB();
      });
    }
    {
      LAS char* rowp = (LAS char*)(uintptr_t)qrow;
#pragma unroll
      for (int f = 0; f < 9; ++f) {
        const u32x2 a = *(LAS u32x2*)(rowp + (f >> 1) * 64 + (f & 1) * 16 * QK_RS), b = *(LAS u32x2*)(rowp + (f < 8 ? (f >> 1) * 64 + (f & 1) * 16 * QK_RS + 32 : 256 + 16 * QK_RS));
        __builtin_amdgcn_raw_buffer_store_b128(u32x4{a[0], a[1], b[0], b[1]}, rws, l16, oso + f * 1024, 16);
      }
    }
    WAITV(0); BAR();
    bstore8_sc1(get_tag(), vflag, srdws, flo + (FL_O + kg) * 128);
  }
  asm volatile(".Lk_end1:");
}

extern "C" void kernel_launch(void* const* d_in, const int* in_sizes, int n_in, void* d_out, int out_size, void* d_ws, size_t ws_size, hipStream_t stream) {
  (void)hipFuncSetAttribute((const void*)main_k, hipFuncAttributeMaxDynamicSharedMemorySize, LDS_BYTES);
  const Args args = {d_in[0], d_in[1], d_in[2], d_in[4], d_in[5], d_in[6], d_in[7], d_out, d_ws, make_tab()};
  main_k<<<256, 256, LDS_BYTES, stream>>>(args);
}
```

```cpp
#include <hip/hip_runtime.h>
#include <stdint.h>
#include <utility>

typedef __bf16 bf16x8 __attribute__((ext_vector_type(8)));
typedef __bf16 bf16x4 __attribute__((ext_vector_type(4)));
typedef __bf16 bf16x2 __attribute__((ext_vector_type(2)));
typedef float f32x4 __attribute__((ext_vector_type(4)));
typedef float f32x2 __attribute__((ext_vector_type(2)));
typedef unsigned u32x4 __attribute__((ext_vector_type(4)));
typedef unsigned u32x2 __attribute__((ext_vector_type(2)));
typedef unsigned u32x16 __attribute__((ext_vector_type(16)));
typedef unsigned long long u64;
#define LAS __attribute__((address_space(3)))
#define GAS __attribute__((address_space(1)))
typedef GAS u64 gu64;
#define DI __device__ __forceinline__
#define SB() __builtin_amdgcn_sched_barrier(0)
#define IC(n) std::integral_constant<int, (n)>{}
#define RLX_AGENT __ATOMIC_RELAXED, __HIP_MEMORY_SCOPE_AGENT

template <class F, int... I> DI void sfor_(F&& f, std::integer_sequence<int, I...>) { (f(std::integral_constant<int, I>{}), ...); }
template <int N, class F> DI void sfor(F&& f) { sfor_(f, std::make_integer_sequence<int, N>{}); }

constexpr int S_ = 2048, D_ = 576, F_ = 4608;
constexpr int SLOT = 24576;
constexpr int TAB_OFF = 147456;
constexpr int LDS_BYTES = 163840;
constexpr int QK_OFF = 80512, QK_RS = 584, V_RS = 592;
constexpr unsigned H_OFF = 0, O_OFF = 20u << 20, P_OFF = 24u << 20, FLAG_OFF = 40u << 20, WS_BYTES = 41u << 20;
constexpr unsigned OOB = 0x40000000u;
constexpr int FL_HA = 0, FL_HB = 12, FL_O = 24, FL_FIN = 28, FL_EPOCH = 40, FL_N = 41;
constexpr int A_H = 56;
constexpr int A_Q = 16;
constexpr int A_V = 92;
constexpr int A_HH = 164;
constexpr int A_HX = 184;

DI int map_role(int bid) { return (bid >> 3) & 3; }
DI int map_kg(int bid) { return bid & 3; }
DI int map_rt(int bid) { return ((bid >> 2) & 1) * 8 + (bid >> 5); }

DI u32x4 make_srd(const void* p, unsigned bytes) {
  uint64_t a = (uint64_t)p;
  u32x4 r; r[0] = (unsigned)a; r[1] = (unsigned)(a >> 32) & 0xffffu; r[2] = bytes; r[3] = 0x00020000u;
  return r;
}
template <unsigned MC, unsigned SC> DI void glds16c(unsigned mb, unsigned voff, u32x4 srd, unsigned sb) {
  unsigned tmp;
  asm volatile("s_add_u32 m0, %1, %5\n\ts_add_u32 %0, %4, %6\n\tbuffer_load_dwordx4 %2, %3, %0 offen lds"
               : "=&s"(tmp) : "s"(mb), "v"(voff), "s"(srd), "s"(sb), "i"(MC), "i"(SC) : "memory", "scc");
}
template <unsigned C> DI void set_m0(unsigned b) { asm volatile("s_add_u32 m0, %0, %1" :: "s"(b), "i"(C) : "scc"); }
template <unsigned C> DI unsigned sadd(unsigned b) { unsigned r; asm volatile("s_add_u32 %0, %1, %2" : "=s"(r) : "s"(b), "i"(C) : "scc"); return r; }
template <int J> DI void glds3(unsigned v, u32x4 srd, unsigned so) { asm volatile("buffer_load_dwordx4 %0, %1, %2 offen offset:%c3 lds" :: "v"(v), "s"(srd), "s"(so), "i"(J * 1024) : "memory"); }
template <unsigned MC> DI void glds16r(unsigned mb, unsigned voff, u32x4 srd, unsigned& so, unsigned step) {
  asm volatile("s_add_u32 m0, %1, %5\n\ts_add_u32 %0, %0, %4\n\tbuffer_load_dwordx4 %2, %3, %0 offen lds"
               : "+s"(so) : "s"(mb), "v"(voff), "s"(srd), "s"(step), "i"(MC) : "memory", "scc");
}
constexpr int x_base(int t) { return (t / 3 == 1 ? 2 : 4) * SLOT + (t % 3 == 2 ? SLOT : 0); }
template <int T> DI void x_line(unsigned m6, unsigned vx0, unsigned vx1, u32x4 srd, unsigned s0, unsigned s1, unsigned s2, unsigned s3) {
  constexpr int d0 = x_base(T) + (T % 3 == 1 ? 4096 : 0) - (T % 3) * 128, d2 = T % 3 == 1 ? SLOT - 1024 : 1024;
  asm volatile("s_add_u32 m0, %0, %8\n\ts_nop 0\n\tbuffer_load_dwordx4 %1, %3, %4 offen offset:%c10 lds\n\t"
               "s_addk_i32 m0, 0x400\n\ts_nop 0\n\tbuffer_load_dwordx4 %2, %3, %5 offen offset:%c10 lds\n\t"
               "s_addk_i32 m0, %c9\n\ts_nop 0\n\tbuffer_load_dwordx4 %1, %3, %6 offen offset:%c10 lds\n\t"
               "s_addk_i32 m0, 0x400\n\ts_nop 0\n\tbuffer_load_dwordx4 %2, %3, %7 offen offset:%c10 lds"
               :: "s"(m6), "v"(vx0), "v"(vx1), "s"(srd), "s"(s0), "s"(s1), "s"(s2), "s"(s3), "i"(d0), "i"(d2), "i"((T % 3) * 128) : "memory", "scc");
}
DI u32x4 bload16(unsigned voff, u32x4 srd, unsigned soff) {
  u32x4 r;
  asm volatile("buffer_load_dwordx4 %0, %1, %2, %3 offen" : "=v"(r) : "v"(voff), "s"(srd), "s"(soff) : "memory");
  return r;
}
DI void warm(unsigned voff, u32x4 srd, unsigned soff) {
  asm volatile("buffer_load_dword a11, %0, %1, %2 offen" :: "v"(voff), "s"(srd), "s"(soff) : "memory");
}
DI u64 bload8_sc1(unsigned voff, u32x4 srd, unsigned soff) {
  u64 r;
  asm volatile("buffer_load_dwordx2 %0, %1, %2, %3 offen sc1" : "=v"(r) : "v"(voff), "s"(srd), "s"(soff) : "memory");
  return r;
}
template <int AR, int OFF> DI void aload16_sc1(unsigned voff, u32x4 srd, unsigned soff) {
  asm volatile("buffer_load_dwordx4 a[%c3:%c4], %0, %1, %2 offen offset:%c5 sc1" :: "v"(voff), "s"(srd), "s"(soff), "i"(AR), "i"(AR + 3), "i"(OFF) : "memory");
}
template <int ROLE, int K> DI void mark() { asm volatile("s_branch 1f\n\t.p2align 12\n.Lm%c0_%c1:\n\ts_setpc_b64 vcc\n1:" :: "i"(ROLE), "i"(K)); }
template <int R, int N0, int N1> DI void visit(unsigned sel) {
  constexpr auto c = [](int k, int n) { return k < n ? k : n - 1; };
  asm volatile("s_getpc_b64 vcc\n\ts_add_u32 s90, vcc_lo, %0\n\ts_addc_u32 s91, vcc_hi, 0\n\ts_add_u32 vcc_lo, vcc_lo, 52\n\ts_addc_u32 vcc_hi, vcc_hi, 0\n\ts_setpc_b64 s[90:91]\n\t"
               "s_branch .Lm0_%c1\n\ts_branch .Lm0_%c2\n\ts_branch .Lm0_%c3\n\ts_branch .Lm0_%c4\n\ts_branch .Lm1_%c5\n\ts_branch .Lm1_%c6\n\ts_branch .Lm1_%c7\n\ts_branch .Lm1_%c8"
               :: "s"(sel + 20), "i"(c(4 * R, N0)), "i"(c(4 * R + 1, N0)), "i"(c(4 * R + 2, N0)), "i"(c(4 * R + 3, N0)),
                  "i"(c(4 * R, N1)), "i"(c(4 * R + 1, N1)), "i"(c(4 * R + 2, N1)), "i"(c(4 * R + 3, N1)) : "vcc", "s90", "s91", "scc");
}
template <int N> DI void waitv() { asm volatile("s_waitcnt vmcnt(%c0)" :: "i"(N > 63 ? 63 : N) : "memory"); }
#define WAITV(n) asm volatile("s_waitcnt vmcnt(" #n ")" ::: "memory")
#define BAR() asm volatile("s_barrier" ::: "memory")
#define WAITL0() asm volatile("s_waitcnt lgkmcnt(0)" ::: "memory")
#define PIN(x) asm volatile("" : "+v"(x))
#define PINS(x) asm volatile("" : "+s"(x))

DI unsigned cvtpk(float lo, float hi) { f32x2 v = {lo, hi}; bf16x2 b = __builtin_convertvector(v, bf16x2); return __builtin_bit_cast(unsigned, b); }
DI float bflo(unsigned u) { return __uint_as_float(u << 16); }
DI float bfhi(unsigned u) { return __uint_as_float(u & 0xffff0000u); }
DI float dot2(unsigned a, unsigned b, float c) { return __builtin_amdgcn_fdot2_f32_bf16(__builtin_bit_cast(bf16x2, a), __builtin_bit_cast(bf16x2, b), c, false); }
DI float xor32(float v) { auto r = __builtin_amdgcn_permlane32_swap(__float_as_uint(v), __float_as_uint(v), false, false); return __uint_as_float(r[0]) + __uint_as_float(r[1]); }
DI float xor16(float v) { auto r = __builtin_amdgcn_permlane16_swap(__float_as_uint(v), __float_as_uint(v), false, false); return __uint_as_float(r[0]) + __uint_as_float(r[1]); }

DI bf16x4 rdhalf(LAS char* p) { return __builtin_amdgcn_ds_read_tr16_b64_v4bf16((LAS bf16x4*)p); }
DI bf16x8 cat(bf16x4 a, bf16x4 b) { return __builtin_shufflevector(a, b, 0, 1, 2, 3, 4, 5, 6, 7); }
template <bool FIRST> DI void mfma_vv(f32x4& acc, bf16x8 a, u32x4 b) {
  if constexpr (FIRST) asm volatile("v_mfma_f32_16x16x32_bf16 %0, %1, %2, 0" : "=&v"(acc) : "v"(a), "v"(b));
  else                 asm volatile("v_mfma_f32_16x16x32_bf16 %0, %1, %2, %0" : "+v"(acc) : "v"(a), "v"(b));
}
DI void mfma_vvc(f32x4& acc, u32x4 a, u32x4 b, const f32x4& c) { asm volatile("v_mfma_f32_16x16x32_bf16 %0, %1, %2, %3" : "=&v"(acc) : "v"(a), "v"(b), "v"(c)); }
template <bool FIRST, int AR> DI void mfma_va(f32x4& acc, bf16x8 a) {
  if constexpr (FIRST) asm volatile("v_mfma_f32_16x16x32_bf16 %0, %1, a[%c2:%c3], 0" : "=&v"(acc) : "v"(a), "i"(AR), "i"(AR + 3));
  else                 asm volatile("v_mfma_f32_16x16x32_bf16 %0, %1, a[%c2:%c3], %0" : "+v"(acc) : "v"(a), "i"(AR), "i"(AR + 3));
}
template <int AR> DI void mfma_k16(f32x4& acc, bf16x4 a) { asm volatile("v_mfma_f32_16x16x16_bf16 %0, %1, a[%c2:%c3], %0" : "+v"(acc) : "v"(a), "i"(AR), "i"(AR + 1)); }
template <bool FIRST, int AR> DI void mfma16_aa(f32x4& acc) {
  if constexpr (FIRST) asm volatile("v_mfma_f32_16x16x32_bf16 %0, a[%c1:%c2], a[%c1:%c2], 0" : "=&v"(acc) : "i"(AR), "i"(AR + 3));
  else                 asm volatile("v_mfma_f32_16x16x32_bf16 %0, a[%c1:%c2], a[%c1:%c2], %0" : "+v"(acc) : "i"(AR), "i"(AR + 3));
}
template <bool FIRST, int AD, int AR> DI void mfma16_ava(bf16x8 a) {
  if constexpr (FIRST) asm volatile("v_mfma_f32_16x16x32_bf16 a[%c1:%c2], %0, a[%c3:%c4], 0" :: "v"(a), "i"(AD), "i"(AD + 3), "i"(AR), "i"(AR + 3));
  else                 asm volatile("v_mfma_f32_16x16x32_bf16 a[%c1:%c2], %0, a[%c3:%c4], a[%c1:%c2]" :: "v"(a), "i"(AD), "i"(AD + 3), "i"(AR), "i"(AR + 3));
}
template <int AR> DI float aread(void) { float r; asm volatile("v_accvgpr_read_b32 %0, a[%c1]" : "=v"(r) : "i"(AR)); return r; }
template <int AR, int OFF> DI void ds_write_a128(unsigned addr) { asm volatile("ds_write_b128 %0, a[%c1:%c2] offset:%c3" :: "v"(addr), "i"(AR), "i"(AR + 3), "i"(OFF) : "memory"); }
DI float ror8(float v) { return __int_as_float(__builtin_amdgcn_update_dpp(0, __float_as_int(v), 0x128, 0xf, 0xf, false)); }
template <int AR> DI void awrite(unsigned v) { asm volatile("v_accvgpr_write_b32 a[%c1], %0" :: "v"(v), "i"(AR)); }
DI float i_fma(float a, float b, float c) { float r; asm volatile("v_fma_f32 %0, %1, %2, %3" : "=v"(r) : "v"(a), "v"(b), "v"(c)); return r; }
DI float i_mul(float a, float b) { float r; asm volatile("v_mul_f32_e32 %0, %1, %2" : "=v"(r) : "v"(a), "v"(b)); return r; }
DI float i_sqs(float a) { float r; asm volatile("v_mul_f32_e64 %0, %1, |%1|" : "=v"(r) : "v"(a)); return r; }
DI unsigned i_cvt(float a, float b) { unsigned r; asm volatile("v_cvt_pk_bf16_f32 %0, %1, %2" : "=v"(r) : "v"(a), "v"(b)); return r; }
DI unsigned i_relu(unsigned a) { unsigned r; asm volatile("v_pk_max_i16 %0, %1, 0" : "=v"(r) : "v"(a)); return r; }
DI void i_swap16(unsigned& a, unsigned& b) { asm volatile("v_permlane16_swap_b32 %0, %1" : "+v"(a), "+v"(b)); }
template <int OFF> DI void st16_sc1(u32x4 v, unsigned voff, u32x4 srd, unsigned soff) {
  asm volatile("buffer_store_dwordx4 %0, %1, %2, %3 offen offset:%c4 sc1" :: "v"(v), "v"(voff), "s"(srd), "s"(soff), "i"(OFF) : "memory");
}
template <int AR, int OFF> DI void ds_read_a64(unsigned addr) { asm volatile("ds_read_b64 a[%c1:%c2], %0 offset:%c3" :: "v"(addr), "i"(AR), "i"(AR + 1), "i"(OFF) : "memory"); }
DI void bstore8_sc1(u64 v, unsigned voff, u32x4 srd, unsigned soff) { asm volatile("buffer_store_dwordx2 %0, %1, %2, %3 offen sc1" :: "v"(v), "v"(voff), "s"(srd), "s"(soff) : "memory"); }
template <int AR, int OFF> DI void ds_write_a64(unsigned addr) { asm volatile("ds_write_b64 %0, a[%c1:%c2] offset:%c3" :: "v"(addr), "i"(AR), "i"(AR + 1), "i"(OFF) : "memory"); }

constexpr int LA = 7;
constexpr int WP1 = 4, WP2 = 2;
constexpr int NB = LA + 1, BMID = (23 - LA) & ~1;
template <bool ATTN> struct Sched {
  static constexpr int P1 = ATTN ? 504 : 432, NSTEP = ATTN ? 504 : 924, NSTAGE = ATTN ? 21 : 39;
  static constexpr int a_stage(int s) { return s < P1 ? 3 * (s / 72) + (s % 36) / 12 : s < 864 ? s / 24 : 36 + (s - 864) / 20; }
  static constexpr int a_tile(int s) { return s < P1 ? (s % 72) / 36 : s < 864 ? (s % 24) / 12 : ((s - 864) % 20) / 10; }
  static constexpr int a_ks(int s) { return s < 864 ? s % 12 : (s - 864) % 10; }
  static constexpr bool skip(int s) { return ATTN && s >= 108 && s < 144 && s % 2 == 1; }
  static constexpr int g_stage(int s) {
    if (s < 0) return (s + 24) / 6;
    if (s >= 864 || s % 4 != 1) return -1;
    const int st = s / 24 + 4;
    return st >= NSTAGE || (st >= 36 && (s % 24) / 4 == 5) ? -1 : st;
  }
  static constexpr int g_piece(int s) { return (s % 24) / 4; }
  static constexpr int g_last(int st) { int r = -25; for (int s = -24; s < NSTEP; ++s) if (g_stage(s) == st) r = s; return r; }
  static constexpr int warm_k(int s) { return s >= 0 && s % 48 == 0 && s / 48 < 5 ? s / 48 : -1; }
  static constexpr int PUB_HA = 288 + BMID, PUB_HB = 528, POLL_HA = 388, CHK_HA = 432, POLL_HB = 588, CHK_HB = 624, POLL_O = 812, CHK_O = 840;
  static constexpr int h_load(int s) {
    if (ATTN || s % 4 != 3) return -1;
    return s >= 432 && s < 480 ? (s - 432) / 4 : s >= 528 && s < 576 ? 12 + (s - 528) / 4 : s >= 624 && s < 720 ? 24 + (s - 624) / 4 : -1;
  }
  static constexpr int o_load(int s) { return !ATTN && s > CHK_O && s <= CHK_O + 9 ? s - CHK_O - 1 : -1; }
  static constexpr int x_load(int s) { return !ATTN && s >= 723 && s < 771 && s % 16 == 3 ? (s - 723) / 16 : -1; }
  static constexpr int NMARK = ATTN ? 9 : 11;
  static constexpr int mark_k(int s) {
    constexpr int M[] = {2, 84, 162, 240, 318, 396, 474, 574, 674, 774, 876}, A[] = {16, 98, 190, 268, 348, 430, 496};
    if (ATTN) { for (int k = 0; k < 7; ++k) if (s == A[k]) return k; }
    else { for (int k = 0; k < 11; ++k) if (s == M[k]) return k; }
    return -1;
  }
  static constexpr bool bar(int s) { return s < P1 ? (s % 72 == 0 || s % 72 == BMID || (s % 72 == 48 && s < 480)) : (s <= 864 && s % 24 == 0); }
  static constexpr bool lwait(int s) { return (s + 1) % (s < 468 && !ATTN ? WP1 : WP2) == 0; }
  static constexpr int bias_rd(int s) { return !ATTN && s < 432 ? (s % 36 == 28 ? 0 : s % 36 == 35 ? 1 : -1) : -1; }
  static constexpr bool g_set(int s) { return g_stage(s + 3) >= 0 && g_piece(s + 3) % 3 == 0; }
  static constexpr bool fixed(int s, int h) {
    if (bar(s) || g_set(s)) return true;
    return h == 0 ? (g_stage(s) >= 0 || h_load(s) >= 0 || o_load(s) >= 0 || x_load(s) >= 0 || bias_rd(s) >= 0) : lwait(s);
  }
  static constexpr int ST0 = 30, ST1 = 57;
  static constexpr int win0(int s) { return s < 864 ? s / 36 * 36 : 864 + (s - 864) / 10 * 10; }
  static constexpr int wlen(int s) { return s < 864 ? 36 : 10; }
  static constexpr int nop(int s) { return ATTN ? (s >= 36 ? 32 : 0) : s >= 36 && s < 468 ? 59 : s >= 874 ? 14 : 0; }
  static constexpr int epi_to(int s, int h) {
    int f = 0, u = 0;
    for (int q = 2 * win0(s) + 2; q < 2 * (win0(s) + wlen(s)); ++q) if (!fixed(q / 2, q % 2)) { ++f; if (q <= 2 * s + h) ++u; }
    return f >= nop(s) ? (u < nop(s) ? u : nop(s)) : u * nop(s) / f;
  }
  static constexpr int epi_from(int s, int h) { return 2 * s + h <= 2 * win0(s) + 2 ? 0 : h ? epi_to(s, 0) : epi_to(s - 1, 1); }
  static constexpr bool has_op(int s, int k) { return epi_from(s, 0) <= k && k < epi_to(s, 1); }
  static constexpr int stores(int s) { return ATTN || s < 0 || !nop(s) ? 0 : s < 864 ? has_op(s, ST0) + has_op(s, ST1) : has_op(s, 10) + has_op(s, 13); }
  static constexpr int vm(int s) {
    int c = (g_stage(s) >= 0) + (warm_k(s) >= 0) + stores(s);
    if (!ATTN && s >= 0) c += (h_load(s) >= 0) + (o_load(s) >= 0) + (s == POLL_HA) + (s == POLL_HB) + (s == POLL_O) + (x_load(s) >= 0);
    return c;
  }
  static constexpr int need(int s) {
    int r = -25;
    if (bar(s)) r = g_last(s < P1 ? 3 * (s / 72) + (s % 72 == 0 ? 1 : s % 72 == BMID ? 2 : 3) : s < 864 ? s / 24 + 1 : 38);
    if (!ATTN) {
      const int e = s == PUB_HA || s == PUB_HB ? s - 1 : s == CHK_HA ? POLL_HA : s == CHK_HB ? POLL_HB : s == CHK_O ? POLL_O : s == 720 ? 671 : s == 768 ? 719 : s == 863 ? 862 : -25;
      if (e > r) r = e;
    }
    return r;
  }
  static constexpr int allow(int s) { int c = 0; for (int k = need(s) + 1; k < s; ++k) c += vm(k); return c; }
  static constexpr bool ring_ok(int st) {
    int first = NSTEP, last = -1, refill = NSTEP;
    for (int s = 0; s < NSTEP; ++s) {
      if (a_stage(s) == st) { if (s - LA < first) first = s - LA; last = s; }
      if (ATTN && s >= 144 && s % 4 == 2 && 3 * (s / 72) + (s % 72) / 24 == st) { if (s - 8 < first) first = s - 8; if (s + 1 > last) last = s + 1; }
      if (g_stage(s) == st + 6 && s < refill) refill = s;
    }
    bool vis = st == 0, fre = refill == NSTEP;
    for (int b = 0; b < NSTEP; b += 2) if (bar(b)) { if (b < first && b + 72 > first && need(b) >= g_last(st)) vis = true; if (b >= last && b < refill) fre = true; }
    return vis && fre;
  }
};
template <bool ATTN, int... I> constexpr bool rings_ok(std::integer_sequence<int, I...>) { return (std::bool_constant<Sched<ATTN>::ring_ok(I)>::value && ...); }
static_assert(rings_ok<false>(std::make_integer_sequence<int, 39>{}) && rings_ok<true>(std::make_integer_sequence<int, 21>{}), "LDS ring protocol");
constexpr int kb_areg(int kb) { return kb == 0 ? A_H : kb == 1 ? A_H + 48 : kb == 2 ? A_H + 96 : kb == 3 ? A_H + 144 : kb == 4 ? A_H : kb == 5 ? A_H + 48 : A_H + 96; }
constexpr int qkv_pair(int kg, int p) {
  constexpr signed char T[4][28] = {
    {0,2,4,6, 16,8,10,-1, 18,20,22,24, 26,28,30,32, 12,14,34,52, 36,38,40,42, 44,46,48,50},
    {6,8,10,12, 0,2,4,-1, 24,26,28,30, 14,16,32,34, 18,20,22,50, 42,44,46,48, 36,38,40,52},
    {4,6,8,10, 12,14,16,-1, 22,24,26,28, 0,2,18,20, 30,32,34,36, 40,42,44,46, 48,50,52,38},
    {2,4,6,8, 0,10,12,-1, 20,22,24,26, 28,30,32,34, 14,16,18,36, 38,40,42,44, 46,48,50,52}};
  return T[kg][p];
}
struct QkvTab { unsigned v[4][16]; };
constexpr QkvTab make_tab() {
  QkvTab t = {};
  for (int kg = 0; kg < 4; ++kg)
    for (int p = 0; p < 28; ++p) {
      const int v = qkv_pair(kg, p), sv = v < 0 ? qkv_pair(kg, p - 1) : v;
      t.v[kg][p / 4] |= (unsigned)((sv / 18) * 72 + sv % 18) << (8 * (p % 4));
      t.v[kg][8 + p / 4] |= (unsigned)(sv >= 36 ? sv - 36 : sv) << (8 * (p % 4));
    }
  return t;
}

DI void check_flag(u64 f, u64 tag, unsigned voff, u32x4 srd, unsigned soff) {
  unsigned n;
  asm volatile("v_cmp_ne_u64_e32 vcc, %1, %2\n\ts_cbranch_vccz 1f\n\ts_mov_b32 %0, 0x40000\n2:\n\ts_sleep 1\n\t"
               "buffer_load_dwordx2 %1, %3, %4, %5 offen sc1\n\ts_sub_u32 %0, %0, 1\n\ts_waitcnt vmcnt(0)\n\ts_cbranch_scc1 1f\n\t"
               "v_cmp_ne_u64_e32 vcc, %1, %2\n\ts_cbranch_vccnz 2b\n1:"
               : "=&s"(n), "+v"(f) : "v"(tag), "v"(voff), "s"(srd), "s"(soff) : "vcc", "scc", "memory");
}
DI void spin_until(gu64* p, u64 tag) {
#pragma unroll 1
  for (unsigned spins = 0; spins < (1u << 18); ++spins) {
    const u64 f = __hip_atomic_load(p, RLX_AGENT);
    if (__all(f == tag)) break;
  }
}

struct Args { const void *x, *wqkv, *wo, *w1, *b1, *w2, *b2; void *out, *ws; QkvTab qt; };
static_assert(sizeof(Args) == 72 + 256, "no padding");
DI char* as_ptr(unsigned lo, unsigned hi) { return (char*)(((u64)hi << 32) | lo); }

__global__ __launch_bounds__(256, 1) void main_k(const Args) {
  extern __shared__ __attribute__((aligned(16))) char smem[];
  LAS char* lds = (LAS char*)smem;
  const int tid = threadIdx.x, lane = tid & 63, bid = blockIdx.x;
  const int kg = map_kg(bid), cg = map_role(bid), rt = map_rt(bid);
  u32x16 ka, kt; u32x2 kw;
  asm volatile("s_load_dwordx16 %0, %3, 0x0\n\ts_load_dwordx2 %1, %3, 0x40\n\ts_load_dwordx16 %2, %3, %4 offset:0x48"
               : "=&s"(ka), "=&s"(kw), "=&s"(kt) : "s"(__builtin_amdgcn_kernarg_segment_ptr()), "s"(kg * 64));
  const void* code;
  unsigned code_bytes;
  asm volatile("s_getpc_b64 %0\n.Lk_pc:\n\ts_mov_b32 %1, .Lk_end0-.Lk_pc\n\ts_max_i32 %1, %1, .Lk_end1-.Lk_pc" : "=s"(code), "=s"(code_bytes) :: "scc", "a255");
  const int w = __builtin_amdgcn_readfirstlane(tid >> 6);
  const bool attn = cg == 3;
  const int tokbase = 256 * (rt >> 1) + (rt & 1);
  const int n16 = lane & 15, r8 = lane >> 3, pos = lane & 7;
  const int G = lane >> 4, q4 = (lane >> 2) & 3, p4 = lane & 3;

  const unsigned rot = (0x24924924u >> (2 * ((rt & 7) + cg))) & 3;
  unsigned kro[3] = {rot * 192, rot == 2 ? 0 : rot * 192 + 192, rot == 0 ? 384 : rot * 192 - 192};
  unsigned m6 = 6 * w * 1024, sx = tokbase * 1152 + w * 32 * 2304;
  unsigned vx0 = r8 * 2304 + ((pos ^ G) << 4), vx1 = vx0 ^ 64;
  PINS(m6); PINS(sx); PIN(vx0); PIN(vx1); PINS(kro[0]); PINS(kro[1]); PINS(kro[2]);
  const unsigned swz = pos ^ (((r8 >> 1) & 3) << 1);
  const int fs = 3 * kg + cg, f0 = fs * 384;
  unsigned strideA = attn ? 3456u : 9216u;
  PINS(strideA);
  unsigned soA[3];
#pragma unroll
  for (int j = 0; j < 3; ++j) { soA[j] = (kro[j] + 48 * w) * strideA + (attn ? 288 * kg : f0 * 2); PINS(soA[j]); }
  unsigned vab = r8 * strideA + (swz & 1) * 16, vsh = 8 * (swz >> 1), vcode = tid < 64 ? tid * 128 : OOB;
  PIN(vab); PIN(vsh); PIN(vcode);
  u32x4 id0, id1;
  {
    const unsigned one = 0x3f80u << (16 * (n16 & 1));
    const int e = (n16 >> 1) - 4 * G;
#pragma unroll
    for (int j = 0; j < 4; ++j) { id0[j] = e == j ? one : 0u; id1[j] = e + 8 == j ? one : 0u; }
  }
  PIN(id0); PIN(id1);
  unsigned po0 = (4 * G + q4) * 128 + (((2 * G + (q4 >> 1)) & 3) << 5) + p4 * 8;
  LAS char* rb[4][3];
#pragma unroll
  for (int i = 0; i < 3; ++i)
#pragma unroll
    for (int c = 0; c < 4; ++c) { rb[c][i] = lds + (po0 ^ (c << 5)) + 2 * i * SLOT; PIN(rb[c][i]); }
  LAS char* xp[2][2];
#pragma unroll
  for (int u = 0; u < 2; ++u) {
    const unsigned xr0 = w * 6144 + (n16 >> 3) * 1024 + (n16 & 7) * 128 + (((G ^ (n16 >> 1)) << 4) ^ (u << 6));
    xp[0][u] = lds + xr0 + 4 * SLOT; xp[1][u] = lds + xr0 + 2 * SLOT; PIN(xp[0][u]); PIN(xp[1][u]);
  }
  unsigned ones = 0x3f803f80u; PIN(ones);
  const int c1 = cg == 2 ? 0 : cg + 1, c2 = cg == 0 ? 2 : cg - 1;
  unsigned soB[3];
  soB[0] = (f0 + 48 * w) * 1152 + 384 * cg; soB[1] = ((3 * kg + c1) * 384 + 48 * w) * 1152 + 384 * cg; soB[2] = ((3 * kg + c2) * 384 + 48 * w) * 1152 + 384 * cg;
  const int p5 = w == 3 ? 13 : 5 * w;
  unsigned soO = (144 * kg + 8 * p5) * 1152 + 384 * cg, m5 = p5 * 1024;
  unsigned hso[3];
  hso[0] = H_OFF + ((rt * 12 + fs) * 4 + w) * 24576; hso[1] = H_OFF + ((rt * 12 + 3 * kg + c1) * 4 + w) * 24576; hso[2] = H_OFF + ((rt * 12 + 3 * kg + c2) * 4 + w) * 24576;
  unsigned oso = O_OFF + ((rt * 4 + kg) * 4 + w) * 9216;
  unsigned pso = P_OFF + (((rt * 3 + cg) * 4 + kg) * 4 + w) * 12288;
  unsigned os[3];
#pragma unroll
  for (int k = 0; k < 3; ++k) { const int un = 3 * kg + k; os[k] = (tokbase + 64 * w + 32 * (un & 1)) * 1152 + (192 * cg + 32 * (un >> 1)) * 2; }
#pragma unroll
  for (int k = 0; k < 3; ++k) { PINS(soB[k]); PINS(hso[k]); PINS(os[k]); }
  PINS(pso); PINS(oso); PINS(soO); PINS(m5);
  asm volatile("s_waitcnt lgkmcnt(0)" : "+s"(ka), "+s"(kw), "+s"(kt));
  const char *x = as_ptr(ka[0], ka[1]), *wo = as_ptr(ka[4], ka[5]), *b1 = as_ptr(ka[8], ka[9]), *w2 = as_ptr(ka[10], ka[11]), *b2 = as_ptr(ka[12], ka[13]);
  char *out = as_ptr(ka[14], ka[15]), *ws = as_ptr(kw[0], kw[1]);
  const u32x4 srdx = make_srd(x, S_ * D_ * 2);
  auto x_more = [&](auto T) { const unsigned b = sx + kro[T / 3] * 2; x_line<T>(m6, vx0, vx1, srdx, b, b + 8 * 2304, b + 16 * 2304, b + 24 * 2304); };
  sfor<3>(x_more);
  const unsigned vsel = 4 * w + (attn ? 16 : 0);
  visit<0, Sched<false>::NMARK, Sched<true>::NMARK>(vsel);
  u32x4 srdA = make_srd(attn ? as_ptr(ka[2], ka[3]) : as_ptr(ka[6], ka[7]), attn ? D_ * 3 * D_ * 2 : D_ * F_ * 2);
  PINS(srdA);
  unsigned qs[7], qd[7];
#pragma unroll
  for (int i = 0; i < 7; ++i) { qs[i] = kt[i]; qd[i] = kt[8 + i]; }
  auto va_of = [&](unsigned word) { return vab + ((word >> vsh) & 255) * 16; };
  unsigned va = va_of(attn ? qs[0] : 0x06040200u), so = soA[0] - 8 * strideA;
  auto w_piece = [&](auto K) {
    constexpr int n = K / 6, p = K % 6;
    if constexpr (p == 0 && n > 0) so = soA[n % 3] - 8 * strideA;
    if constexpr (K == 18) va = va_of(attn ? qs[1] : 0x0e0c0a08u);
    glds16r<n * SLOT + p * 1024>(m6, va, srdA, so, 8 * strideA);
  };
  sfor<6>(w_piece);
  warm(vcode, make_srd(code, code_bytes), ((bid >> 3) & 15) * 8192);
  visit<1, Sched<false>::NMARK, Sched<true>::NMARK>(vsel);
  const u32x4 srdws = make_srd(ws, WS_BYTES);
  const unsigned flo = FLAG_OFF + rt * FL_N * 128;
  auto misc_loads = [&]() {
    if (attn) {
      int ht = tokbase + 2 * (r8 - 8); ht = ht < 0 ? 0 : ht;
      const unsigned vh = ht * 1152 + pos * 16;
      const unsigned kb = ((0x24924924u >> (2 * (rot + w))) & 3) * 384 | (w == 3 ? OOB : 0);
      glds16c<TAB_OFF, 0>(w * 3072, vh, srdx, kb); glds16c<TAB_OFF + 1024, 128>(w * 3072, vh, srdx, kb); glds16c<TAB_OFF + 2048, 256>(w * 3072, vh, srdx, kb);
    } else {
      asm volatile("buffer_load_dword a14, %0, %1, 0 offen" :: "v"(tid * 4), "s"(make_srd(b1 + f0 * 2, 768)) : "memory");
      asm volatile("buffer_load_dword a15, %0, %1, 0 offen" :: "v"(tid * 4), "s"(make_srd(b2 + 192 * cg * 2, 384)) : "memory");
    }
    asm volatile("buffer_load_dwordx2 a[12:13], %0, %1, %2 offen sc1" :: "v"(0), "s"(srdws), "s"(flo + FL_EPOCH * 128) : "memory");
  };
  u32x4 xf[36];
  f32x4 nm[2];
  float rstd[2];
  {
    float sm[2] = {0.f, 0.f}, sq[2] = {0.f, 0.f};
    SB();
    sfor<10>([&](auto T) {
      constexpr int t = T;
      if constexpr (t == 0) x_more(IC(3));
      if constexpr (t + 4 < 9) x_more(IC(t + 4));
      if constexpr (t == 4) misc_loads();
      if constexpr (t == 0) visit<2, Sched<false>::NMARK, Sched<true>::NMARK>(vsel);
      if constexpr (t < 9) {
        waitv<4 * ((t + 4 > 8 ? 8 : t + 4) - t) + (t < 3 ? 7 : t >= 4 ? 3 : 0)>();
#pragma unroll
        for (int u = 0; u < 4; ++u) xf[4 * t + u] = *(LAS u32x4*)(xp[t / 3 == 1][u >> 1] + (t % 3 == 1 ? 4096 + (u & 1) * SLOT : (t % 3 == 2 ? SLOT : 0) + (u & 1) * 2048));
      }
      SB();
      if constexpr (t > 0) {
#pragma unroll
        for (int u = 0; u < 4; u += 2)
#pragma unroll
          for (int j = 0; j < 4; ++j) {
            const unsigned v0 = xf[4 * t - 4 + u][j], v1 = xf[4 * t - 3 + u][j];
            sm[0] = dot2(v0, ones, sm[0]); sm[1] = dot2(v1, ones, sm[1]); sq[0] = dot2(v0, v0, sq[0]); sq[1] = dot2(v1, v1, sq[1]);
          }
        PIN(sm[0]); PIN(sm[1]); PIN(sq[0]); PIN(sq[1]);
      }
      if constexpr (t == 8) sfor<6>([&](auto P) { w_piece(IC(6 + P)); });
      SB();
    });
#pragma unroll
    for (int tb = 0; tb < 2; ++tb) {
      const float negmu = xor32(xor16(sm[tb])) * (-1.0f / D_);
      nm[tb] = f32x4{negmu, negmu, negmu, negmu};
      rstd[tb] = __builtin_amdgcn_rsqf(fmaxf(xor32(xor16(sq[tb])) * (1.0f / D_) - negmu * negmu, 0.f) + 1e-5f);
    }
  }
  bf16x4 wl[NB], wu[NB];
  {
    f32x4 dl[3], dh[3];
    asm volatile("s_nop 1" : "+v"(nm[0]), "+v"(nm[1]));
    sfor<38>([&](auto B) {
      constexpr int b = B;
      if constexpr (b < 36) mfma_vvc(dl[b % 3], id0, xf[b], nm[b & 1]);
      if constexpr (b == 36) {
        asm volatile("s_waitcnt vmcnt(18)\n\ts_barrier" : "+v"(dl[1]), "+v"(dh[1]), "+v"(dl[2]), "+v"(dh[2]) : "v"(nm[0]), "v"(nm[1]) : "memory");
#pragma unroll
        for (int t = 0; t < LA; ++t) { wl[t] = rdhalf(rb[t & 1][0] + (t >> 1) * 4096); wu[t] = rdhalf(rb[t & 1][0] + (t >> 1) * 4096 + 2048); }
      }
      SB();
      if constexpr (b >= 2) { xf[b - 2][0] = cvtpk(dl[(b - 2) % 3][0], dl[(b - 2) % 3][1]); xf[b - 2][1] = cvtpk(dl[(b - 2) % 3][2], dl[(b - 2) % 3][3]); }
      SB();
      if constexpr (b < 36) mfma_vvc(dh[b % 3], id1, xf[b], nm[b & 1]);
      SB();
      if constexpr (b >= 2) { xf[b - 2][2] = cvtpk(dh[(b - 2) % 3][0], dh[(b - 2) % 3][1]); xf[b - 2][3] = cvtpk(dh[(b - 2) % 3][2], dh[(b - 2) % 3][3]); PIN(xf[b - 2]); }
      if constexpr (b % 2 == 0 && b < 24) w_piece(IC(12 + b / 2));
      SB();
    });
  }

  const u32x4 srdW2 = make_srd(w2, F_ * D_ * 2), srdWo = make_srd(wo, D_ * D_ * 2);
  const __amdgpu_buffer_rsrc_t rws = __builtin_amdgcn_make_buffer_rsrc(ws, 0, WS_BYTES, 0x00020000);
  gu64* flags = (gu64*)((char*)ws + flo);
  auto get_tag = [&]() __attribute__((always_inline)) { return (((u64)__float_as_uint(aread<13>()) << 32) | __float_as_uint(aread<12>())) * 0x9E3779B97F4A7C15ull + 0x632BE59BD9B4E019ull; };
  float t0, t1;
  f32x4 acc[2][2][2];
#define LANE_VALUES int tid_ = threadIdx.x; PIN(tid_); \
  const int tid = tid_, lane = tid & 63, n16 = lane & 15, r8 = lane >> 3, pos = lane & 7, G = lane >> 4, q4 = (lane >> 2) & 3, p4 = lane & 3; \
  const unsigned swz = pos ^ (((r8 >> 1) & 3) << 1), l16 = lane * 16, vflag = tid == 0 ? 0u : OOB, wa = rt * 72 + w * 18 + lane, wm = lane < 18 ? 0 : OOB; \
  (void)q4; (void)p4;

  if (!attn) {
    using SC = Sched<false>;
    LANE_VALUES
    constexpr bool attn_role = false;
    auto rd_a = [&](auto Sc, auto U) {
      constexpr int s = Sc, u = U;
      if constexpr (s < SC::NSTEP && !SC::skip(s) && !(u == 1 && s >= 864 && SC::a_ks(s) >= 8)) {
        constexpr int sl = SC::a_stage(s) % 6, ks = SC::a_ks(s);
        (u ? wu : wl)[s % NB] = rdhalf(rb[2 * SC::a_tile(s) + (ks & 1)][sl >> 1] + (sl & 1) * SLOT + (ks >> 1) * 4096 + u * 2048);
      }
    };
    auto mma1 = [&](auto Sc, auto H) {
      constexpr int s = Sc, k = s % 36, o = k & 1, tb = o ^ H;
      if constexpr (!SC::skip(s)) mfma_vv<k < 2>(acc[SC::a_tile(s)][o][tb], cat(wl[s % NB], wu[s % NB]), xf[(k & ~1) + tb]);
    };
    auto sync = [&](auto Sc, auto H) {
      constexpr int s = Sc, h = H;
      if constexpr (h == 0 && SC::need(s) > -25) waitv<SC::allow(s)>();
      if constexpr (h == 1 && SC::bar(s)) BAR();
      if constexpr (h == 1 && SC::lwait(s)) __builtin_amdgcn_s_waitcnt(0xC07F | ((2 * (LA - (s < 468 && !attn_role ? WP1 : WP2))) << 8));
    };
    unsigned vA[3] = {va - 128, va - 128 + 8 * 9216 - 1024, va - 128 + 2 * (8 * 9216 - 1024)}, vB[3], lk[3], gso = sadd<128>(soA[1]), hst = hso[0];
    set_m0<4 * SLOT>(m6);
    u64 tag;
    unsigned vpoll, pso_;
    LAS char* tab = lds + TAB_OFF + G * 16;
    f32x4 bs[2], bn;
    f32x4 ac[6][2][2];
    unsigned e[8];
    u64 pollv = 0;
    const unsigned ov = n16 * 2304 + (G & 1) * 32 + (G >> 1) * 16;
    u32x4 xr[3];
    float u0[2], u1[2];
    auto op1 = [&](auto Tc, auto K) {
      constexpr int T = Tc, k = K;
      if constexpr (k == SC::ST0 || k == SC::ST1) { constexpr int q = k == SC::ST1; st16_sc1<(T & 1) * 2048 + q * 1024>(u32x4{e[4 * q], e[4 * q + 1], e[4 * q + 2], e[4 * q + 3]}, l16, srdws, hst); }
      else if constexpr (k == 58) { if constexpr (T & 1) hst = sadd<4096>(hst); }
      else {
        constexpr int kk = k > SC::ST0 ? k - 1 : k, r = (kk - 4) % 7;
        constexpr int j = kk < 4 ? kk : kk >= 53 ? kk - 49 : r == 0 ? 4 : r == 1 ? 0 : r == 2 ? 5 : r == 3 ? 1 : r == 4 ? 2 : r == 5 ? 3 : 6;
        constexpr int d = kk < 4 ? 0 : kk >= 53 ? 7 : (kk - 4) / 7 + (j < 4), hf = d & 1;
        const f32x4& a = acc[T & 1][(d >> 1) & 1][d >> 2];
        if constexpr (j == 0) u0[hf] = i_fma(a[2 * hf], rstd[d >> 2], bs[(d >> 1) & 1][2 * hf]);
        if constexpr (j == 1) u1[hf] = i_fma(a[2 * hf + 1], rstd[d >> 2], bs[(d >> 1) & 1][2 * hf + 1]);
        if constexpr (j == 2) u0[hf] = i_sqs(u0[hf]);
        if constexpr (j == 3) u1[hf] = i_sqs(u1[hf]);
        if constexpr (j == 4) e[d] = i_cvt(u0[hf], u1[hf]);
        if constexpr (j == 5) e[d] = i_relu(e[d]);
        if constexpr (j == 6) awrite<A_H + 8 * T + d>(e[d]);
      }
    };
    auto op2 = [&](auto Tc, auto K) {
      constexpr int T = Tc, k = K;
      if constexpr (k < 8) e[k] = i_cvt(ac[T][(k >> 1) & 1][k >> 2][2 * (k & 1)], ac[T][(k >> 1) & 1][k >> 2][2 * (k & 1) + 1]);
      else {
        constexpr int q = (k - 8) / 3, j = (k - 8) % 3;
        if constexpr (j < 2) i_swap16(e[4 * q + j], e[4 * q + 2 + j]);
        else st16_sc1<(T & 1) * 2048 + q * 1024>(u32x4{e[4 * q], e[4 * q + 1], e[4 * q + 2], e[4 * q + 3]}, lk[T >> 1], srdws, pso);
      }
    };
    auto slot = [&](auto Sc, auto H) {
      constexpr int s = Sc, h = H;
      sync(Sc, H);
      if constexpr (SC::g_set(s)) {
        constexpr int np = SC::g_stage(s + 3), p = SC::g_piece(s + 3), kb = (np - 18) / 3, src = kb < 2 ? 0 : 1 + (kb & 1), half = kb < 2 ? kb : (kb >= 4);
        if constexpr (h == 0) set_m0<(np % 6) * SLOT + p * 1024>(np < 36 ? m6 : m5);
        else if constexpr (np < 18) gso = sadd<8 * p * 9216 + 128 * (np / 3)>(soA[np % 3]);
        else if constexpr (np < 36) gso = sadd<(192 * half + 8 * p) * 1152 + 128 * ((np - 18) % 3)>(soB[src]);
        else gso = sadd<8 * p * 1152 + 128 * (np - 36)>(soO);
      }
      if constexpr (h == 0) {
        if constexpr (SC::g_stage(s) >= 0) { constexpr int np = SC::g_stage(s), j = SC::g_piece(s) % 3; glds3<j>(np < 18 ? vA[j] : vB[j], np < 18 ? srdA : np < 36 ? srdW2 : srdWo, gso); }
        if constexpr (SC::bias_rd(s) == 0) bn = *(LAS f32x4*)(tab + (s / 36) * 128);
        if constexpr (SC::bias_rd(s) == 1) { bs[0] = bn; bs[1] = *(LAS f32x4*)(tab + (s / 36) * 128 + 64); }
        if constexpr (SC::x_load(s) >= 0) { constexpr int k = SC::x_load(s); xr[k] = bload16(ov, srdx, os[k]); }
        if constexpr (SC::o_load(s) >= 0) { constexpr int f = SC::o_load(s); aload16_sc1<kb_areg(6) + 4 * f, (f & 3) * 1024>(lk[f >> 2], srdws, oso); }
        if constexpr (SC::h_load(s) >= 0) { constexpr int b = SC::h_load(s) / 12, f = SC::h_load(s) % 12; aload16_sc1<kb_areg(2 + b) + 4 * f, (f & 3) * 1024>(lk[f >> 2], srdws, hso[1 + (b & 1)]); }
      }
      if constexpr (SC::nop(s) > 0) {
        static_assert(SC::bias_rd(s) != 1 || s < 36 || SC::epi_from(s, 0) > 52, "the bias registers are free");
        constexpr int k0 = SC::epi_from(s, h), k1 = SC::epi_to(s, h);
        sfor<k1 - k0>([&](auto I) { if constexpr (s < 864) op1(IC(s / 36 - 1), IC(k0 + I)); else op2(IC((s - 864) / 10 - 1), IC(k0 + I)); if constexpr (s < 864 && k0 + I == SC::ST0 && k0 + I + 1 < k1) asm volatile("s_nop 1"); });
      }
    };
    sfor<77>([&](auto So) { sfor<12>([&](auto Si) {
      constexpr int s = 12 * So + Si;
      sfor<2>([&](auto H) {
        if constexpr (s < 432) mma1(IC(s), H);
        else {
          constexpr int n = SC::a_stage(s) - 18, kb = n / 3, T = 2 * (n % 3) + SC::a_tile(s), ks = SC::a_ks(s), o = ks & 1, tb = o ^ H, ar = kb_areg(kb) + 8 * (ks >> 1);
          if constexpr (kb == 6 && ks >= 8) mfma_k16<ar + 2 * tb>(ac[T][o][tb], wl[s % NB]);
          else mfma_va<kb == 0 && ks < 2, ar + 4 * tb>(ac[T][o][tb], cat(wl[s % NB], wu[s % NB]));
        }
        SB(); rd_a(IC(s + LA), H); SB();
        slot(IC(s), H); SB();
      });
      constexpr int k = SC::warm_k(s);
      if constexpr (SC::mark_k(s) >= 0) mark<0, SC::mark_k(s)>();
      if constexpr (k >= 0 && k < 2) warm(((wa >> 1) * 9216 + (wa & 1) * 128) | wm, srdA, f0 * 2 + 256 + 256 * k);
      if constexpr (k >= 2) warm((wa * 128) | wm, srdW2, f0 * 1152 + (k - 2) * 147456);
      if constexpr (s == SC::PUB_HA || s == SC::PUB_HB) bstore8_sc1(tag, vflag, srdws, flo + ((s == SC::PUB_HA ? FL_HA : FL_HB) + fs) * 128);
      if constexpr (s == SC::CHK_HA || s == SC::CHK_HB || s == SC::CHK_O) check_flag(pollv, tag, vpoll, srdws, pso_);
      if constexpr (s == 4) { const unsigned r = __float_as_uint(aread<14>()); *(LAS f32x2*)(lds + TAB_OFF + tid * 8) = f32x2{bflo(r), bfhi(r)}; }
      if constexpr (s == 8) { const unsigned r = __float_as_uint(aread<15>()); *(LAS f32x2*)(lds + TAB_OFF + 2048 + tid * 8) = f32x2{bflo(r), bfhi(r)}; }
      if constexpr (s == 12) { tag = get_tag(); PIN(tag); }
      if constexpr (s == 334) { vB[0] = r8 * 1152 + swz * 16; vB[1] = vB[0] + 8192; vB[2] = vB[0] + 16384; PIN(vB[0]); PIN(vB[1]); PIN(vB[2]); }
      if constexpr (s == 386) { vpoll = (3 * kg + ((lane & 1) ? c1 : c2)) * 128; PIN(vpoll); }
      if constexpr (s == 432) { lk[0] = l16; lk[1] = l16 + 4096; lk[2] = l16 + 8192; PIN(lk[1]); PIN(lk[2]); }
      if constexpr (s == 600) { hso[1] += 12288; hso[2] += 12288; PINS(hso[1]); PINS(hso[2]); }
      if constexpr (s == SC::POLL_HA || s == SC::POLL_HB || s == SC::POLL_O) {
        if constexpr (s == SC::POLL_O) vpoll = kg * 128;
        pso_ = flo + (s == SC::POLL_HA ? FL_HA : s == SC::POLL_HB ? FL_HB : FL_O) * 128;
        pollv = bload8_sc1(vpoll, srdws, pso_);
      }
      SB();
    }); });
    asm volatile("s_nop 7" : "+v"(ac[5][0][0]), "+v"(ac[5][0][1]), "+v"(ac[5][1][0]), "+v"(ac[5][1][1]));
    sfor<14>([&](auto K) { op2(IC(5), K); SB(); });
    asm volatile("s_nop 1" : "+v"(xr[0]), "+v"(xr[1]), "+v"(xr[2]));
    SB();
    float a[3][8];
    f32x4 cb[3][2];
#pragma unroll
    for (int k = 0; k < 3; ++k) {
      LAS char* bp = lds + TAB_OFF + 2048 + 128 * ((3 * kg + k) >> 1) + (G & 1) * 64 + (G >> 1) * 32;
      cb[k][0] = *(LAS f32x4*)bp; cb[k][1] = *(LAS f32x4*)(bp + 16);
    }
    gu64* fp = flags + (FL_FIN + 3 * (lane & 3) + cg) * 16;
    PIN(fp);
    SB();
#pragma unroll
    for (int k = 0; k < 3; ++k) {
      const f32x4 c0 = cb[k][0], c1 = cb[k][1];
#pragma unroll
      for (int j = 0; j < 4; ++j) { a[k][2 * j] = bflo(xr[k][j]) + (j < 2 ? c0 : c1)[(2 * j) & 3]; a[k][2 * j + 1] = bfhi(xr[k][j]) + (j < 2 ? c0 : c1)[(2 * j + 1) & 3]; PIN(a[k][2 * j]); PIN(a[k][2 * j + 1]); }
    }
    unsigned sel0 = 0x00003f80u, sel1 = 0x3f800000u, pb = P_OFF + ((rt * 3 + cg) * 16 + w) * 12288 + 3 * kg * 1024;
    PIN(sel0); PIN(sel1); PINS(pb);
    WAITV(0); BAR();
    bstore8_sc1(tag, vflag, srdws, flo + (FL_FIN + fs) * 128);
    spin_until(fp, tag);
    const __amdgpu_buffer_rsrc_t rout = __builtin_amdgcn_make_buffer_rsrc(out, 0, S_ * D_ * 2, 0x00020000);
    u32x4 v[3][4];
#pragma unroll
    for (int k = 0; k < 3; ++k)
#pragma unroll
      for (int s = 0; s < 4; ++s) v[k][s] = __builtin_amdgcn_raw_buffer_load_b128(rws, l16 + k * 1024, pb + s * 4 * 12288, 16);
#pragma unroll
    for (int k = 0; k < 3; ++k) {
#pragma unroll
      for (int s = 0; s < 4; ++s)
#pragma unroll
        for (int j = 0; j < 4; ++j) { a[k][2 * j] = dot2(v[k][s][j], sel0, a[k][2 * j]); a[k][2 * j + 1] = dot2(v[k][s][j], sel1, a[k][2 * j + 1]); }
      __builtin_amdgcn_raw_buffer_store_b128(u32x4{cvtpk(a[k][0], a[k][1]), cvtpk(a[k][2], a[k][3]), cvtpk(a[k][4], a[k][5]), cvtpk(a[k][6], a[k][7])}, rout, ov, os[k], 0);
    }
    if (fs == 0) bstore8_sc1(tag, vflag, srdws, flo + FL_EPOCH * 128);
    asm volatile(".Lk_end0:");
    __builtin_amdgcn_endpgm();
  }
  {
    using SC = Sched<true>;
    LANE_VALUES
    auto va_of = [&](unsigned word) { return r8 * 3456 + (swz & 1) * 16 + ((word >> (8 * (swz >> 1))) & 255) * 16; };
    constexpr bool attn_role = true;
    auto rd_a = [&](auto Sc, auto U) {
      constexpr int s = Sc, u = U;
      if constexpr (s < SC::NSTEP && !SC::skip(s) && !(u == 1 && s >= 864 && SC::a_ks(s) >= 8)) {
        constexpr int sl = SC::a_stage(s) % 6, ks = SC::a_ks(s);
        (u ? wu : wl)[s % NB] = rdhalf(rb[2 * SC::a_tile(s) + (ks & 1)][sl >> 1] + (sl & 1) * SLOT + (ks >> 1) * 4096 + u * 2048);
      }
    };
    auto mma1 = [&](auto Sc, auto H) {
      constexpr int s = Sc, k = s % 36, o = k & 1, tb = o ^ H;
      if constexpr (!SC::skip(s)) mfma_vv<k < 2>(acc[SC::a_tile(s)][o][tb], cat(wl[s % NB], wu[s % NB]), xf[(k & ~1) + tb]);
    };
    auto sync = [&](auto Sc, auto H) {
      constexpr int s = Sc, h = H;
      if constexpr (h == 0 && SC::need(s) > -25) waitv<SC::allow(s)>();
      if constexpr (h == 1 && SC::bar(s)) BAR();
      if constexpr (h == 1 && SC::lwait(s)) __builtin_amdgcn_s_waitcnt(0xC07F | ((2 * (LA - (s < 468 && !attn_role ? WP1 : WP2))) << 8));
    };
    unsigned vA[3] = {va, va + 8 * 3456 - 1024, va + 2 * (8 * 3456 - 1024)}, gso = soA[1];
    set_m0<4 * SLOT>(m6);
    f32x4 gram;
    float rstdh, negmuh;
    const unsigned hrow = n16 < 8 ? n16 : 0;
    auto halo_val = [&](auto I) {
      f32x4 r;
      sfor<4>([&](auto K) { const float a = aread<A_HH + 4 * (I - 2) + K>(); r[(int)K] = fmaf(ror8(a), negmuh, a) * rstdh; });
      return r;
    };
    auto halo_col = [&](auto I) { return ((qd[I] >> (8 * w)) & 255); };
    LAS char* hb[3];
#pragma unroll
    for (int i = 0; i < 3; ++i) { hb[i] = (LAS char*)(uintptr_t)((unsigned)(uintptr_t)rb[0][i] ^ (w << 5)); PIN(hb[i]); }
    const unsigned ha = (unsigned)(uintptr_t)lds + TAB_OFF + hrow * 128 + G * 8;
    const unsigned qrow = (unsigned)(uintptr_t)lds + QK_OFF + (32 * w + n16 + 8) * QK_RS + G * 8, vrow = (unsigned)(uintptr_t)lds + (32 * w + n16 + 8) * V_RS + G * 16;
    bf16x4 hl[2], hu[2];
    unsigned qad;
    auto opa = [&](auto Tc, auto K) {
      constexpr int T = Tc, d = K / 4, j = K % 4, p = 2 * T + (d >> 2), tb = (d >> 1) & 1, hf = d & 1;
      if constexpr (p != 7) {
        const f32x4& a = acc[T & 1][d >> 2][tb];
        if constexpr (j == 0) t0 = i_mul(a[2 * hf], rstd[tb]);
        if constexpr (j == 1) t1 = i_mul(a[2 * hf + 1], rstd[tb]);
        if constexpr (p < 19) {
          if constexpr (j == 2) t0 = __uint_as_float(i_cvt(t0, t1));
          if constexpr (j == 3) awrite<A_Q + 4 * p + 2 * tb + hf>(__float_as_uint(t0));
        } else if constexpr (j >= 2) awrite<A_V + 8 * (p - 19) + 4 * tb + 2 * hf + j - 2>(__float_as_uint(j == 2 ? t0 : t1));
      }
    };
    sfor<28>([&](auto So) { sfor<18>([&](auto Si) {
      constexpr int s = 18 * So + Si, i = s / 72, u = s % 72;
      sfor<2>([&](auto H) {
        constexpr int h = H;
        mma1(IC(s), H);
        SB(); rd_a(IC(s + LA), H); SB();
        sync(IC(s), H);
        if constexpr (SC::g_set(s)) {
          constexpr int np = SC::g_stage(s + 3), p = SC::g_piece(s + 3);
          if constexpr (h == 0) set_m0<(np % 6) * SLOT + p * 1024>(m6); else gso = sadd<8 * p * 3456>(soA[np % 3]);
        }
        if constexpr (h == 0 && SC::g_stage(s) >= 0) glds3<SC::g_piece(s) % 3>(vA[SC::g_piece(s) % 3], srdA, gso);
        if constexpr (SC::nop(s) > 0) { constexpr int k0 = SC::epi_from(s, h), k1 = SC::epi_to(s, h); sfor<k1 - k0>([&](auto I) { opa(IC(s / 36 - 1), IC(k0 + I)); }); }
        SB();
      });
      if constexpr (u % 4 == 2) {
        constexpr int sg = u / 4;
        if constexpr (s >= 46 && s <= 114) mfma16_aa<s == 46, A_HX + s - 46>(gram);
        if constexpr (i >= 2) mfma16_ava<sg == 0, A_HH + 4 * (i - 2), A_HX + 4 * sg>(cat(hl[sg & 1], hu[sg & 1]));
        SB();
        if constexpr (s >= 22 && s < 94) { constexpr int f = (s - 22) / 4; ds_read_a64<A_HX + 4 * f, (f >> 1) * 1024 + (f & 1) * 64>(ha); ds_read_a64<A_HX + 4 * f + 2, (f >> 1) * 1024 + (f & 1) * 64 + 32>(ha); }
        constexpr int s2 = s + 8, sl = (3 * (s2 / 72) + (s2 % 72) / 24) % 6;
        if constexpr (s2 >= 144 && s2 < 504) { LAS char* p = hb[sl >> 1] + (sl & 1) * SLOT + (((s2 % 72) / 4) % 6) * 4096; hl[sg & 1] = rdhalf(p); hu[sg & 1] = rdhalf(p + 2048); }
        if constexpr (s == 122) {
          const float dg = (p4 == 0) ? gram[0] : (p4 == 1) ? gram[1] : (p4 == 2) ? gram[2] : gram[3];
          rstdh = G == q4 ? dg : 0.f; negmuh = G == 2 ? gram[0] : 0.f;
          PIN(rstdh); PIN(negmuh);
        }
        if constexpr (s == 126) { rstdh = xor16(rstdh); negmuh = xor16(negmuh); PIN(rstdh); PIN(negmuh); }
        if constexpr (s == 130) { rstdh = xor32(rstdh); negmuh = xor32(negmuh); PIN(rstdh); PIN(negmuh); }
        if constexpr (s == 134) {
          negmuh *= -1.0f / D_;
          rstdh = __builtin_amdgcn_rsqf(fmaxf(rstdh * (1.0f / D_) - negmuh * negmuh, 0.f) + 1e-5f);
          PIN(rstdh); PIN(negmuh);
        }
      }
      constexpr int k = SC::warm_k(s);
      if constexpr (SC::mark_k(s) >= 0) mark<1, SC::mark_k(s)>();
      if constexpr (k >= 0 && k < 4) {
        const unsigned la = (288 * kg + ((qs[k < 3 ? 2 + k : 5] >> (k == 2 ? 16 : 0)) & 255) * 16) & ~127u, lb = (288 * kg + (k < 3 ? qs[2 + k] >> 24 : qs[6] & 255) * 16) & ~127u;
        warm(((wa >> 1) * 3456 + ((wa & 1) ? lb : la)) | wm, srdA, 0);
      }
      if constexpr (k == 4) warm(((rt * 81 + w * 21 + lane) * 128) | (lane < 21 ? 0 : OOB), srdWo, 144 * kg * 1152);
      if constexpr (s == 2) *(LAS u32x4*)(lds + TAB_OFF + w * 3072 + (r8 < 3 ? r8 : 0) * 1024 + pos * 16) = u32x4{0x3f803f80u, 0x3f803f80u, 0x3f803f80u, 0x3f803f80u};
      if constexpr (u == 47 && i < 5) { vA[0] = va_of(qs[i + 2]); vA[1] = vA[0] + 8 * 3456 - 1024; vA[2] = vA[0] + 2 * (8 * 3456 - 1024); PIN(vA[0]); PIN(vA[1]); PIN(vA[2]); }
      if constexpr (s > 432 && s < 490 && (s - 433) / 3 != 7) {
        constexpr int p = (s - 433) / 3, j = (s - 433) % 3;
        if constexpr (j == 0) { qad = qrow + ((qd[p / 4] >> (8 * (p % 4))) & 255) * 16; PIN(qad); }
        else ds_write_a64<A_Q + 4 * p + 2 * (j - 1), (j - 1) * 16 * QK_RS>(qad);
      }
      if constexpr (s == 491 || s == 495 || s == 499) {
        constexpr int hi = 2 + (s - 491) / 4;
        const f32x4 r = halo_val(IC(hi));
        const unsigned row = (hi == 4 && w == 3) ? 0 : hrow;
        *(LAS u32x2*)(lds + QK_OFF + row * QK_RS + halo_col(IC(hi)) * 16 + G * 8) = u32x2{cvtpk(r[0], r[1]), cvtpk(r[2], r[3])};
      }
      SB();
    }); });
    asm volatile("s_nop 7" : "+v"(acc[1][0][0]), "+v"(acc[1][0][1]), "+v"(acc[1][1][0]), "+v"(acc[1][1][1]));
    WAITL0(); BAR();
    {
      const int hd = lane & 7, i0 = 32 * w + 4 * r8;
      LAS char* rp[11];
#pragma unroll
      for (int jj = 0; jj < 11; ++jj) { rp[jj] = lds + QK_OFF + (i0 + 1 + jj) * QK_RS + hd * 36; PIN(rp[jj]); }
      unsigned qv[4][9], kv[11][9];
#pragma unroll
      for (int a = 0; a < 4; ++a)
#pragma unroll
        for (int c = 0; c < 9; ++c) qv[a][c] = *(LAS unsigned*)(rp[7 + a] + c * 4);
#pragma unroll
      for (int jj = 0; jj < 4; ++jj)
#pragma unroll
        for (int c = 0; c < 9; ++c) kv[jj][c] = *(LAS unsigned*)(rp[jj] + 288 + c * 4);
      {
#pragma unroll
        for (int k = 0; k < 4; ++k)
          *(LAS f32x4*)(uintptr_t)(vrow + ((qd[6] >> (16 + 8 * (k >> 1))) & 255) * 32 + (k & 1) * 16 * V_RS) = acc[1][k >> 1][k & 1] * rstd[k & 1];
        sfor<3>([&](auto I) {
          constexpr int hi = I + 4;
          const unsigned row = (hi == 4 && w != 3) ? 0 : hrow, col = (hi == 4 && w != 3) ? 0 : halo_col(IC(hi));
          *(LAS f32x4*)(lds + row * V_RS + col * 32 + G * 16) = halo_val(IC(hi));
        });
      }
      SB();
      float lg[4][8];
      sfor<8>([&](auto U) {
        constexpr int u = U;
        if constexpr (u + 4 < 11) {
#pragma unroll
          for (int c = 0; c < 9; ++c) kv[u + 4][c] = *(LAS unsigned*)(rp[u + 4] + 288 + c * 4);
        }
        if constexpr (u < 7) {
          constexpr int p = 19 + u;
          const unsigned ad = vrow + ((qd[p / 4] >> (8 * (p % 4))) & 255) * 32;
          ds_write_a128<A_V + 8 * u, 0>(ad); ds_write_a128<A_V + 8 * u + 4, 16 * V_RS>(ad);
        }
#pragma unroll
        for (int a = 0; a < 4; ++a) lg[a][u] = 0.f;
#pragma unroll
        for (int c = 0; c < 9; ++c)
#pragma unroll
          for (int a = 0; a < 4; ++a) lg[a][u] = dot2(qv[a][c], kv[a + u][c], lg[a][u]);
        SB();
      });
      WAITL0(); BAR();
      mark<1, 7>();
      LAS char* vp = lds + (i0 + 1) * V_RS + hd * 72;
      f32x2 vv[11][9];
#pragma unroll
      for (int jj = 0; jj < 3; ++jj)
#pragma unroll
        for (int c = 0; c < 9; ++c) vv[jj][c] = *(volatile LAS f32x2*)(vp + jj * V_RS + c * 8);
      if (rt < 2) {
#pragma unroll
        for (int a = 0; a < 4; ++a)
#pragma unroll
          for (int u = 0; u < 7 - a; ++u) lg[a][u] = (i0 + a + u < 7) ? -1e9f : lg[a][u];
      }
      constexpr float CS = 0.23570226039551584f * 1.4426950408889634f;
#pragma unroll
      for (int a = 0; a < 4; ++a) {
        float mx = lg[a][0];
#pragma unroll
        for (int u = 1; u < 8; ++u) mx = fmaxf(mx, lg[a][u]);
        const float nb = -mx * CS;
        float sum = 0.f;
#pragma unroll
        for (int u = 0; u < 8; ++u) { lg[a][u] = __builtin_amdgcn_exp2f(fmaf(lg[a][u], CS, nb)); sum += lg[a][u]; }
        const float inv = __builtin_amdgcn_rcpf(sum);
#pragma unroll
        for (int u = 0; u < 8; ++u) lg[a][u] *= inv;
      }
      SB();
      f32x2 o[4][9];
      sfor<11>([&](auto J) {
        constexpr int jj = J;
        if constexpr (jj + 3 < 11) {
#pragma unroll
          for (int c = 0; c < 9; ++c) vv[jj + 3][c] = *(volatile LAS f32x2*)(vp + (jj + 3) * V_RS + c * 8);
        }
#pragma unroll
        for (int c = 0; c < 9; ++c) {
#pragma unroll
          for (int a = 0; a < 4; ++a) {
            if (jj == a) o[a][c] = vv[jj][c] * lg[a][0];
            else if (jj > a && jj <= a + 7) o[a][c] = __builtin_elementwise_fma(vv[jj][c], f32x2{lg[a][jj - a], lg[a][jj - a]}, o[a][c]);
          }
        }
        if constexpr (jj == 7) mark<1, 8>();
        if constexpr (jj >= 7) {
#pragma unroll
          for (int c = 0; c < 9; ++c) *(LAS unsigned*)(rp[jj] + c * 4) = cvtpk(o[jj - 7][c][0], o[jj - 7][c][1]);
        }
        SB();
      });
    }
    {
      LAS char* rowp = (LAS char*)(uintptr_t)qrow;
#pragma unroll
      for (int f = 0; f < 9; ++f) {
        const u32x2 a = *(LAS u32x2*)(rowp + (f >> 1) * 64 + (f & 1) * 16 * QK_RS), b = *(LAS u32x2*)(rowp + (f < 8 ? (f >> 1) * 64 + (f & 1) * 16 * QK_RS + 32 : 256 + 16 * QK_RS));
        __builtin_amdgcn_raw_buffer_store_b128(u32x4{a[0], a[1], b[0], b[1]}, rws, l16, oso + f * 1024, 16);
      }
    }
    WAITV(0); BAR();
    bstore8_sc1(get_tag(), vflag, srdws, flo + (FL_O + kg) * 128);
  }
  asm volatile(".Lk_end1:");
}

extern "C" void kernel_launch(void* const* d_in, const int* in_sizes, int n_in, void* d_out, int out_size, void* d_ws, size_t ws_size, hipStream_t stream) {
  (void)hipFuncSetAttribute((const void*)main_k, hipFuncAttributeMaxDynamicSharedMemorySize, LDS_BYTES);
  const Args args = {d_in[0], d_in[1], d_in[2], d_in[4], d_in[5], d_in[6], d_in[7], d_out, d_ws, make_tab()};
  main_k<<<256, 256, LDS_BYTES, stream>>>(args);
}
```
